```python
import torch

def _softmax(x, axis=-1):
    return torch.softmax(x, dim=axis)

def _gelu(x):
    return torch.nn.functional.gelu(x, approximate='tanh')

def _silu(x):
    return torch.nn.functional.silu(x)

def _sigmoid(x):
    return torch.sigmoid(x)

def _layernorm(x, axis=-1, eps=1e-05):
    m = x.mean(dim=axis, keepdim=True)
    v = x.var(dim=axis, keepdim=True, unbiased=False)
    return (x - m) / torch.sqrt(v + eps)

def _scatter_add(tgt, idx, src, axis=0):
    return tgt.clone().index_add_(axis, idx, src)

def _segment_sum(src, seg, n):
    out = torch.zeros((n,) + src.shape[1:], dtype=src.dtype)
    return out.index_add_(0, seg, src)

def _pad(x, pw):
    flat = tuple((v for pair in reversed(pw) for v in pair))
    return torch.nn.functional.pad(x, flat)
kernel_name = 'adaLN_cond__ssm_conv_h3__mixer_channel_mlp__prompt'

def setup_inputs():

    _w = lambda a: (lambda t: t.to(torch.bfloat16) if t.is_floating_point() else t)(torch.as_tensor(a))

    def _A_setup():
        B, S, D = (1, 2048, 640)
        ks = 4
        return {'q': _w((lambda *s: torch.randn(*s, dtype=torch.bfloat16) * 0.05)(B, S, D)), 'k': _w((lambda *s: torch.randn(*s, dtype=torch.bfloat16) * 0.8)(B, S, D)), 'v': _w((lambda *s: torch.randn(*s, dtype=torch.bfloat16) * 0.8)(B, S, D)), 'shift_k': _w((lambda *s: torch.randn(*s, dtype=torch.bfloat16) * 0.8)(D, ks) * 0.5), 'ssm_k': _w((lambda *s: torch.randn(*s, dtype=torch.bfloat16) * 0.8)(D, S) * (1.0 / S ** 0.5)), 'ssm_D': _w((lambda *s: torch.randn(*s, dtype=torch.bfloat16) * 0.8)(D))}
    _A_in = _A_setup()

    def _B_setup():
        B, S, D, F = (1, 2048, 640, 1728)
        return {'x': _w((lambda *s: torch.randn(*s, dtype=torch.bfloat16) * 0.05)(B, S, D)), 'w1': _w((lambda *s: torch.randn(*s, dtype=torch.bfloat16) * 3.2)(D, F) * D ** (-0.5)), 'b1': _w((lambda *s: torch.randn(*s, dtype=torch.bfloat16) * 1.6)(F)), 'w2': _w((lambda *s: torch.randn(*s, dtype=torch.bfloat16) * 1.6)(F, D) * F ** (-0.5)), 'b2': _w((lambda *s: torch.randn(*s, dtype=torch.bfloat16) * 3.2)(D))}
    _B_in = _B_setup()
    return {'A_q': _A_in['q'], 'A_k': _A_in['k'], 'A_v': _A_in['v'], 'A_shift_k': _A_in['shift_k'], 'A_ssm_k': _A_in['ssm_k'], 'A_ssm_D': _A_in['ssm_D'], 'B_x': _B_in['x'], 'B_w1': _B_in['w1'], 'B_b1': _B_in['b1'], 'B_w2': _B_in['w2'], 'B_b2': _B_in['b2'], 'c': _w((lambda *s: torch.randn(*s, dtype=torch.bfloat16) * 0.1)(1, 640) * 8.0), 'w_mod': _w((lambda *s: torch.randn(*s, dtype=torch.bfloat16) * 0.1)(640, 6 * 640) * 640 ** (-0.5) * 8.0)}

def reference(A_q, A_k, A_v, A_shift_k, A_ssm_k, A_ssm_D, B_x, B_w1, B_b1, B_w2, B_b2, c, w_mod):

    def _A_ref(q, k, v, shift_k, ssm_k, ssm_D):
        B, S, D = q.shape
        ks = shift_k.shape[-1]
        k_pad = _pad(k, ((0, 0), (ks - 1, 0), (0, 0)))
        k_shift = torch.zeros_like(k)
        for t in range(ks):
            k_shift = k_shift + k_pad[:, t:t + S, :] * shift_k[:, ks - 1 - t][None, None, :]
        kv = k_shift * v
        L = 2 * S
        kv_pad = _pad(kv, ((0, 0), (0, S), (0, 0)))
        kf_pad = _pad(ssm_k, ((0, 0), (0, S)))
        kv_f = (lambda x, axes=(-2, -1): torch.fft.rfft2(x.float(), dim=axes))(kv_pad[:, None, :, :], axes=(1, 2))
        kf_f = (lambda x, axes=(-2, -1): torch.fft.rfft2(x.float(), dim=axes))(kf_pad[:, None, :], axes=(1, 2))
        kf_f = (lambda x, *d: x.permute(*d))(kf_f, 1, 2, 0)
        y_f = kv_f * kf_f[None, :, :, :]
        y = (lambda x, s, axes=(-2, -1): torch.fft.irfft2(x, s=s, dim=axes).to(torch.bfloat16))(y_f, (1, L), axes=(1, 2))[:, 0, :S, :]
        y = y + kv * ssm_D[None, None, :]
        return q * y

    def _B_ref(x, w1, b1, w2, b2):
        y = _layernorm(x, axis=-1)
        h = _gelu(torch.einsum('bsd,df->bsf', y, w1) + b1)
        return torch.einsum('bsf,fd->bsd', h, w2) + b2
    x = A_q
    mod = (_silu(c) @ w_mod).reshape(c.shape[0], 6, 1, -1)
    sa, ca, ga, sb, cb, gb = (mod[:, 0].clone(), mod[:, 1].clone(), mod[:, 2].clone(), mod[:, 3].clone(), mod[:, 4].clone(), mod[:, 5].clone())
    h = _layernorm(x, axis=-1) * (1 + ca) + sa
    x = x + ga * _A_ref(h, A_k, A_v, A_shift_k, A_ssm_k, A_ssm_D)
    h = _layernorm(x, axis=-1) * (1 + cb) + sb
    x = x + gb * _B_ref(h, B_w1, B_b1, B_w2, B_b2)
    return x
```

```cpp
#include <hip/hip_runtime.h>
#include <hip/hip_bf16.h>
typedef unsigned short u16;
constexpr int S=2048, D=640, F=1728;
__device__ __forceinline__ float b2f(u16 x){ return __uint_as_float(((unsigned)x)<<16); }
__device__ __forceinline__ u16 f2b(float f){ __hip_bfloat16 h = __float2bfloat16(f); return *(u16*)&h; }
__device__ __forceinline__ float rb(float f){ return b2f(f2b(f)); }

__global__ void k_mod(const u16* c, const u16* w, float* mod){
  int j = blockIdx.x*blockDim.x+threadIdx.x; if(j>=6*D) return;
  float acc=0;
  for(int i=0;i<D;i++){ float x=b2f(c[i]); float s=rb(x/(1.f+__expf(-x))); acc+=s*b2f(w[(size_t)i*6*D+j]); }
  mod[j]=rb(acc);
}
__global__ void k_kv(const u16* k,const u16* v,const u16* sk,u16* kv){
  int i=blockIdx.x*blockDim.x+threadIdx.x; if(i>=S*D) return; int t=i/D,d=i%D;
  float a=0;
  for(int j=0;j<4;j++){ int tt=t-3+j; float kk = tt>=0? b2f(k[tt*D+d]):0.f; a=rb(a+rb(kk*b2f(sk[d*4+3-j]))); }
  kv[i]=f2b(a*b2f(v[i]));
}
__global__ void k_conv(const u16* kv,const u16* sk,const u16* sD,u16* y){
  int i=blockIdx.x*blockDim.x+threadIdx.x; if(i>=S*D) return; int t=i/D,d=i%D;
  float a=0;
  for(int s=0;s<=t;s++) a+=b2f(kv[s*D+d])*b2f(sk[(size_t)d*S+(t-s)]);
  y[i]=f2b(rb(a)+rb(b2f(kv[i])*b2f(sD[d])));
}
__device__ float block_sum(float v,float* sm){
  for(int o=32;o>0;o>>=1) v+=__shfl_xor(v,o);
  __syncthreads(); if((threadIdx.x&63)==0) sm[threadIdx.x>>6]=v; __syncthreads();
  float r=0; for(int i=0;i<(int)(blockDim.x>>6);i++) r+=sm[i]; return r;
}
__device__ void ln_row(float* x,float* sm){
  float m=block_sum(*x,sm)/D; float dv=*x-m; float var=block_sum(dv*dv,sm)/D;
  float mb=rb(m), vb=rb(var); float s=rb(sqrtf(rb(vb+1e-5f)));
  *x=rb(rb(*x-mb)/s);
}
__global__ void k_mid(const u16* q,const u16* y,const float* mod,u16* x1o,u16* y3o){
  __shared__ float sm[16];
  int t=blockIdx.x,d=threadIdx.x;
  float sa=mod[d],ca=mod[D+d],ga=mod[2*D+d],sb=mod[3*D+d],cb=mod[4*D+d];
  float x=b2f(q[t*D+d]); float n=x; ln_row(&n,sm);
  float h=rb(rb(n*rb(1.f+ca))+sa);
  float a=rb(h*b2f(y[t*D+d]));
  float x1=rb(x+rb(ga*a));
  x1o[t*D+d]=f2b(x1);
  n=x1; ln_row(&n,sm);
  h=rb(rb(n*rb(1.f+cb))+sb);
  ln_row(&h,sm);
  y3o[t*D+d]=f2b(h);
}
__global__ void k_g1(const u16* y3,const u16* w1,const u16* b1,u16* H){
  int i=blockIdx.x*blockDim.x+threadIdx.x; if(i>=S*F) return; int t=i/F,f=i%F;
  float a=0; for(int d=0;d<D;d++) a+=b2f(y3[t*D+d])*b2f(w1[d*F+f]);
  float x=rb(rb(a)+b2f(b1[f]));
  float u=0.7978845608028654f*(x+0.044715f*x*x*x);
  float g=0.5f*x*(1.f+tanhf(u));
  H[i]=f2b(g);
}
__global__ void k_g2(const u16* H,const u16* w2,const u16* b2,const u16* x1,const float* mod,u16* out){
  int i=blockIdx.x*blockDim.x+threadIdx.x; if(i>=S*D) return; int t=i/D,d=i%D;
  float a=0; for(int f=0;f<F;f++) a+=b2f(H[t*F+f])*b2f(w2[f*D+d]);
  float x=rb(rb(a)+b2f(b2[d]));
  out[i]=f2b(b2f(x1[i])+rb(mod[5*D+d]*x));
}
extern "C" void kernel_launch(void* const* d_in, const int* in_sizes, int n_in,
                              void* d_out, int out_size, void* d_ws, size_t ws_size,
                              hipStream_t stream) {
  const u16* q=(const u16*)d_in[0],*k=(const u16*)d_in[1],*v=(const u16*)d_in[2],*shk=(const u16*)d_in[3],*ssk=(const u16*)d_in[4],*sD=(const u16*)d_in[5];
  const u16* w1=(const u16*)d_in[7],*b1=(const u16*)d_in[8],*w2=(const u16*)d_in[9],*b2=(const u16*)d_in[10],*c=(const u16*)d_in[11],*wm=(const u16*)d_in[12];
  char* ws=(char*)d_ws;
  float* mod=(float*)ws; u16* kv=(u16*)(ws+65536); u16* y=kv+S*D; u16* x1=y+S*D; u16* y3=x1+S*D; u16* H=y3+S*D;
  k_mod<<<(6*D+63)/64,64,0,stream>>>(c,wm,mod);
  k_kv<<<S*D/256,256,0,stream>>>(k,v,shk,kv);
  k_conv<<<S*D/256,256,0,stream>>>(kv,ssk,sD,y);
  k_mid<<<S,D,0,stream>>>(q,y,mod,x1,y3);
  k_g1<<<S*F/256,256,0,stream>>>(y3,w1,b1,H);
  k_g2<<<S*D/256,256,0,stream>>>(H,w2,b2,x1,mod,(u16*)d_out);
}
```

```cpp
#include <hip/hip_runtime.h>
#include <stdint.h>

typedef unsigned short u16;
typedef unsigned int u32;
typedef short s16x4 __attribute__((ext_vector_type(4)));
typedef short s16x8 __attribute__((ext_vector_type(8)));
typedef __bf16 bf16x8 __attribute__((ext_vector_type(8)));
typedef __bf16 bf16x2 __attribute__((ext_vector_type(2)));
typedef float f32x4 __attribute__((ext_vector_type(4)));
typedef float f32x2 __attribute__((ext_vector_type(2)));
typedef u32 u32x4 __attribute__((ext_vector_type(4)));
typedef u32 u32x2 __attribute__((ext_vector_type(2)));
#define LAS __attribute__((address_space(3)))
#define DI __device__ __forceinline__

constexpr int S = 2048, D = 640, F = 1728;
constexpr size_t WS_MODP = 0;
constexpr size_t WS_U    = 65536;
constexpr size_t WS_Y2T  = WS_U + 2621440;
constexpr size_t WS_X1   = WS_Y2T + 2621440;
constexpr size_t WS_Y3F  = WS_X1 + 2621440;
constexpr size_t WS_HF   = WS_Y3F + 2621440;
constexpr size_t WS_W1G  = WS_HF + 7077888;
constexpr size_t WS_W2F  = WS_W1G + 2293760;

#define STAMP_BEGIN(ws)
#define STAMP_END(ws, kid)
#define MARK(ws, kid, i)
DI float lo16(u32 x) { return __uint_as_float(x << 16); }
DI float hi16(u32 x) { return __uint_as_float(x & 0xffff0000u); }
DI u32 pk2(float a, float b) { f32x2 v = {a, b}; return __builtin_bit_cast(u32, __builtin_convertvector(v, bf16x2)); }
DI float rbf(float a) { return lo16(pk2(a, a) & 0xffffu); }
DI int rfl(int x) { return __builtin_amdgcn_readfirstlane(x); }
DI s16x4 trread(u32 ldsaddr) { return __builtin_amdgcn_ds_read_tr16_b64_v4i16((LAS s16x4*)(size_t)ldsaddr); }
DI bf16x8 cat8(s16x4 a, s16x4 b) { return __builtin_bit_cast(bf16x8, __builtin_shufflevector(a, b, 0, 1, 2, 3, 4, 5, 6, 7)); }
DI f32x4 mfma16(bf16x8 a, bf16x8 b, f32x4 c) { return __builtin_amdgcn_mfma_f32_16x16x32_bf16(a, b, c, 0, 0, 0); }
DI u32 ldsaddr(const void* p) { return (u32)(size_t)(LAS const char*)p; }
DI __amdgpu_buffer_rsrc_t mksrd(const void* p, u32 bytes) { return __builtin_amdgcn_make_buffer_rsrc((void*)p, 0, bytes, 0x00020000); }

constexpr int K1_KV_BLOCKS = 320;
constexpr int K1_RT_BLOCKS = 270;
__global__ __launch_bounds__(256) void k_prep(const u16* __restrict__ k, const u16* __restrict__ v, const u16* __restrict__ shk,
                                              const u16* __restrict__ w1, const u16* __restrict__ w2, char* __restrict__ ws) {
  STAMP_BEGIN(ws)
  __shared__ __attribute__((aligned(16))) char sm[4 * 32 * 144];
  const int tid = threadIdx.x, lane = tid & 63, w = rfl(tid >> 6);
  const int b = blockIdx.x;
  if (b < K1_KV_BLOCKS) {
    const int cg = b % 10, tt = b / 10;
    const int cq = tid & 15, m = tid >> 4;
    const int t0 = 64 * tt + 4 * m, ch0 = 64 * cg + 4 * cq;
    u32x2 kr[7], vr[4];
#pragma unroll
    for (int i = 0; i < 7; i++) {
      int t = t0 - 3 + i;
      u32x2 x = *(const u32x2*)(k + (size_t)(t < 0 ? 0 : t) * D + ch0);
      if (t < 0) x = (u32x2){0u, 0u};
      kr[i] = x;
    }
#pragma unroll
    for (int i = 0; i < 4; i++) vr[i] = *(const u32x2*)(v + (size_t)(t0 + i) * D + ch0);
    const u32x4 s0 = *(const u32x4*)(shk + ch0 * 4), s1 = *(const u32x4*)(shk + ch0 * 4 + 8);
    float sk[4][4];
    sk[0][0] = lo16(s0[0]); sk[0][1] = hi16(s0[0]); sk[0][2] = lo16(s0[1]); sk[0][3] = hi16(s0[1]);
    sk[1][0] = lo16(s0[2]); sk[1][1] = hi16(s0[2]); sk[1][2] = lo16(s0[3]); sk[1][3] = hi16(s0[3]);
    sk[2][0] = lo16(s1[0]); sk[2][1] = hi16(s1[0]); sk[2][2] = lo16(s1[1]); sk[2][3] = hi16(s1[1]);
    sk[3][0] = lo16(s1[2]); sk[3][1] = hi16(s1[2]); sk[3][2] = lo16(s1[3]); sk[3][3] = hi16(s1[3]);
    float kf[7][4];
#pragma unroll
    for (int i = 0; i < 7; i++) { kf[i][0] = lo16(kr[i][0]); kf[i][1] = hi16(kr[i][0]); kf[i][2] = lo16(kr[i][1]); kf[i][3] = hi16(kr[i][1]); }
#pragma unroll
    for (int r = 0; r < 4; r++) {
      float o[4];
#pragma unroll
      for (int e = 0; e < 4; e++) {
        float a = kf[r][e] * sk[e][3];
        a = fmaf(kf[r + 1][e], sk[e][2], a);
        a = fmaf(kf[r + 2][e], sk[e][1], a);
        a = fmaf(kf[r + 3][e], sk[e][0], a);
        float vv = (e & 1) ? hi16(vr[r][e >> 1]) : lo16(vr[r][e >> 1]);
        o[e] = a * vv;
      }
      *(u32x2*)(sm + (4 * m + r) * 144 + 8 * cq) = (u32x2){pk2(o[0], o[1]), pk2(o[2], o[3])};
    }
    __syncthreads();
    const int grp = lane >> 4, i16 = lane & 15, jj = i16 >> 2, p = i16 & 3;
    const int cgrp = 2 * (w & 1) + (grp & 1), rho = 2 * (w >> 1) + (grp >> 1);
    const u32 base = ldsaddr(sm) + (4 * jj + rho) * 144 + (16 * cgrp + 4 * p) * 2;
    u16* U = (u16*)(ws + WS_U);
    u16* dst = U + ((size_t)(tt * D + 64 * cg + 16 * cgrp + i16) * 64 + rho * 16);
#pragma unroll
    for (int half = 0; half < 2; half++) {
      s16x4 a = trread(base + (8 * half) * 4 * 144), c = trread(base + (8 * half + 4) * 4 * 144);
      *(bf16x8*)(dst + 8 * half) = cat8(a, c);
    }
  } else {
    const int u = (b - K1_KV_BLOCKS) * 4 + w;
    char* my = sm + w * 32 * 144;
    const bool isw1 = u < 540;
    const int uu = isw1 ? u : u - 540;
    int ks, cc; const u16* src; int ld;
    if (isw1) { ks = uu / 27; cc = uu % 27; src = w1; ld = F; } else { ks = uu / 10; cc = uu % 10; src = w2; ld = D; }
    u32x4 r[4];
#pragma unroll
    for (int i = 0; i < 4; i++) r[i] = *(const u32x4*)(src + (size_t)(32 * ks + 8 * i + (lane >> 3)) * ld + 64 * cc + 8 * (lane & 7));
#pragma unroll
    for (int i = 0; i < 4; i++) *(u32x4*)(my + (8 * i + (lane >> 3)) * 144 + 16 * (lane & 7)) = r[i];
    const int q = lane >> 4, i16 = lane & 15, jj = i16 >> 2, p = i16 & 3;
    const u32 a0 = ldsaddr(my) + (isw1 ? (8 * q + jj) : (4 * q + jj)) * 144 + 8 * p;
    const u32 dt = isw1 ? 4 * 144 : 16 * 144;
#pragma unroll
    for (int f = 0; f < 4; f++) {
      s16x4 a = trread(a0 + 32 * f), c = trread(a0 + 32 * f + dt);
      const int ft = 4 * cc + f;
      size_t off;
      if (isw1) off = WS_W1G + ((size_t)((ft / 7) * 20 + ks) * 7 + (ft % 7)) * 1024;
      else      off = WS_W2F + ((size_t)((ft / 5) * 54 + ks) * 5 + (ft % 5)) * 1024;
      *(bf16x8*)(ws + off + lane * 16) = cat8(a, c);
    }
    if (u < 80) *(u32x4*)(ws + WS_W1G + ((size_t)(15 * 20 + (u >> 2)) * 7 + 3 + (u & 3)) * 1024 + lane * 16) = (u32x4){0u, 0u, 0u, 0u};
  }
  STAMP_END(ws, 0);
}

constexpr int CV_RS = 1184;
constexpr int CV_KB = 4128;
constexpr int CV_WB = CV_KB + 4 * CV_RS;
constexpr int K2_LDS = 84 * 1024;
__global__ __launch_bounds__(256, 1) void k_conv(const u16* __restrict__ ssk, const u16* __restrict__ ssD, const u16* __restrict__ c,
                                                 const u16* __restrict__ wmod, char* __restrict__ ws) {
  STAMP_BEGIN(ws)
  extern __shared__ __attribute__((aligned(16))) char smem[];
  const int tid = threadIdx.x, lane = tid & 63, w = rfl(tid >> 6);
  const int b = blockIdx.x;
  if (b < 160) {
    const int ch = 4 * b + w;
    char* kim = smem + w * CV_WB;
    char* uim = kim + CV_KB;
    const u16* U = (const u16*)(ws + WS_U);
    u32x4 ru[4], rk[4];
#pragma unroll
    for (int j = 0; j < 4; j++) ru[j] = *(const u32x4*)(U + ((size_t)((8 * j + (lane >> 3)) * D + ch) * 64 + 8 * (lane & 7)));
#pragma unroll
    for (int j = 0; j < 4; j++) rk[j] = *(const u32x4*)(ssk + (size_t)ch * S + 8 * (64 * j + lane));
    const float Dch = lo16(ssD[ch]);
    if (lane < 32) *(u32x4*)(uim + (lane >> 3) * CV_RS + 16 * (lane & 7)) = (u32x4){0u, 0u, 0u, 0u};
    if (lane < 2) *(u32x4*)(kim + 16 * lane) = (u32x4){0u, 0u, 0u, 0u};
#pragma unroll
    for (int j = 0; j < 4; j++) *(u32x4*)(kim + 32 + 16 * (64 * j + lane)) = rk[j];
#pragma unroll
    for (int j = 0; j < 4; j++) {
      const int pc = lane & 7, tt = 8 * j + (lane >> 3);
      *(u32x4*)(uim + (pc >> 1) * CV_RS + 128 + 32 * tt + 16 * (pc & 1)) = ru[j];
    }
    MARK(ws, 1, 0);
    const int q = lane >> 4, cc = lane & 15, jj = cc >> 2, p = cc & 3;
    const u32 va = ldsaddr(kim) + 224 - 64 * q - 8 * jj + 8 * p;
    const u32 vb = ldsaddr(uim) + 8 + 8 * cc + 16 * q;
    bf16x8 A[16];
#pragma unroll
    for (int s = 0; s < 16; s++) A[s] = cat8(trread(va + 256 * s + 32), trread(va + 256 * s));
    MARK(ws, 1, 1);
    f32x4 acc[4][8];
#pragma unroll
    for (int r = 0; r < 4; r++)
#pragma unroll
      for (int cb = 0; cb < 8; cb++) acc[r][cb] = (f32x4){0.f, 0.f, 0.f, 0.f};
#pragma unroll
    for (int r = 0; r < 4; r++) {
#pragma unroll
      for (int dl = 1; dl >= -14; dl--) {
        const u32 a = vb + r * CV_RS + 64 * (1 - dl);
        s16x4 b0 = *(LAS const s16x4*)(size_t)a, b1 = *(LAS const s16x4*)(size_t)(a + 8);
        bf16x8 B = cat8(b0, b1);
#pragma unroll
        for (int cb = 0; cb < 8; cb++) {
          const int s = dl + 2 * cb;
          if (s >= 0 && s <= 15) acc[r][cb] = mfma16(A[s], B, acc[r][cb]);
        }
      }
    }
    MARK(ws, 1, 2);
    const int idx = 4 * (lane == 0 ? 63 : (lane >= 16 ? lane - 16 : lane + 47));
    u16* Y = (u16*)(ws + WS_Y2T) + (size_t)ch * S + 16 * cc + 4 * q;
    const char* ub = uim + 2 * (64 + 4 * cc + q);
    float pc0 = 0.f, pc1 = 0.f, pc2 = 0.f;
#pragma unroll
    for (int cb = 0; cb < 8; cb++) {
      const f32x4 z0 = acc[0][cb], z1 = acc[1][cb], z2 = acc[2][cb], z3 = acc[3][cb];
      float o0 = z0[0], o1 = z0[1] + z1[0], o2 = z0[2] + z1[1] + z2[0], o3 = (z0[3] + z1[2]) + (z2[1] + z3[0]);
      const float c0 = z1[3] + z2[2] + z3[1], c1 = z2[3] + z3[2], c2 = z3[3];
      const float t0 = lane == 63 ? pc0 : c0, t1 = lane == 63 ? pc1 : c1, t2 = lane == 63 ? pc2 : c2;
      pc0 = c0; pc1 = c1; pc2 = c2;
      o0 += __int_as_float(__builtin_amdgcn_ds_bpermute(idx, __float_as_int(t0)));
      o1 += __int_as_float(__builtin_amdgcn_ds_bpermute(idx, __float_as_int(t1)));
      o2 += __int_as_float(__builtin_amdgcn_ds_bpermute(idx, __float_as_int(t2)));
      const float u0 = lo16(*(const u16*)(ub + 0 * CV_RS + 128 * cb)), u1 = lo16(*(const u16*)(ub + 1 * CV_RS + 128 * cb));
      const float u2 = lo16(*(const u16*)(ub + 2 * CV_RS + 128 * cb)), u3 = lo16(*(const u16*)(ub + 3 * CV_RS + 128 * cb));
      o0 = fmaf(u0, Dch, o0); o1 = fmaf(u1, Dch, o1); o2 = fmaf(u2, Dch, o2); o3 = fmaf(u3, Dch, o3);
      *(u32x2*)(Y + 256 * cb) = (u32x2){pk2(o0, o1), pk2(o2, o3)};
    }
    MARK(ws, 1, 3);
  } else if (b < 220) {
    float* sc = (float*)smem;
    float* red = sc + 640;
    const int col0 = 64 * (b - 160), cl = tid & 7, rg = tid >> 3;
    u32x4 r[20];
#pragma unroll
    for (int i = 0; i < 20; i++) r[i] = *(const u32x4*)(wmod + (size_t)(rg + 32 * i) * (6 * D) + col0 + 8 * cl);
    for (int i = tid; i < D; i += 256) { float x = lo16(c[i]); sc[i] = rbf(x / (1.f + __expf(-x))); }
    __syncthreads();
    float a[8] = {0.f, 0.f, 0.f, 0.f, 0.f, 0.f, 0.f, 0.f};
#pragma unroll
    for (int i = 0; i < 20; i++) {
      const float s = sc[rg + 32 * i];
#pragma unroll
      for (int e = 0; e < 4; e++) { a[2 * e] = fmaf(s, lo16(r[i][e]), a[2 * e]); a[2 * e + 1] = fmaf(s, hi16(r[i][e]), a[2 * e + 1]); }
    }
#pragma unroll
    for (int e = 0; e < 8; e++) { a[e] += __shfl_xor(a[e], 8); a[e] += __shfl_xor(a[e], 16); a[e] += __shfl_xor(a[e], 32); }
    if (lane < 8) {
#pragma unroll
      for (int e = 0; e < 8; e++) red[w * 64 + 8 * lane + e] = a[e];
    }
    __syncthreads();
    if (tid < 64) {
      float s = rbf((red[tid] + red[64 + tid]) + (red[128 + tid] + red[192 + tid]));
      const int j = col0 + tid, vv = j / D, ch = j - vv * D;
      if (vv == 1 || vv == 4) s = 1.f + s;
      const int slot = vv == 0 ? 1 : vv == 1 ? 0 : vv == 2 ? 2 : vv == 3 ? 4 : vv == 4 ? 3 : 5;
      ((u16*)(ws + WS_MODP))[(ch >> 2) * 24 + slot * 4 + (ch & 3)] = (u16)(pk2(s, s) & 0xffffu);
    }
  }
  STAMP_END(ws, 1);
}

DI float xsum8_16_32(float x) {
  x += __int_as_float(__builtin_amdgcn_update_dpp(0, __float_as_int(x), 0x128, 0xf, 0xf, false));
  { auto r = __builtin_amdgcn_permlane16_swap(__float_as_uint(x), __float_as_uint(x), false, false); x = __uint_as_float(r[0]) + __uint_as_float(r[1]); }
  { auto r = __builtin_amdgcn_permlane32_swap(__float_as_uint(x), __float_as_uint(x), false, false); x = __uint_as_float(r[0]) + __uint_as_float(r[1]); }
  return x;
}
DI void ln_stats(float s1, float s2, f32x2* red, int w, int lane, float& mean, float& rstd) {
  s1 = xsum8_16_32(s1); s2 = xsum8_16_32(s2);
  if (lane < 8) red[w * 8 + lane] = (f32x2){s1, s2};
  __syncthreads();
  const int tk = lane & 7;
  const f32x2 t = (red[tk] + red[8 + tk]) + (red[16 + tk] + red[24 + tk]);
  mean = t[0] * (1.f / D);
  const float var = fmaxf(t[1] * (1.f / D) - mean * mean, 0.f);
  rstd = __builtin_amdgcn_rsqf(var + 1e-5f);
}
__global__ __launch_bounds__(256) void k_mid(const u16* __restrict__ qin, char* __restrict__ ws) {
  STAMP_BEGIN(ws)
  __shared__ __attribute__((aligned(16))) char sm[640 * 16 + 3 * 32 * 8];
  const int tid = threadIdx.x, lane = tid & 63, w = rfl(tid >> 6);
  const int b = blockIdx.x, tg = 32 * (b & 7) + (b >> 3), t0 = 8 * tg;
  const int tk = lane & 7, slot = lane >> 3;
  const u16* Y = (const u16*)(ws + WS_Y2T);
  u32x4 yr[3];
#pragma unroll
  for (int i = 0; i < 3; i++) { int ch = tid + 256 * i; if (ch < D) yr[i] = *(const u32x4*)(Y + (size_t)ch * S + t0); }
  u32x2 xr[5]; u32x4 mp[5][3];
#pragma unroll
  for (int i = 0; i < 5; i++) {
    const int Q = 40 * w + 8 * i + slot;
    xr[i] = *(const u32x2*)(qin + (size_t)(t0 + tk) * D + 4 * Q);
#pragma unroll
    for (int j = 0; j < 3; j++) mp[i][j] = *(const u32x4*)(ws + WS_MODP + Q * 48 + 16 * j);
  }
#pragma unroll
  for (int i = 0; i < 3; i++) { int ch = tid + 256 * i; if (ch < D) *(u32x4*)(sm + ch * 16) = yr[i]; }
  MARK(ws, 2, 0);
  f32x2* red = (f32x2*)(sm + 640 * 16);
  float x[5][4];
  float s1 = 0.f, s2 = 0.f;
#pragma unroll
  for (int i = 0; i < 5; i++) {
    x[i][0] = lo16(xr[i][0]); x[i][1] = hi16(xr[i][0]); x[i][2] = lo16(xr[i][1]); x[i][3] = hi16(xr[i][1]);
#pragma unroll
    for (int e = 0; e < 4; e++) { s1 += x[i][e]; s2 = fmaf(x[i][e], x[i][e], s2); }
  }
  float mean, rstd;
  MARK(ws, 2, 1);
  ln_stats(s1, s2, red, w, lane, mean, rstd);
  MARK(ws, 2, 2);
  const int i16 = lane & 15, jj = i16 >> 2, p = i16 & 3, grp = lane >> 4;
  const u32 ya = ldsaddr(sm) + (4 * (40 * w + 2 * grp + (p >> 1)) + jj) * 16 + 8 * (p & 1);
  u16* X1 = (u16*)(ws + WS_X1);
  s1 = 0.f; s2 = 0.f;
#pragma unroll
  for (int i = 0; i < 5; i++) {
    const s16x4 yv = trread(ya + i * 8 * 64);
    const u32x2 yu = __builtin_bit_cast(u32x2, yv);
    const float y[4] = {lo16(yu[0]), hi16(yu[0]), lo16(yu[1]), hi16(yu[1])};
    const float w1[4] = {lo16(mp[i][0][0]), hi16(mp[i][0][0]), lo16(mp[i][0][1]), hi16(mp[i][0][1])};
    const float sa[4] = {lo16(mp[i][0][2]), hi16(mp[i][0][2]), lo16(mp[i][0][3]), hi16(mp[i][0][3])};
    const float ga[4] = {lo16(mp[i][1][0]), hi16(mp[i][1][0]), lo16(mp[i][1][1]), hi16(mp[i][1][1])};
    float o[4];
#pragma unroll
    for (int e = 0; e < 4; e++) {
      const float h = fmaf((x[i][e] - mean) * rstd, w1[e], sa[e]);
      o[e] = fmaf(h * y[e], ga[e], x[i][e]);
    }
    const u32x2 pk = {pk2(o[0], o[1]), pk2(o[2], o[3])};
    *(u32x2*)(X1 + (size_t)(t0 + tk) * D + 4 * (40 * w + 8 * i + slot)) = pk;
    x[i][0] = lo16(pk[0]); x[i][1] = hi16(pk[0]); x[i][2] = lo16(pk[1]); x[i][3] = hi16(pk[1]);
#pragma unroll
    for (int e = 0; e < 4; e++) { s1 += x[i][e]; s2 = fmaf(x[i][e], x[i][e], s2); }
  }
  MARK(ws, 2, 3);
  ln_stats(s1, s2, red + 32, w, lane, mean, rstd);
  MARK(ws, 2, 4);
  s1 = 0.f; s2 = 0.f;
#pragma unroll
  for (int i = 0; i < 5; i++) {
    const float w2[4] = {lo16(mp[i][1][2]), hi16(mp[i][1][2]), lo16(mp[i][1][3]), hi16(mp[i][1][3])};
    const float sb[4] = {lo16(mp[i][2][0]), hi16(mp[i][2][0]), lo16(mp[i][2][1]), hi16(mp[i][2][1])};
#pragma unroll
    for (int e = 0; e < 4; e++) {
      x[i][e] = fmaf((x[i][e] - mean) * rstd, w2[e], sb[e]);
      s1 += x[i][e]; s2 = fmaf(x[i][e], x[i][e], s2);
    }
  }
  ln_stats(s1, s2, red + 64, w, lane, mean, rstd);
  MARK(ws, 2, 5);
  const int rt = tg >> 1, r = 8 * (tg & 1) + tk;
  char* Y3 = ws + WS_Y3F + (size_t)(rt >> 1) * 40960 + (rt & 1) * 1024 + ((slot >> 1) * 16 + r) * 16 + 8 * (slot & 1);
#pragma unroll
  for (int i = 0; i < 5; i++) {
    const int ks = 5 * w + i;
    const float a0 = (x[i][0] - mean) * rstd, a1 = (x[i][1] - mean) * rstd, a2 = (x[i][2] - mean) * rstd, a3 = (x[i][3] - mean) * rstd;
    *(u32x2*)(Y3 + ks * 2048) = (u32x2){pk2(a0, a1), pk2(a2, a3)};
  }
  MARK(ws, 2, 6);
  STAMP_END(ws, 2);
}

template <int IMM>
DI void bload(u32x4& dst, u32 voff, __amdgpu_buffer_rsrc_t srd, u32 soff) {
  asm volatile("buffer_load_dwordx4 %0, %1, %2, %3 offen offset:%c4" : "=v"(dst) : "v"(voff), "s"(srd), "s"(soff), "i"(IMM));
}
DI void dma16(u32 m0v, u32 voff, __amdgpu_buffer_rsrc_t srd, u32 soff) {
  asm volatile("s_mov_b32 m0, %0\n\ts_nop 0\n\tbuffer_load_dwordx4 %1, %2, %3 offen lds" ::"s"(m0v), "v"(voff), "s"(srd), "s"(soff) : "m0");
}
#define WAITV(n) asm volatile("s_waitcnt vmcnt(" #n ")" ::: "memory")
DI bf16x8 asb(u32x4 x) { return __builtin_bit_cast(bf16x8, x); }

DI float gelu_f(float x) {
  const float c0 = -2.f * 0.7978845608028654f * 1.4426950408889634f, c1 = c0 * 0.044715f;
  const float z = x * fmaf(x * x, c1, c0);
  return x * __builtin_amdgcn_rcpf(1.f + __builtin_amdgcn_exp2f(z));
}

constexpr int G1_LDS = 143360;
template <int ST>
DI void g1_issue(u32x4 (&Y)[20][2], u32 lds0, u32 l16, __amdgpu_buffer_rsrc_t srdW, u32 wbase, __amdgpu_buffer_rsrc_t srdY, u32 ybase, int w) {
#pragma unroll
  for (int i = 0; i < 9; i++) {
    int idx = 4 * i + w; idx = idx > 34 ? 34 : idx;
    const u32 o = (35 * ST + idx) * 1024;
    dma16(lds0 + o, l16, srdW, wbase + o);
  }
#pragma unroll
  for (int k = 0; k < 5; k++) {
    const int ks = 5 * ST + k;
    bload<0>(Y[ks][0], l16, srdY, ybase + ks * 2048);
    bload<1024>(Y[ks][1], l16, srdY, ybase + ks * 2048);
  }
}
template <int ST>
DI void g1_compute(u32x4 (&Y)[20][2], f32x4 (&acc)[7][2], const char* smem, int lane) {
#pragma unroll
  for (int k = 0; k < 5; k++) asm volatile("" : "+v"(Y[5 * ST + k][0]), "+v"(Y[5 * ST + k][1]));
#pragma unroll
  for (int k = 0; k < 5; k++) {
    const int ks = 5 * ST + k;
#pragma unroll
    for (int f = 0; f < 7; f++) {
      const bf16x8 a = *(const bf16x8*)(smem + (ks * 7 + f) * 1024 + lane * 16);
      acc[f][0] = mfma16(a, asb(Y[ks][0]), acc[f][0]);
      acc[f][1] = mfma16(a, asb(Y[ks][1]), acc[f][1]);
    }
  }
}
__global__ __launch_bounds__(256, 1) void k_gemm1(const u16* __restrict__ b1, char* __restrict__ ws) {
  STAMP_BEGIN(ws)
  extern __shared__ __attribute__((aligned(16))) char smem[];
  const int tid = threadIdx.x, lane = tid & 63, w = rfl(tid >> 6);
  const int b = blockIdx.x, j = b >> 3, tile = 2 * (b & 7) + (j >> 4), g = j & 15;
  const __amdgpu_buffer_rsrc_t srdW = mksrd(ws + WS_W1G, 2293760), srdY = mksrd(ws + WS_Y3F, 2621440);
  const u32 l16 = lane * 16, lds0 = ldsaddr(smem);
  const u32 wbase = g * 143360, ybase = (tile * 4 + w) * 40960;
  u32x4 Y[20][2];
  asm volatile("s_nop 4");
  g1_issue<0>(Y, lds0, l16, srdW, wbase, srdY, ybase, w);
  g1_issue<1>(Y, lds0, l16, srdW, wbase, srdY, ybase, w);
  g1_issue<2>(Y, lds0, l16, srdW, wbase, srdY, ybase, w);
  const int q = lane >> 4;
  f32x4 acc[7][2];
#pragma unroll
  for (int f = 0; f < 7; f++) acc[f][0] = acc[f][1] = (f32x4){0.f, 0.f, 0.f, 0.f};
  MARK(ws, 3, 0);
  WAITV(38); __builtin_amdgcn_s_barrier();
  MARK(ws, 3, 1);
  g1_issue<3>(Y, lds0, l16, srdW, wbase, srdY, ybase, w);
  g1_compute<0>(Y, acc, smem, lane);
  MARK(ws, 3, 2);
  WAITV(38); __builtin_amdgcn_s_barrier();
  MARK(ws, 3, 3);
  g1_compute<1>(Y, acc, smem, lane);
  MARK(ws, 3, 4);
  WAITV(19); __builtin_amdgcn_s_barrier();
  MARK(ws, 3, 5);
  g1_compute<2>(Y, acc, smem, lane);
  MARK(ws, 3, 6);
  WAITV(0); __builtin_amdgcn_s_barrier();
  MARK(ws, 3, 7);
  g1_compute<3>(Y, acc, smem, lane);
  MARK(ws, 3, 8);
  char* Hf = ws + WS_HF;
#pragma unroll
  for (int f = 0; f < 7; f++) {
    const int ft = 7 * g + f;
    if (ft < 108) {
      const u32x2 bb = *(const u32x2*)(b1 + 16 * ft + 4 * q);
      const float bv[4] = {lo16(bb[0]), hi16(bb[0]), lo16(bb[1]), hi16(bb[1])};
#pragma unroll
      for (int tt = 0; tt < 2; tt++) {
        const int rt = 8 * tile + 2 * w + tt;
        const float h0 = gelu_f(acc[f][tt][0] + bv[0]), h1 = gelu_f(acc[f][tt][1] + bv[1]);
        const float h2 = gelu_f(acc[f][tt][2] + bv[2]), h3 = gelu_f(acc[f][tt][3] + bv[3]);
        *(u32x2*)(Hf + ((size_t)((rt >> 2) * 54 + (ft >> 1)) * 4 + (rt & 3)) * 1024 + lane * 16 + 8 * (ft & 1)) = (u32x2){pk2(h0, h1), pk2(h2, h3)};
      }
    }
  }
  MARK(ws, 3, 9);
  STAMP_END(ws, 3);
}

struct G2F { u32x4 a[5]; u32x4 h[4]; };
DI void g2_load(G2F& f, u32 l16, __amdgpu_buffer_rsrc_t srdW, u32 wo, __amdgpu_buffer_rsrc_t srdH, u32 ho) {
  bload<0>(f.a[0], l16, srdW, wo); bload<1024>(f.a[1], l16, srdW, wo); bload<2048>(f.a[2], l16, srdW, wo); bload<3072>(f.a[3], l16, srdW, wo);
  bload<0>(f.a[4], l16, srdW, wo + 4096);
  bload<0>(f.h[0], l16, srdH, ho); bload<1024>(f.h[1], l16, srdH, ho); bload<2048>(f.h[2], l16, srdH, ho); bload<3072>(f.h[3], l16, srdH, ho);
}
DI void g2_mma(G2F& f, f32x4 (&acc)[5][4]) {
  asm volatile("" : "+v"(f.a[0]), "+v"(f.a[1]), "+v"(f.a[2]), "+v"(f.a[3]), "+v"(f.a[4]), "+v"(f.h[0]), "+v"(f.h[1]), "+v"(f.h[2]), "+v"(f.h[3]));
#pragma unroll
  for (int d = 0; d < 5; d++)
#pragma unroll
    for (int t = 0; t < 4; t++) acc[d][t] = mfma16(asb(f.a[d]), asb(f.h[t]), acc[d][t]);
}
constexpr int G2_LDS = 4 * 20 * 1024;
__global__ __launch_bounds__(256, 1) void k_gemm2(const u16* __restrict__ b2, char* __restrict__ ws, u16* __restrict__ out) {
  STAMP_BEGIN(ws)
  extern __shared__ __attribute__((aligned(16))) char smem[];
  const int tid = threadIdx.x, lane = tid & 63, w = rfl(tid >> 6);
  const int b = blockIdx.x, j = b >> 3, TT = 4 * (b & 7) + (j >> 3), cg = j & 7;
  const __amdgpu_buffer_rsrc_t srdW = mksrd(ws + WS_W2F, 2211840), srdH = mksrd(ws + WS_HF, 7077888);
  const u32 l16 = lane * 16;
  const int ks0 = w < 2 ? 14 * w + 1 : 28 + 13 * (w - 2);
  const u32 wo = cg * 276480 + ks0 * 5120, ho = TT * 221184 + ks0 * 4096;
  f32x4 acc[5][4];
#pragma unroll
  for (int d = 0; d < 5; d++)
#pragma unroll
    for (int t = 0; t < 4; t++) acc[d][t] = (f32x4){0.f, 0.f, 0.f, 0.f};
  G2F f0, f1, f2, f3, fx;
  asm volatile("s_nop 4");
  if (w < 2) g2_load(fx, l16, srdW, wo - 5120, srdH, ho - 4096);
  g2_load(f0, l16, srdW, wo, srdH, ho);
  g2_load(f1, l16, srdW, wo + 5120, srdH, ho + 4096);
  g2_load(f2, l16, srdW, wo + 2 * 5120, srdH, ho + 2 * 4096);
  g2_load(f3, l16, srdW, wo + 3 * 5120, srdH, ho + 3 * 4096);
  MARK(ws, 4, 0);
  if (w < 2) { WAITV(36); g2_mma(fx, acc); }
  MARK(ws, 4, 1);
#define G2_STEP(F, N, WN, MORE) WAITV(WN); g2_mma(F, acc); if (MORE) g2_load(F, l16, srdW, wo + (N + 4) * 5120, srdH, ho + (N + 4) * 4096);
  G2_STEP(f0, 0, 27, true) G2_STEP(f1, 1, 27, true) G2_STEP(f2, 2, 27, true) G2_STEP(f3, 3, 27, true)
  MARK(ws, 4, 2);
  G2_STEP(f0, 4, 27, true) G2_STEP(f1, 5, 27, true) G2_STEP(f2, 6, 27, true) G2_STEP(f3, 7, 27, true)
  MARK(ws, 4, 3);
  G2_STEP(f0, 8, 27, true) G2_STEP(f1, 9, 27, false) G2_STEP(f2, 10, 18, false) G2_STEP(f3, 11, 9, false)
  G2_STEP(f0, 12, 0, false)
#undef G2_STEP
  MARK(ws, 4, 4);
  f32x4* red = (f32x4*)smem;
#pragma unroll
  for (int d = 0; d < 5; d++)
#pragma unroll
    for (int t = 0; t < 4; t++) if (t != w) red[((w * 5 + d) * 4 + t) * 64 + lane] = acc[d][t];
  __syncthreads();
  MARK(ws, 4, 5);
  const int q = lane >> 4, cc = lane & 15;
  const int tok = 64 * TT + 16 * w + cc;
  const u16* X1 = (const u16*)(ws + WS_X1);
#pragma unroll
  for (int d = 0; d < 5; d++) {
    f32x4 s = {0.f, 0.f, 0.f, 0.f};
#pragma unroll
    for (int t = 0; t < 4; t++) if (t == w) s = acc[d][t];
#pragma unroll
    for (int sw = 0; sw < 4; sw++) if (sw != w) s += red[((sw * 5 + d) * 4 + w) * 64 + lane];
    const int d0 = 80 * cg + 16 * d + 4 * q;
    const u32x2 bb = *(const u32x2*)(b2 + d0);
    const u32x2 gg = *(const u32x2*)(ws + WS_MODP + (d0 >> 2) * 48 + 40);
    const u32x2 xx = *(const u32x2*)(X1 + (size_t)tok * D + d0);
    const float o0 = fmaf(s[0] + lo16(bb[0]), lo16(gg[0]), lo16(xx[0])), o1 = fmaf(s[1] + hi16(bb[0]), hi16(gg[0]), hi16(xx[0]));
    const float o2 = fmaf(s[2] + lo16(bb[1]), lo16(gg[1]), lo16(xx[1])), o3 = fmaf(s[3] + hi16(bb[1]), hi16(gg[1]), hi16(xx[1]));
    *(u32x2*)(out + (size_t)tok * D + d0) = (u32x2){pk2(o0, o1), pk2(o2, o3)};
  }
  MARK(ws, 4, 6);
  STAMP_END(ws, 4);
}

extern "C" void kernel_launch(void* const* d_in, const int* in_sizes, int n_in, void* d_out, int out_size, void* d_ws, size_t ws_size,
                              hipStream_t stream) {
  const u16* q = (const u16*)d_in[0]; const u16* k = (const u16*)d_in[1]; const u16* v = (const u16*)d_in[2];
  const u16* shk = (const u16*)d_in[3]; const u16* ssk = (const u16*)d_in[4]; const u16* ssD = (const u16*)d_in[5];
  const u16* w1 = (const u16*)d_in[7]; const u16* b1 = (const u16*)d_in[8]; const u16* w2 = (const u16*)d_in[9];
  const u16* b2 = (const u16*)d_in[10]; const u16* c = (const u16*)d_in[11]; const u16* wmod = (const u16*)d_in[12];
  char* ws = (char*)d_ws;
  (void)hipFuncSetAttribute((const void*)k_conv, hipFuncAttributeMaxDynamicSharedMemorySize, K2_LDS);
  (void)hipFuncSetAttribute((const void*)k_gemm1, hipFuncAttributeMaxDynamicSharedMemorySize, G1_LDS);
  (void)hipFuncSetAttribute((const void*)k_gemm2, hipFuncAttributeMaxDynamicSharedMemorySize, G2_LDS);
  k_prep<<<K1_KV_BLOCKS + K1_RT_BLOCKS, 256, 0, stream>>>(k, v, shk, w1, w2, ws);
  k_conv<<<220, 256, K2_LDS, stream>>>(ssk, ssD, c, wmod, ws);
  k_mid<<<256, 256, 0, stream>>>(q, ws);
  k_gemm1<<<256, 256, G1_LDS, stream>>>(b1, ws);
  k_gemm2<<<256, 256, G2_LDS, stream>>>(b2, ws, (u16*)d_out);
}
```

```cpp
#include <hip/hip_runtime.h>
#include <stdint.h>

typedef unsigned short u16;
typedef unsigned int u32;
typedef short s16x4 __attribute__((ext_vector_type(4)));
typedef short s16x8 __attribute__((ext_vector_type(8)));
typedef __bf16 bf16x8 __attribute__((ext_vector_type(8)));
typedef __bf16 bf16x2 __attribute__((ext_vector_type(2)));
typedef float f32x4 __attribute__((ext_vector_type(4)));
typedef float f32x2 __attribute__((ext_vector_type(2)));
typedef u32 u32x4 __attribute__((ext_vector_type(4)));
typedef u32 u32x2 __attribute__((ext_vector_type(2)));
#define LAS __attribute__((address_space(3)))
#define GAS __attribute__((address_space(1)))
#define DI __device__ __forceinline__

constexpr int S = 2048, D = 640, F = 1728;
constexpr size_t WS_MODP = 0;
constexpr size_t WS_FLAG = 8192;
constexpr size_t WS_FLAGA = WS_FLAG, WS_FLAGB = WS_FLAG + 16384, WS_GSYNC = WS_FLAG + 32768, WS_FLAG_BYTES = 32768 + 2048;
constexpr size_t WS_ST1  = 49152;
constexpr size_t WS_U    = 65536;
constexpr size_t WS_Y2B  = WS_U + 2621440;
constexpr size_t WS_X1   = WS_Y2B + 2621440;
constexpr size_t WS_Y3F  = WS_X1 + 2621440;
constexpr size_t WS_HF   = WS_Y3F + 2621440;
constexpr size_t WS_W2F  = WS_HF + 7077888;

#define STAMP_BEGIN(ws)
#define STAMP_END(ws, kid)
#define MARK_DECL
#define MARK(i)
#define MARK_FLUSH(ws, kid, n)

DI float lo16(u32 x) { return __uint_as_float(x << 16); }
DI float hi16(u32 x) { return __uint_as_float(x & 0xffff0000u); }
DI u32 pk(f32x2 v) { return __builtin_bit_cast(u32, __builtin_convertvector(v, bf16x2)); }
DI u32 pk2(float a, float b) { return pk((f32x2){a, b}); }
DI f32x2 up(u32 x) { return (f32x2){lo16(x), hi16(x)}; }
DI f32x2 rb2(f32x2 v) { return up(pk(v)); }
DI float rbf(float a) { return lo16(pk2(a, a)); }
DI int rfl(int x) { return __builtin_amdgcn_readfirstlane(x); }
DI s16x4 trread(u32 ldsaddr) { return __builtin_amdgcn_ds_read_tr16_b64_v4i16((LAS s16x4*)(size_t)ldsaddr); }
DI bf16x8 cat8(s16x4 a, s16x4 b) { return __builtin_bit_cast(bf16x8, __builtin_shufflevector(a, b, 0, 1, 2, 3, 4, 5, 6, 7)); }
DI f32x4 mfma16(bf16x8 a, bf16x8 b, f32x4 c) { return __builtin_amdgcn_mfma_f32_16x16x32_bf16(a, b, c, 0, 0, 0); }
DI u32 ldsaddr(const void* p) { return (u32)(size_t)(LAS const char*)p; }
DI __amdgpu_buffer_rsrc_t mksrd(const void* p, u32 bytes) { return __builtin_amdgcn_make_buffer_rsrc((void*)p, 0, bytes, 0x00020000); }

DI void dma4(u32 m0v, u32 voff, __amdgpu_buffer_rsrc_t srd, u32 soff) {
  asm volatile("s_mov_b32 m0, %0\n\ts_nop 0\n\tbuffer_load_dword %1, %2, %3 offen lds" ::"s"(m0v), "v"(voff), "s"(srd), "s"(soff) : "m0");
}
__global__ __launch_bounds__(640) void k_prep(const u16* __restrict__ k, const u16* __restrict__ v, const u16* __restrict__ shk,
                                              const u16* __restrict__ ssk, char* __restrict__ ws) {
  STAMP_BEGIN(ws)
  __shared__ __attribute__((aligned(16))) char sm[64 * 176 + 256];
  const int tid = threadIdx.x, lane = tid & 63, w = rfl(tid >> 6);
  const int b = blockIdx.x;
  const int xcd = b & 7, tt = b >> 3;
  const int mh = tid / 20, cq = tid - 20 * mh;
  const int t0 = 64 * tt + 2 * mh, ch0 = 80 * xcd + 4 * cq;
  u32x2 kr[5], vr[2];
#pragma unroll
  for (int i = 0; i < 5; i++) {
    const int t = t0 - 3 + i;
    u32x2 x = *(const u32x2*)(k + (size_t)(t < 0 ? 0 : t) * D + ch0);
    if (t < 0) x = (u32x2){0u, 0u};
    kr[i] = x;
  }
#pragma unroll
  for (int i = 0; i < 2; i++) vr[i] = *(const u32x2*)(v + (size_t)(t0 + i) * D + ch0);
  const u32x4 s0 = *(const u32x4*)(shk + ch0 * 4), s1 = *(const u32x4*)(shk + ch0 * 4 + 8);
  if (w < 3) {
    const __amdgpu_buffer_rsrc_t srd = mksrd(ssk + (size_t)80 * xcd * S + tt * 5120, 10240);
    dma4(rfl(ldsaddr(sm) + 64 * 176), tid * 64, srd, 0);
  }
  if (b < 34 && tid < 64) *(u32x4*)(ws + WS_FLAG + b * 1024 + tid * 16) = (u32x4){0u, 0u, 0u, 0u};
  const f32x2 sk01[4] = {{lo16(s0[0]), lo16(s0[2])}, {hi16(s0[0]), hi16(s0[2])}, {lo16(s0[1]), lo16(s0[3])}, {hi16(s0[1]), hi16(s0[3])}};
  const f32x2 sk23[4] = {{lo16(s1[0]), lo16(s1[2])}, {hi16(s1[0]), hi16(s1[2])}, {lo16(s1[1]), lo16(s1[3])}, {hi16(s1[1]), hi16(s1[3])}};
#pragma unroll
  for (int r = 0; r < 2; r++) {
    u32x2 o;
#pragma unroll
    for (int h = 0; h < 2; h++) {
      const f32x2* sk = h ? sk23 : sk01;
      f32x2 a = rb2(up(kr[r][h]) * sk[3]);
      a = rb2(a + rb2(up(kr[r + 1][h]) * sk[2]));
      a = rb2(a + rb2(up(kr[r + 2][h]) * sk[1]));
      a = rb2(a + rb2(up(kr[r + 3][h]) * sk[0]));
      o[h] = pk(a * up(vr[r][h]));
    }
    *(u32x2*)(sm + (2 * mh + r) * 176 + 8 * cq) = o;
  }
  __syncthreads();
  const int grp = lane >> 4, i16 = lane & 15, jj = i16 >> 2, p = i16 & 3;
  const int task = 4 * w + grp, hr = task / 5, cgrp = task - 5 * hr, rho = hr >> 1, half = hr & 1;
  const u32 base = ldsaddr(sm) + (4 * (8 * half + jj) + rho) * 176 + (16 * cgrp + 4 * p) * 2;
  const s16x4 a = trread(base), c = trread(base + 16 * 176);
  *(bf16x8*)(ws + WS_U + ((size_t)(80 * xcd + 16 * cgrp + i16) * 32 + tt) * 128 + rho * 32 + half * 16) = cat8(a, c);
  asm volatile("s_waitcnt vmcnt(0)" ::: "memory");
  STAMP_END(ws, 0);
}

template <int IMM> DI void bload(u32x4& dst, u32 voff, __amdgpu_buffer_rsrc_t srd, u32 soff) {
  asm volatile("buffer_load_dwordx4 %0, %1, %2, %3 offen offset:%c4" : "=v"(dst) : "v"(voff), "s"(srd), "s"(soff), "i"(IMM));
}
template <int IMM> DI void bload_sc1(u32x4& dst, u32 voff, __amdgpu_buffer_rsrc_t srd, u32 soff) {
  asm volatile("buffer_load_dwordx4 %0, %1, %2, %3 offen offset:%c4 sc1" : "=v"(dst) : "v"(voff), "s"(srd), "s"(soff), "i"(IMM));
}
template <int IMM> DI void bload_a(u32x4& dst, u32 voff, __amdgpu_buffer_rsrc_t srd, u32 soff) {
  asm volatile("buffer_load_dwordx4 %0, %1, %2, %3 offen offset:%c4" : "=a"(dst) : "v"(voff), "s"(srd), "s"(soff), "i"(IMM));
}
template <int IMM> DI void bload_a_sc1(u32x4& dst, u32 voff, __amdgpu_buffer_rsrc_t srd, u32 soff) {
  asm volatile("buffer_load_dwordx4 %0, %1, %2, %3 offen offset:%c4 sc1" : "=a"(dst) : "v"(voff), "s"(srd), "s"(soff), "i"(IMM));
}
template <int IMM> DI void bload2_sc1(u32x2& dst, u32 voff, __amdgpu_buffer_rsrc_t srd, u32 soff) {
  asm volatile("buffer_load_dwordx2 %0, %1, %2, %3 offen offset:%c4 sc1" : "=v"(dst) : "v"(voff), "s"(srd), "s"(soff), "i"(IMM));
}
template <int IMM> DI void bload2(u32x2& dst, u32 voff, __amdgpu_buffer_rsrc_t srd, u32 soff) {
  asm volatile("buffer_load_dwordx2 %0, %1, %2, %3 offen offset:%c4" : "=v"(dst) : "v"(voff), "s"(srd), "s"(soff), "i"(IMM));
}
DI void dma16(u32 m0v, u32 voff, __amdgpu_buffer_rsrc_t srd, u32 soff) {
  asm volatile("s_mov_b32 m0, %0\n\ts_nop 0\n\tbuffer_load_dwordx4 %1, %2, %3 offen lds" ::"s"(m0v), "v"(voff), "s"(srd), "s"(soff) : "m0");
}
template <int IMM> DI void dsread64(u32x2& dst, u32 addr) { asm volatile("ds_read_b64 %0, %1 offset:%c2" : "=v"(dst) : "v"(addr), "i"(IMM)); }
typedef u32x4 frag;
DI void mfma_vv(f32x4& acc, const frag& a, const frag& b) { asm volatile("v_mfma_f32_16x16x32_bf16 %0, %1, %2, %0" : "+v"(acc) : "v"(a), "v"(b)); }
DI void mfma_vv0(f32x4& acc, const frag& a, const frag& b) { asm volatile("v_mfma_f32_16x16x32_bf16 %0, %1, %2, 0" : "=&v"(acc) : "v"(a), "v"(b)); }
DI void mfma_av(f32x4& acc, const frag& a, const frag& b) { asm volatile("v_mfma_f32_16x16x32_bf16 %0, %1, %2, %0" : "+v"(acc) : "a"(a), "v"(b)); }
DI void mfma_av0(f32x4& acc, const frag& a, const frag& b) { asm volatile("v_mfma_f32_16x16x32_bf16 %0, %1, %2, 0" : "=&v"(acc) : "a"(a), "v"(b)); }
template <int N> DI void waitv() { asm volatile("s_waitcnt vmcnt(%c0)" ::"i"(N) : "memory"); }
template <int N> DI void waitv2(u32x4& a, u32x4& b) { asm volatile("s_waitcnt vmcnt(%c2)" : "+v"(a), "+v"(b) : "i"(N)); }
template <int N> DI void waitv4(u32x4& a, u32x4& b, u32x4& c, u32x4& d) { asm volatile("s_waitcnt vmcnt(%c4)" : "+v"(a), "+v"(b), "+v"(c), "+v"(d) : "i"(N)); }
DI frag catf(s16x4 a, s16x4 b) { return __builtin_bit_cast(frag, __builtin_shufflevector(a, b, 0, 1, 2, 3, 4, 5, 6, 7)); }
#define SB0 __builtin_amdgcn_sched_barrier(0)

constexpr int CV_RS = 1184;
constexpr int CV_KB = 4128;
constexpr int CV_WB = CV_KB + 4 * CV_RS;
template <int OFF> DI frag cv_ldB(u32 vb) { return catf(*(LAS const s16x4*)(size_t)(vb + OFF), *(volatile LAS const s16x4*)(size_t)(vb + OFF + 8)); }
template <int G> constexpr int cv_off0() { return 2 * (G >> 4) * CV_RS + 64 * (G & 15); }
template <int G> constexpr int cv_off1() { return (2 * (G >> 4) + 1) * CV_RS + 64 * (15 - (G & 15)); }
template <int G, int I> DI void cv_one(f32x4 (&acc)[4][8], const frag (&A)[16], const frag& B0, const frag& B1) {
  constexpr int R = 2 * (G >> 4), N = G & 15;
  constexpr int c0 = N <= 1 ? 0 : N / 2;
  constexpr int n0 = 8 - c0;
  if constexpr (I < n0) {
    constexpr int cb = c0 + I, s = 1 - N + 2 * cb;
    if constexpr (N == 0) mfma_vv0(acc[R][cb], A[s], B0); else mfma_vv(acc[R][cb], A[s], B0);
  } else {
    constexpr int c1 = (15 - N) / 2, cb = c1 + (I - n0), s = N - 14 + 2 * cb;
    static_assert(cb <= 7 && s >= 0 && s <= 15, "");
    if constexpr (s == 0) mfma_vv0(acc[R + 1][cb], A[s], B1); else mfma_vv(acc[R + 1][cb], A[s], B1);
  }
}
struct W1C { u32 vo[7]; u32 m0b, sob; __amdgpu_buffer_rsrc_t srd; };
template <int N, int R> DI void w1_m0(const W1C& c) { asm volatile("s_add_u32 m0, %0, %1" ::"s"(c.m0b), "i"(N * 4 * 7168 + R * 1024) : "scc", "m0"); }
template <int N, int R> DI void w1_ld(const W1C& c) {
  asm volatile("buffer_load_dwordx4 %0, %1, %2 offen lds" ::"v"(c.vo[R]), "s"(c.srd), "s"(c.sob + N * 4 * (32 * F * 2)));
}
template <int N, int R> DI void w1_piece(const W1C& c) { w1_m0<N, R>(c); asm volatile("s_nop 0"); w1_ld<N, R>(c); }
template <int N> DI void w1_step(const W1C& c) {
  w1_piece<N, 0>(c); w1_piece<N, 1>(c); w1_piece<N, 2>(c); w1_piece<N, 3>(c); w1_piece<N, 4>(c); w1_piece<N, 5>(c); w1_piece<N, 6>(c);
}
template <int G> DI void cv_steps(f32x4 (&acc)[4][8], const frag (&A)[16], u32 vb, frag P0, frag P1, frag Q0, frag Q1, const W1C& wc) {
  frag N0, N1;
  cv_one<G, 0>(acc, A, P0, P1); SB0;
  if constexpr (G + 2 < 32) N0 = cv_ldB<cv_off0<G + 2>()>(vb);
  SB0; cv_one<G, 1>(acc, A, P0, P1); cv_one<G, 2>(acc, A, P0, P1); SB0;
  if constexpr (G + 2 < 32) N1 = cv_ldB<cv_off1<G + 2>()>(vb);
  SB0; cv_one<G, 3>(acc, A, P0, P1); cv_one<G, 4>(acc, A, P0, P1);
  if constexpr (G < 28) w1_m0<G / 7, G % 7>(wc);
  cv_one<G, 5>(acc, A, P0, P1);
  if constexpr (G < 28) w1_ld<G / 7, G % 7>(wc);
  cv_one<G, 6>(acc, A, P0, P1); cv_one<G, 7>(acc, A, P0, P1); cv_one<G, 8>(acc, A, P0, P1);
  SB0;
  if constexpr (G + 1 < 32) cv_steps<G + 1>(acc, A, vb, Q0, Q1, N0, N1, wc);
}
DI float wsum64(float x) {
  x += __int_as_float(__builtin_amdgcn_update_dpp(0, __float_as_int(x), 0xB1, 0xf, 0xf, false));
  x += __int_as_float(__builtin_amdgcn_update_dpp(0, __float_as_int(x), 0x4E, 0xf, 0xf, false));
  x += __int_as_float(__builtin_amdgcn_update_dpp(0, __float_as_int(x), 0x141, 0xf, 0xf, false));
  x += __int_as_float(__builtin_amdgcn_update_dpp(0, __float_as_int(x), 0x128, 0xf, 0xf, false));
  { auto r = __builtin_amdgcn_permlane16_swap(__float_as_uint(x), __float_as_uint(x), false, false); x = __uint_as_float(r[0]) + __uint_as_float(r[1]); }
  { auto r = __builtin_amdgcn_permlane32_swap(__float_as_uint(x), __float_as_uint(x), false, false); x = __uint_as_float(r[0]) + __uint_as_float(r[1]); }
  return x;
}
DI void ln_finish(float sum, float sq, float& mean, float& rinv) {
  const float m = sum * (1.f / D);
  const float var = fmaxf(sq * (1.f / D) - m * m, 0.f);
  mean = rbf(m);
  rinv = __builtin_amdgcn_rcpf(rbf(__builtin_amdgcn_sqrtf(rbf(rbf(var) + 1e-5f))));
}

constexpr int L_W1 = 0;
constexpr int L_Y = 143360;
constexpr int L_MOD = L_Y + 10240;
constexpr int L_RED = L_MOD + 8192;
constexpr int L_TOTAL = 163840;
constexpr int L_CONV = L_TOTAL - 4 * CV_WB;
static_assert(L_CONV >= 16 * 7168 && (L_CONV & 15) == 0 && L_RED + 1024 <= L_TOTAL, "");

template <int NL> DI void poll_ge(const u32* words, u32 want, int lane) {
  const GAS u32* p = (const GAS u32*)words + (lane & (NL - 1)) * 16;
  for (u32 spins = 0; spins < (1u << 18); spins++) {
    const u32 v = __hip_atomic_load(p, __ATOMIC_RELAXED, __HIP_MEMORY_SCOPE_AGENT);
    if (__all(v >= want)) break;
    __builtin_amdgcn_s_sleep(1);
  }
}
DI void lds_barrier() { asm volatile("s_waitcnt lgkmcnt(0)" ::: "memory"); __builtin_amdgcn_s_barrier(); asm volatile("" ::: "memory"); }

DI void phase_conv(const u16* __restrict__ ssk, const u16* __restrict__ ssD, char* __restrict__ ws, char* smem, int xcd, int j, int w, int lane, const W1C& wc
) {
  const int ch = 80 * xcd + 4 * j + w;
  char* kim = smem + L_CONV + w * CV_WB;
  char* uim = kim + CV_KB;
  u32x4 ru[4], rk[4];
#pragma unroll
  for (int i = 0; i < 4; i++) ru[i] = *(const u32x4*)(ws + WS_U + (size_t)ch * 4096 + (64 * i + lane) * 16);
#pragma unroll
  for (int i = 0; i < 4; i++) rk[i] = *(const u32x4*)(ssk + (size_t)ch * S + 8 * (64 * i + lane));
  float Dch = lo16(ssD[ch]);
  if (lane < 32) *(u32x4*)(uim + (lane >> 3) * CV_RS + 16 * (lane & 7)) = (u32x4){0u, 0u, 0u, 0u};
  if (lane < 2) *(u32x4*)(kim + 16 * lane) = (u32x4){0u, 0u, 0u, 0u};
#pragma unroll
  for (int i = 0; i < 4; i++) *(u32x4*)(kim + 32 + 16 * (64 * i + lane)) = rk[i];
  {
    char* ud = uim + ((lane & 7) >> 1) * CV_RS + 128 + 32 * (lane >> 3) + 16 * (lane & 1);
#pragma unroll
    for (int i = 0; i < 4; i++) *(u32x4*)(ud + 256 * i) = ru[i];
  }
  asm volatile("" : "+v"(Dch));
  MARK(0);
  const int q = lane >> 4, cc = lane & 15, jj = cc >> 2, p = cc & 3;
  const u32 va = ldsaddr(kim) + 224 - 64 * q - 8 * jj + 8 * p;
  const u32 vb = ldsaddr(uim) + 8 + 8 * cc + 16 * q;
  frag A[16];
#pragma unroll
  for (int s = 0; s < 16; s++) A[s] = catf(trread(va + 256 * s + 32), trread(va + 256 * s));
  f32x4 acc[4][8];
  frag P0, P1, Q0, Q1;
  P0 = cv_ldB<cv_off0<0>()>(vb); P1 = cv_ldB<cv_off1<0>()>(vb); Q0 = cv_ldB<cv_off0<1>()>(vb); Q1 = cv_ldB<cv_off1<1>()>(vb);
  MARK(1);
  cv_steps<0>(acc, A, vb, P0, P1, Q0, Q1, wc);
  asm volatile("s_nop 7\n\ts_nop 1");
#pragma unroll
  for (int r = 0; r < 4; r++)
#pragma unroll
    for (int cb = 0; cb < 8; cb += 4) asm volatile("" : "+v"(acc[r][cb]), "+v"(acc[r][cb + 1]), "+v"(acc[r][cb + 2]), "+v"(acc[r][cb + 3]));
  MARK(2);
  const int idx = 4 * (lane == 0 ? 63 : (lane >= 16 ? lane - 16 : lane + 47));
  const u32 ub = ldsaddr(uim) + 2 * (64 + 4 * cc + q);
  const __amdgpu_buffer_rsrc_t sY = mksrd(ws + WS_Y2B, 2621440);
  const u32 yo = ((cc >> 2) * D + ch) * 128 + 32 * (cc & 3) + 8 * q;
  const f32x2 DD = {Dch, Dch};
  float o[8][4], cy[8][3];
  u32 u01[8], u23[8];
#pragma unroll
  for (int cb = 0; cb < 8; cb++) {
    const LAS u16* up_ = (const LAS u16*)(size_t)(ub + 128 * cb);
    u01[cb] = (u32)up_[0] | ((u32)up_[CV_RS / 2] << 16);
    u23[cb] = (u32)up_[CV_RS] | ((u32)up_[3 * CV_RS / 2] << 16);
  }
#pragma unroll
  for (int cb = 0; cb < 8; cb++) {
    const f32x4 z0 = acc[0][cb], z1 = acc[1][cb], z2 = acc[2][cb], z3 = acc[3][cb];
    o[cb][0] = z0[0]; o[cb][1] = z0[1] + z1[0]; o[cb][2] = z0[2] + z1[1] + z2[0]; o[cb][3] = (z0[3] + z1[2]) + (z2[1] + z3[0]);
    cy[cb][0] = z1[3] + z2[2] + z3[1]; cy[cb][1] = z2[3] + z3[2]; cy[cb][2] = z3[3];
  }
#pragma unroll
  for (int cb = 0; cb < 8; cb++)
#pragma unroll
    for (int e = 0; e < 3; e++) {
      const float t = lane == 63 ? (cb ? cy[cb - 1][e] : 0.f) : cy[cb][e];
      o[cb][e] += __int_as_float(__builtin_amdgcn_ds_bpermute(idx, __float_as_int(t)));
    }
#pragma unroll
  for (int cb = 0; cb < 8; cb++) {
    const f32x2 y01 = rb2((f32x2){o[cb][0], o[cb][1]}) + rb2(up(u01[cb]) * DD), y23 = rb2((f32x2){o[cb][2], o[cb][3]}) + rb2(up(u23[cb]) * DD);
    __builtin_amdgcn_raw_buffer_store_b64((u32x2){pk(y01), pk(y23)}, sY, yo + cb * (4 * D * 128), 0, 16);
  }
  MARK(3);
}

DI void phase_spare(const u16* __restrict__ c, const u16* __restrict__ wmod, const u16* __restrict__ w2, const u16* __restrict__ qin,
                    char* __restrict__ ws, char* smem, int xcd, int j, int w, int tid, int lane) {
  const int r0 = (j - 20) * 8 + xcd;
  const int sw = (j - 20) * 4 + w;
  u32x4 qa[6], qb[6];
#pragma unroll
  for (int i = 0; i < 6; i++) {
    const int tl = sw + 48 * i, t = 256 * xcd + (tl < 256 ? tl : 255);
    qa[i] = *(const u32x4*)(qin + (size_t)t * D + 8 * lane);
    qb[i] = *(const u32x4*)(qin + (size_t)t * D + 512 + 8 * (lane & 15));
  }
  const __amdgpu_buffer_rsrc_t sWS = mksrd(ws, 65536);
  if (r0 < 60) {
    float* sc = (float*)(smem + L_Y);
    float* red = sc + 640;
    const int col0 = 64 * r0, cl = tid & 7, rg = tid >> 3;
    u32x4 r[20];
#pragma unroll
    for (int i = 0; i < 20; i++) r[i] = *(const u32x4*)(wmod + (size_t)(rg + 32 * i) * (6 * D) + col0 + 8 * cl);
    for (int i = tid; i < D; i += 256) { float x = lo16(c[i]); sc[i] = rbf(x / (1.f + __expf(-x))); }
    __syncthreads();
    f32x2 a[4] = {{0.f, 0.f}, {0.f, 0.f}, {0.f, 0.f}, {0.f, 0.f}};
#pragma unroll
    for (int i = 0; i < 20; i++) {
      const float s = sc[rg + 32 * i];
#pragma unroll
      for (int e = 0; e < 4; e++) a[e] += up(r[i][e]) * (f32x2){s, s};
    }
    float af[8] = {a[0][0], a[0][1], a[1][0], a[1][1], a[2][0], a[2][1], a[3][0], a[3][1]};
#pragma unroll
    for (int e = 0; e < 8; e++) af[e] += __int_as_float(__builtin_amdgcn_update_dpp(0, __float_as_int(af[e]), 0x128, 0xf, 0xf, false));
#pragma unroll
    for (int e = 0; e < 8; e++) { auto rr = __builtin_amdgcn_permlane16_swap(__float_as_uint(af[e]), __float_as_uint(af[e]), false, false); af[e] = __uint_as_float(rr[0]) + __uint_as_float(rr[1]); }
#pragma unroll
    for (int e = 0; e < 8; e++) { auto rr = __builtin_amdgcn_permlane32_swap(__float_as_uint(af[e]), __float_as_uint(af[e]), false, false); af[e] = __uint_as_float(rr[0]) + __uint_as_float(rr[1]); }
    if (lane < 8) { *(f32x4*)(red + w * 64 + 8 * lane) = (f32x4){af[0], af[1], af[2], af[3]}; *(f32x4*)(red + w * 64 + 8 * lane + 4) = (f32x4){af[4], af[5], af[6], af[7]}; }
    __syncthreads();
    if (tid < 32) {
      const int c0 = 2 * tid;
      float s0 = rbf((red[c0] + red[64 + c0]) + (red[128 + c0] + red[192 + c0]));
      float s1 = rbf((red[c0 + 1] + red[65 + c0]) + (red[129 + c0] + red[193 + c0]));
      const int jc = col0 + c0, vv = jc / D, ch = jc - vv * D;
      if (vv == 1 || vv == 4) { s0 += 1.f; s1 += 1.f; }
      const int slot = vv == 0 ? 1 : vv == 1 ? 0 : vv == 2 ? 2 : vv == 3 ? 4 : vv == 4 ? 3 : 5;
      __builtin_amdgcn_raw_buffer_store_b32(pk2(s0, s1), sWS, WS_MODP + ((ch >> 2) * 24 + slot * 4 + (ch & 3)) * 2, 0, 16);
    }
  } else {
    const int wv = (r0 - 60) * 4 + w;
    char* mb = smem + L_Y + w * (32 * 144);
    u32x4 r[4][4];
#pragma unroll
    for (int n = 0; n < 4; n++) {
      const int u = wv + 144 * n, uc = u < 540 ? u : 539, ks = uc / 10, cc = uc - 10 * ks;
#pragma unroll
      for (int i = 0; i < 4; i++) r[n][i] = *(const u32x4*)(w2 + (size_t)(32 * ks + 8 * i + (lane >> 3)) * D + 64 * cc + 8 * (lane & 7));
    }
    const int q = lane >> 4, i16 = lane & 15, jj = i16 >> 2, p = i16 & 3;
    const __amdgpu_buffer_rsrc_t sO = mksrd(ws + WS_W2F, 2211840);
    const u32 a0 = ldsaddr(mb) + (4 * q + jj) * 144 + 8 * p;
#pragma unroll
    for (int n = 0; n < 4; n++) {
      const int u = wv + 144 * n, uc = u < 540 ? u : 539, ks = uc / 10, cc = uc - 10 * ks;
#pragma unroll
      for (int i = 0; i < 4; i++) *(u32x4*)(mb + (8 * i + (lane >> 3)) * 144 + 16 * (lane & 7)) = r[n][i];
      frag v[4];
#pragma unroll
      for (int f = 0; f < 4; f++) v[f] = catf(trread(a0 + 32 * f), trread(a0 + 32 * f + 16 * 144));
#pragma unroll
      for (int f = 0; f < 4; f++) {
        const int ft = 4 * cc + f;
        if (u < 540) __builtin_amdgcn_raw_buffer_store_b128(v[f], sO, (((ft / 5) * 54 + ks) * 5 + (ft % 5)) * 1024 + lane * 16, 0, 16);
      }
    }
  }
  float s1[6], s2[6];
#pragma unroll
  for (int i = 0; i < 6; i++) {
    f32x2 a = {0.f, 0.f}, bq = {0.f, 0.f};
#pragma unroll
    for (int e = 0; e < 4; e++) {
      const f32x2 x = up(qa[i][e]), y = lane < 16 ? up(qb[i][e]) : (f32x2){0.f, 0.f};
      a += x + y; bq += x * x + y * y;
    }
    s1[i] = a[0] + a[1]; s2[i] = bq[0] + bq[1];
  }
#define RED12(EXPR) _Pragma("unroll") for (int i = 0; i < 6; i++) { { float& x = s1[i]; EXPR } { float& x = s2[i]; EXPR } }
  RED12(x += __int_as_float(__builtin_amdgcn_update_dpp(0, __float_as_int(x), 0xB1, 0xf, 0xf, false));)
  RED12(x += __int_as_float(__builtin_amdgcn_update_dpp(0, __float_as_int(x), 0x4E, 0xf, 0xf, false));)
  RED12(x += __int_as_float(__builtin_amdgcn_update_dpp(0, __float_as_int(x), 0x141, 0xf, 0xf, false));)
  RED12(x += __int_as_float(__builtin_amdgcn_update_dpp(0, __float_as_int(x), 0x128, 0xf, 0xf, false));)
  RED12(auto rr = __builtin_amdgcn_permlane16_swap(__float_as_uint(x), __float_as_uint(x), false, false); x = __uint_as_float(rr[0]) + __uint_as_float(rr[1]);)
  RED12(auto rr = __builtin_amdgcn_permlane32_swap(__float_as_uint(x), __float_as_uint(x), false, false); x = __uint_as_float(rr[0]) + __uint_as_float(rr[1]);)
#undef RED12
  float a = s1[0], bq = s2[0];
#pragma unroll
  for (int i = 1; i < 6; i++) { a = lane == i ? s1[i] : a; bq = lane == i ? s2[i] : bq; }
  float mean, rinv;
  ln_finish(a, bq, mean, rinv);
  const int tl = sw + 48 * lane;
  if (lane < 6 && tl < 256) __builtin_amdgcn_raw_buffer_store_b64((u32x2){__float_as_uint(mean), __float_as_uint(rinv)}, sWS, WS_ST1 + (256 * xcd + tl) * 8, 0, 16);
}

DI float xsum8_16_32(float x) {
  x += __int_as_float(__builtin_amdgcn_update_dpp(0, __float_as_int(x), 0x128, 0xf, 0xf, false));
  { auto r = __builtin_amdgcn_permlane16_swap(__float_as_uint(x), __float_as_uint(x), false, false); x = __uint_as_float(r[0]) + __uint_as_float(r[1]); }
  { auto r = __builtin_amdgcn_permlane32_swap(__float_as_uint(x), __float_as_uint(x), false, false); x = __uint_as_float(r[0]) + __uint_as_float(r[1]); }
  return x;
}
DI void ln_stats(const f32x2 (&x)[10], f32x2* red, int w, int lane, float& mean, float& rinv) {
  f32x2 a = ((x[0] + x[1]) + (x[2] + x[3])) + ((x[4] + x[5]) + (x[6] + x[7])) + (x[8] + x[9]);
  f32x2 b0 = x[0] * x[0], b1 = x[1] * x[1];
#pragma unroll
  for (int h = 2; h < 10; h += 2) { b0 += x[h] * x[h]; b1 += x[h + 1] * x[h + 1]; }
  b0 += b1;
  const float s1 = xsum8_16_32(a[0] + a[1]), s2 = xsum8_16_32(b0[0] + b0[1]);
  if (lane < 8) red[w * 8 + lane] = (f32x2){s1, s2};
  lds_barrier();
  const int tk = lane & 7;
  const f32x2 t = (red[tk] + red[8 + tk]) + (red[16 + tk] + red[24 + tk]);
  ln_finish(t[0], t[1], mean, rinv);
}
DI f32x2 gelu2(f32x2 x) {
  const float c0 = -2.f * 0.7978845608028654f * 1.4426950408889634f, c1 = c0 * 0.044715f;
  const f32x2 z = x * (x * x * (f32x2){c1, c1} + (f32x2){c0, c0});
  const f32x2 d = (f32x2){__builtin_amdgcn_exp2f(z[0]), __builtin_amdgcn_exp2f(z[1])} + (f32x2){1.f, 1.f};
  return x * (f32x2){__builtin_amdgcn_rcpf(d[0]), __builtin_amdgcn_rcpf(d[1])};
}
DI int rotk(int n, int rot, int m) { int k = n + rot; return k >= m ? k - m : k; }
struct W2C { u32 l16; int rot2; __amdgpu_buffer_rsrc_t srd; };
template <int I> DI void w2_load1(frag (&W2)[60], const W2C& c) {
  const u32 vo = c.l16 + rotk(I / 5, c.rot2, 14) * 5120;
  if constexpr (I % 5 < 4) bload_a_sc1<(I % 5) * 1024>(W2[I], vo, c.srd, 0); else bload_a_sc1<0>(W2[I], vo + 4096, c.srd, 0);
}
template <int I, int N> DI void w2_loads(frag (&W2)[60], const W2C& c) {
  w2_load1<I>(W2, c);
  if constexpr (N > 1) w2_loads<I + 1, N - 1>(W2, c);
}
constexpr int W2_M0 = 10, W2_M1 = 10;

struct G1S {
  frag Y[10][2];
  frag A0[7], A1[7];
  f32x4 acc[7][2];
};
template <int FI> DI frag g1_lda(u32 ab) { return catf(trread(ab + 32 * FI), trread(ab + 32 * FI + 1792)); }
struct G1C { u32 b0, l16, ybase; int rot1; __amdgpu_buffer_rsrc_t srdY; };
template <int KS, int FI>
DI void g1_f(G1S& s, frag (&Ac)[7], frag (&An)[7], frag (&W2)[60], const G1C& c, const W2C& c2, u32 ab) {
  mfma_vv(s.acc[FI][0], Ac[FI], s.Y[KS % 10][0]);
  SB0;
  if constexpr (KS < 19) An[FI] = g1_lda<FI>(ab);
  SB0;
  mfma_vv(s.acc[FI][1], Ac[FI], s.Y[KS % 10][1]);
  if constexpr (FI == 1 || FI == 4) w2_load1<W2_M0 + W2_M1 + 2 * KS + (FI == 4)>(W2, c2);
  SB0;
  if constexpr (FI < 6) g1_f<KS, FI + 1>(s, Ac, An, W2, c, c2, ab);
}
template <int KS>
DI void g1_step(G1S& s, frag (&W2)[60], const G1C& c, const W2C& c2) {
  constexpr int NY = KS < 10 ? 2 * (9 - KS) + 4 * KS : 2 * 9 + 2 * (19 - KS);
  waitv2<NY>(s.Y[KS % 10][0], s.Y[KS % 10][1]);
  const u32 ab = c.b0 + rotk(KS + 1, c.rot1, 20) * 7168;
  if constexpr (KS & 1) g1_f<KS, 0>(s, s.A1, s.A0, W2, c, c2, ab);
  else                  g1_f<KS, 0>(s, s.A0, s.A1, W2, c, c2, ab);
  if constexpr (KS < 10) {
    const u32 yo = c.ybase + rotk(KS + 10, c.rot1, 20) * 2048;
    bload_sc1<0>(s.Y[KS][0], c.l16, c.srdY, yo);
    bload_sc1<1024>(s.Y[KS][1], c.l16, c.srdY, yo);
  }
  if constexpr (KS < 19) g1_step<KS + 1>(s, W2, c, c2);
}
struct G2S {
  frag H[4][4];
  frag WX[10];
  f32x4 acc[5][4];
  u32x2 xx[5], gg[5], b2v[5];
};
struct G2C { u32 l16, vx, vb2, vg; int rot2; __amdgpu_buffer_rsrc_t srdH, srdX, srdB2, srdMD; };
template <int N, int T> DI void g2_hload1(G2S& s, const G2C& c, u32 vo) { bload_sc1<T * 1024>(s.H[N & 3][T], vo, c.srdH, 0); }
template <int N> DI void g2_hload(G2S& s, const G2C& c) {
  const u32 vo = c.l16 + rotk(N, c.rot2, 14) * 4096;
  g2_hload1<N, 0>(s, c, vo); g2_hload1<N, 1>(s, c, vo); g2_hload1<N, 2>(s, c, vo); g2_hload1<N, 3>(s, c, vo);
}
template <int I> DI void g2_eload(G2S& s, const G2C& c) {
  if constexpr (I < 5) bload2_sc1<32 * I>(s.xx[I], c.vx, c.srdX, 0);
  else if constexpr (I < 10) bload2<32 * (I - 5)>(s.b2v[I - 5], c.vb2, c.srdB2, 0);
  else bload2_sc1<192 * (I - 10)>(s.gg[I - 10], c.vg, c.srdMD, 0);
}
template <int N, int M>
DI void g2_m(G2S& s, frag (&W2)[60], const G2C& c, u32 vo) {
  constexpr int d = M >> 2, t = M & 3;
  if constexpr (N == 0) mfma_av0(s.acc[d][t], W2[d], s.H[0][t]);
  else if constexpr (N < 12) mfma_av(s.acc[d][t], W2[N * 5 + d], s.H[N & 3][t]);
  else mfma_vv(s.acc[d][t], s.WX[(N - 12) * 5 + d], s.H[N & 3][t]);
  if constexpr (N >= 1 && N + 3 < 14 && (M == 1 || M == 4 || M == 7 || M == 10)) g2_hload1<N + 3, (M - 1) / 3>(s, c, vo);
  if constexpr (N >= 8 && N < 11 && M >= 13 && M < 18) g2_eload<5 * (N - 8) + M - 13>(s, c);
  if constexpr (M < 19) g2_m<N, M + 1>(s, W2, c, vo);
}
template <int N> constexpr int g2_younger() {
  int cnt = 0; bool seen = false;
  for (int h = 0; h < 4; h++) { if (seen) cnt += 4; if (h == N) seen = true; }
  for (int n = 1; n < N; n++) {
    if (n + 3 < 14) { if (seen) cnt += 4; if (n + 3 == N) seen = true; }
    if (n >= 8 && n < 11 && seen) cnt += 5;
  }
  return cnt;
}
static_assert(g2_younger<0>() == 12 && g2_younger<1>() == 8 && g2_younger<4>() == 8 && g2_younger<9>() == 13 && g2_younger<10>() == 18 &&
              g2_younger<11>() == 23 && g2_younger<12>() == 14 && g2_younger<13>() == 5, "");
template <int N>
DI void g2_step(G2S& s, frag (&W2)[60], const G2C& c) {
  waitv4<g2_younger<N>()>(s.H[N & 3][0], s.H[N & 3][1], s.H[N & 3][2], s.H[N & 3][3]);
  const u32 vo = c.l16 + rotk(N + 3, c.rot2, 14) * 4096;
  g2_m<N, 0>(s, W2, c, vo);
  if constexpr (N < 13) g2_step<N + 1>(s, W2, c);
}

__global__ __launch_bounds__(256, 1) void k_main(const u16* __restrict__ qin, const u16* __restrict__ ssk, const u16* __restrict__ ssD,
                                                 const u16* __restrict__ cin, const u16* __restrict__ wmod, const u16* __restrict__ w1,
                                                 const u16* __restrict__ b1, const u16* __restrict__ w2, const u16* __restrict__ b2,
                                                 char* __restrict__ ws, u16* __restrict__ out) {
  STAMP_BEGIN(ws)
  MARK_DECL
  extern __shared__ __attribute__((aligned(16))) char smem[];
  const int tid = threadIdx.x, lane = tid & 63, w = rfl(tid >> 6);
  const int b = blockIdx.x, xcd = b & 7, j = b >> 3;
  const int tile = 4 * (xcd >> 1) + (j >> 3), g = 8 * (xcd & 1) + (j & 7);
  const int TT = 4 * xcd + (j >> 3), cg = j & 7;
  const u32 l16 = lane * 16, lds0 = ldsaddr(smem);
  W1C wc;
#pragma unroll
  for (int r = 0; r < 7; r++) { const u32 idx = 64 * r + lane, row = (idx * 4682u) >> 16; wc.vo[r] = row * (F * 2 - 224) + idx * 16; }
  wc.m0b = lds0 + L_W1 + w * 7168; wc.sob = w * (32 * F * 2) + 224 * g; wc.srd = mksrd(w1, D * F * 2);
  if (j < 20) {
    phase_conv(ssk, ssD, ws, smem, xcd, j, w, lane, wc
    );
  } else {
    phase_spare(cin, wmod, w2, qin, ws, smem, xcd, j, w, tid, lane);
  }
  waitv<0>();
  __builtin_amdgcn_s_barrier();
  MARK(4);
  u32* gsync = (u32*)(ws + WS_GSYNC);
  if (tid < 4) __hip_atomic_fetch_add((GAS u32*)gsync + (tid * 8 + xcd) * 16, 1u, __ATOMIC_RELAXED, __HIP_MEMORY_SCOPE_AGENT);
  if (j >= 20) { w1_step<0>(wc); w1_step<1>(wc); w1_step<2>(wc); w1_step<3>(wc); }
  w1_step<4>(wc);
  if (w == 0) poll_ge<8>(gsync + (j & 3) * 128, 32u, lane);
  __builtin_amdgcn_s_barrier();
  MARK(5);
  const int tg = 16 * tile + g, t0 = 8 * tg;
  const int tk = lane & 7, slot = lane >> 3, q = lane >> 4;
  const int ks0 = w < 2 ? 14 * w : 28 + 13 * (w - 2), nks = w < 2 ? 14 : 13;
  W2C c2;
  c2.l16 = l16; c2.srd = mksrd(ws + WS_W2F + (size_t)cg * 276480 + ks0 * 5120, nks * 5120);
  { int r2 = 2 * cg + 4 * (j >> 3); r2 = r2 >= 14 ? r2 - 14 : r2; c2.rot2 = r2 >= 14 ? r2 - 14 : r2; }
  const __amdgpu_buffer_rsrc_t srdMD = mksrd(ws + WS_MODP, 7680);
  frag W2[60];
  u32x2 bb[7];
  f32x2 x[10];
  {
    const __amdgpu_buffer_rsrc_t srdYB = mksrd(ws + WS_Y2B + (size_t)(tg >> 3) * 81920, 81920);
    const __amdgpu_buffer_rsrc_t srdQ = mksrd(qin + (size_t)t0 * D, 10240);
    const __amdgpu_buffer_rsrc_t srdST = mksrd(ws + WS_ST1 + (size_t)t0 * 8, 64);
    const __amdgpu_buffer_rsrc_t srdB1 = mksrd(b1, F * 2);
    u32x4 yr[3], mr[2];
    u32x2 xr[5], st;
    const u32 vy = tid * 128 + 16 * (tg & 7), vq = tk * 1280 + (40 * w + slot) * 8, vb = (16 * 7 * g + 4 * q) * 2;
    asm volatile("s_nop 4");
    bload_sc1<0>(yr[0], vy, srdYB, 0); bload_sc1<0>(yr[1], vy + 32768, srdYB, 0); bload_sc1<0>(yr[2], vy + 65536, srdYB, 0);
    bload_sc1<0>(mr[0], tid * 16, srdMD, 0); bload_sc1<0>(mr[1], tid * 16 + 4096, srdMD, 0);
    bload2<0>(xr[0], vq, srdQ, 0); bload2<64>(xr[1], vq, srdQ, 0); bload2<128>(xr[2], vq, srdQ, 0); bload2<192>(xr[3], vq, srdQ, 0); bload2<256>(xr[4], vq, srdQ, 0);
    bload2_sc1<0>(st, tk * 8, srdST, 0);
    bload2<0>(bb[0], vb, srdB1, 0); bload2<32>(bb[1], vb, srdB1, 0); bload2<64>(bb[2], vb, srdB1, 0); bload2<96>(bb[3], vb, srdB1, 0);
    bload2<128>(bb[4], vb, srdB1, 0); bload2<160>(bb[5], vb, srdB1, 0); bload2<192>(bb[6], vb, srdB1, 0);
    w2_loads<0, W2_M0>(W2, c2);
    asm volatile("s_waitcnt vmcnt(%c18)" : "+v"(yr[0]), "+v"(yr[1]), "+v"(yr[2]), "+v"(mr[0]), "+v"(mr[1]), "+v"(xr[0]), "+v"(xr[1]), "+v"(xr[2]), "+v"(xr[3]), "+v"(xr[4]), "+v"(st),
                 "+v"(bb[0]), "+v"(bb[1]), "+v"(bb[2]), "+v"(bb[3]), "+v"(bb[4]), "+v"(bb[5]), "+v"(bb[6]) : "i"(W2_M0));
    *(u32x4*)(smem + L_Y + tid * 16) = yr[0]; *(u32x4*)(smem + L_Y + 4096 + tid * 16) = yr[1];
    if (tid < 128) *(u32x4*)(smem + L_Y + 8192 + tid * 16) = yr[2];
    *(u32x4*)(smem + L_MOD + tid * 16) = mr[0]; *(u32x4*)(smem + L_MOD + 4096 + tid * 16) = mr[1];
#pragma unroll
    for (int i = 0; i < 5; i++) { x[2 * i] = up(xr[i][0]); x[2 * i + 1] = up(xr[i][1]); }
    lds_barrier();
    MARK(6);
    const int i16 = lane & 15, jj = i16 >> 2, p = i16 & 3;
    const u32 ya = lds0 + L_Y + (4 * (40 * w + 2 * q + (p >> 1)) + jj) * 16 + 8 * (p & 1);
    const char* mdp = smem + L_MOD + (40 * w + slot) * 48;
    u32 yy[10], w1v[10], sav[10], gav[10];
#pragma unroll
    for (int i = 0; i < 5; i++) {
      const u32x2 yu = __builtin_bit_cast(u32x2, trread(ya + i * 8 * 64));
      const u32x4 m0 = *(const u32x4*)(mdp + i * 384);
      const u32x2 m1 = *(const u32x2*)(mdp + i * 384 + 16);
      yy[2 * i] = yu[0]; yy[2 * i + 1] = yu[1]; w1v[2 * i] = m0[0]; w1v[2 * i + 1] = m0[1]; sav[2 * i] = m0[2]; sav[2 * i + 1] = m0[3]; gav[2 * i] = m1[0]; gav[2 * i + 1] = m1[1];
    }
    const f32x2 mm = {__uint_as_float(st[0]), __uint_as_float(st[0])}, rr = {__uint_as_float(st[1]), __uint_as_float(st[1])};
    f32x2 t[10];
    u32 o[10];
#define STAGE(EXPR) _Pragma("unroll") for (int h = 0; h < 10; h++) { EXPR; }
    STAGE(t[h] = rb2(x[h] - mm))
    STAGE(t[h] = rb2(t[h] * rr))
    STAGE(t[h] = rb2(t[h] * up(w1v[h])))
    STAGE(t[h] = rb2(t[h] + up(sav[h])))
    STAGE(t[h] = rb2(t[h] * up(yy[h])))
    STAGE(t[h] = rb2(up(gav[h]) * t[h]))
    STAGE(o[h] = pk(x[h] + t[h]))
    const __amdgpu_buffer_rsrc_t srdX1 = mksrd(ws + WS_X1, 2621440);
    const u32 xo = (t0 + tk) * 1280 + (40 * w + slot) * 8;
#pragma unroll
    for (int i = 0; i < 5; i++) __builtin_amdgcn_raw_buffer_store_b64((u32x2){o[2 * i], o[2 * i + 1]}, srdX1, xo + i * 64, 0, 16);
    STAGE(x[h] = up(o[h]))
    MARK(7);
    f32x2* red = (f32x2*)(smem + L_RED);
    float mean, rinv;
    ln_stats(x, red, w, lane, mean, rinv);
    {
      const f32x2 m2 = {mean, mean}, r2 = {rinv, rinv};
#pragma unroll
      for (int i = 0; i < 5; i++) {
        const u32x4 m0 = *(const u32x4*)(mdp + i * 384 + 16);
        const u32x2 m1 = *(const u32x2*)(mdp + i * 384 + 32);
        x[2 * i] = (x[2 * i] - m2) * r2 * up(m0[2]) + up(m1[0]);
        x[2 * i + 1] = (x[2 * i + 1] - m2) * r2 * up(m0[3]) + up(m1[1]);
      }
    }
    MARK(8);
    ln_stats(x, red + 32, w, lane, mean, rinv);
    {
      const f32x2 m3 = {mean, mean}, r3 = {rinv, rinv};
      STAGE(o[h] = pk((x[h] - m3) * r3))
      const int rt = tg >> 1, r = 8 * (tg & 1) + tk;
      const __amdgpu_buffer_rsrc_t srdY3 = mksrd(ws + WS_Y3F, 2621440);
      const u32 yo = (rt >> 1) * 40960 + (rt & 1) * 1024 + ((2 * (slot >> 2) + (slot & 1)) * 16 + r) * 16 + 8 * ((slot >> 1) & 1) + 5 * w * 2048;
#pragma unroll
      for (int i = 0; i < 5; i++) __builtin_amdgcn_raw_buffer_store_b64((u32x2){o[2 * i], o[2 * i + 1]}, srdY3, yo + i * 2048, 0, 16);
    }
#undef STAGE
  }
  MARK(9);
  waitv<0>();
  __builtin_amdgcn_s_barrier();
  u32* flagA = (u32*)(ws + WS_FLAGA) + tile * 256;
  if (tid == 0) __hip_atomic_store((GAS u32*)flagA + g * 16, 1u, __ATOMIC_RELAXED, __HIP_MEMORY_SCOPE_AGENT);
  MARK(10);
  w2_loads<W2_M0, W2_M1>(W2, c2);
  if (w == 0) poll_ge<16>(flagA, 1u, lane);
  __builtin_amdgcn_s_barrier();
  MARK(11);
  f32x4 hacc[7][2];
  {
    G1C c1;
    const int i16 = lane & 15, jj = i16 >> 2, p = i16 & 3;
    c1.srdY = mksrd(ws + WS_Y3F, 2621440); c1.l16 = l16;
    c1.rot1 = (5 * (j & 7)) >> 1;
    c1.ybase = (tile * 4 + w) * 40960;
    c1.b0 = lds0 + L_W1 + (16 * (q >> 1) + 4 * (q & 1) + jj) * 224 + 8 * p;
    G1S s;
    asm volatile("s_nop 4");
#pragma unroll
    for (int k = 0; k < 10; k++) { const u32 yo = c1.ybase + rotk(k, c1.rot1, 20) * 2048; bload_sc1<0>(s.Y[k][0], l16, c1.srdY, yo); bload_sc1<1024>(s.Y[k][1], l16, c1.srdY, yo); }
#pragma unroll
    for (int f = 0; f < 7; f++) {
      const f32x4 bv = {lo16(bb[f][0]), hi16(bb[f][0]), lo16(bb[f][1]), hi16(bb[f][1])};
      s.acc[f][0] = bv; s.acc[f][1] = bv;
      asm volatile("" : "+v"(s.acc[f][0]), "+v"(s.acc[f][1]));
    }
    const u32 ab0 = c1.b0 + c1.rot1 * 7168;
    s.A0[0] = g1_lda<0>(ab0); s.A0[1] = g1_lda<1>(ab0); s.A0[2] = g1_lda<2>(ab0); s.A0[3] = g1_lda<3>(ab0);
    s.A0[4] = g1_lda<4>(ab0); s.A0[5] = g1_lda<5>(ab0); s.A0[6] = g1_lda<6>(ab0);
    SB0;
    g1_step<0>(s, W2, c1, c2);
    asm volatile("s_nop 7\n\ts_nop 1");
#pragma unroll
    for (int f = 0; f < 7; f++) { asm volatile("" : "+v"(s.acc[f][0]), "+v"(s.acc[f][1])); hacc[f][0] = s.acc[f][0]; hacc[f][1] = s.acc[f][1]; }
  }
  MARK(12);
  G2S s2_;
  {
    const u32 vo = l16 + rotk(12, c2.rot2, 14) * 5120, v1 = l16 + rotk(13, c2.rot2, 14) * 5120;
    bload_sc1<0>(s2_.WX[0], vo, c2.srd, 0); bload_sc1<1024>(s2_.WX[1], vo, c2.srd, 0); bload_sc1<2048>(s2_.WX[2], vo, c2.srd, 0); bload_sc1<3072>(s2_.WX[3], vo, c2.srd, 0);
    bload_sc1<0>(s2_.WX[4], vo + 4096, c2.srd, 0);
    bload_sc1<0>(s2_.WX[5], v1, c2.srd, 0); bload_sc1<1024>(s2_.WX[6], v1, c2.srd, 0); bload_sc1<2048>(s2_.WX[7], v1, c2.srd, 0); bload_sc1<3072>(s2_.WX[8], v1, c2.srd, 0);
    bload_sc1<0>(s2_.WX[9], v1 + 4096, c2.srd, 0);
    const __amdgpu_buffer_rsrc_t srdH = mksrd(ws + WS_HF, 7077888);
    u32x2 hp[7][2];
#pragma unroll
    for (int f = 0; f < 7; f++) {
      f32x2 g0 = gelu2((f32x2){hacc[f][0][0], hacc[f][0][1]}), g1 = gelu2((f32x2){hacc[f][0][2], hacc[f][0][3]});
      f32x2 g2 = gelu2((f32x2){hacc[f][1][0], hacc[f][1][1]}), g3 = gelu2((f32x2){hacc[f][1][2], hacc[f][1][3]});
      hp[f][0] = (u32x2){pk(g0), pk(g1)}; hp[f][1] = (u32x2){pk(g2), pk(g3)};
    }
    const int rt0 = 8 * tile + 2 * w;
    const u32 ho = ((rt0 >> 2) * 54 * 4 + (rt0 & 3)) * 1024 + l16;
    const int k0 = (7 * g) >> 1;
    if (g & 1) {
#pragma unroll
      for (int tt = 0; tt < 2; tt++) {
        __builtin_amdgcn_raw_buffer_store_b64(hp[0][tt], srdH, ho + tt * 1024 + k0 * 4096 + 8, 0, 16);
#pragma unroll
        for (int i = 0; i < 3; i++)
          if (7 * g + 2 * i + 2 < 108)
            __builtin_amdgcn_raw_buffer_store_b128((u32x4){hp[2 * i + 1][tt][0], hp[2 * i + 1][tt][1], hp[2 * i + 2][tt][0], hp[2 * i + 2][tt][1]}, srdH, ho + tt * 1024 + (k0 + 1 + i) * 4096, 0, 16);
      }
    } else {
#pragma unroll
      for (int tt = 0; tt < 2; tt++) {
#pragma unroll
        for (int i = 0; i < 3; i++)
          __builtin_amdgcn_raw_buffer_store_b128((u32x4){hp[2 * i][tt][0], hp[2 * i][tt][1], hp[2 * i + 1][tt][0], hp[2 * i + 1][tt][1]}, srdH, ho + tt * 1024 + (k0 + i) * 4096, 0, 16);
        __builtin_amdgcn_raw_buffer_store_b64(hp[6][tt], srdH, ho + tt * 1024 + (k0 + 3) * 4096, 0, 16);
      }
    }
  }
  MARK(13);
  asm volatile("s_waitcnt vmcnt(0)" : "+v"(s2_.WX[0]), "+v"(s2_.WX[1]), "+v"(s2_.WX[2]), "+v"(s2_.WX[3]), "+v"(s2_.WX[4]), "+v"(s2_.WX[5]), "+v"(s2_.WX[6]), "+v"(s2_.WX[7]), "+v"(s2_.WX[8]), "+v"(s2_.WX[9]) :: "memory");
  __builtin_amdgcn_s_barrier();
  u32* flagB = (u32*)(ws + WS_FLAGB);
  if (tid == 0) __hip_atomic_store((GAS u32*)flagB + tile * 256 + g * 16, 1u, __ATOMIC_RELAXED, __HIP_MEMORY_SCOPE_AGENT);
  MARK(14);
  const int cc = lane & 15, tok = 64 * TT + 16 * w + cc;
  G2C cg2;
  cg2.l16 = l16; cg2.rot2 = c2.rot2;
  cg2.srdH = mksrd(ws + WS_HF + (size_t)TT * 221184 + ks0 * 4096, nks * 4096);
  cg2.srdX = mksrd(ws + WS_X1, 2621440); cg2.srdB2 = mksrd(b2, D * 2); cg2.srdMD = srdMD;
  cg2.vx = tok * 1280 + (80 * cg + 4 * q) * 2; cg2.vb2 = (80 * cg + 4 * q) * 2; cg2.vg = (20 * cg + q) * 48 + 40;
  if (w == 0) poll_ge<16>(flagB + (TT >> 1) * 256, 1u, lane);
  __builtin_amdgcn_s_barrier();
  MARK(15);
  asm volatile("s_nop 4");
  g2_hload<0>(s2_, cg2); g2_hload<1>(s2_, cg2); g2_hload<2>(s2_, cg2); g2_hload<3>(s2_, cg2);
  g2_step<0>(s2_, W2, cg2);
  asm volatile("s_waitcnt vmcnt(0)\n\ts_nop 7" : "+v"(s2_.xx[0]), "+v"(s2_.xx[1]), "+v"(s2_.xx[2]), "+v"(s2_.xx[3]), "+v"(s2_.xx[4]), "+v"(s2_.b2v[0]), "+v"(s2_.b2v[1]), "+v"(s2_.b2v[2]), "+v"(s2_.b2v[3]), "+v"(s2_.b2v[4]),
               "+v"(s2_.gg[0]), "+v"(s2_.gg[1]), "+v"(s2_.gg[2]), "+v"(s2_.gg[3]), "+v"(s2_.gg[4]));
#pragma unroll
  for (int d = 0; d < 5; d++) asm volatile("" : "+v"(s2_.acc[d][0]), "+v"(s2_.acc[d][1]), "+v"(s2_.acc[d][2]), "+v"(s2_.acc[d][3]));
  MARK(16);
  f32x4* redq = (f32x4*)smem;
  f32x4 own[5];
#define G2_OWN(T) { _Pragma("unroll") for (int d = 0; d < 5; d++) { own[d] = s2_.acc[d][T]; \
    _Pragma("unroll") for (int t = 0; t < 4; t++) if (t != T) redq[((T * 4 + t) * 5 + d) * 64 + lane] = s2_.acc[d][t]; } }
  if (w == 0) G2_OWN(0) else if (w == 1) G2_OWN(1) else if (w == 2) G2_OWN(2) else G2_OWN(3)
#undef G2_OWN
  lds_barrier();
  MARK(17);
  {
    const f32x4* r1 = redq + ((((w + 1) & 3) * 4 + w) * 5) * 64 + lane;
    const f32x4* r2 = redq + ((((w + 2) & 3) * 4 + w) * 5) * 64 + lane;
    const f32x4* r3 = redq + ((((w + 3) & 3) * 4 + w) * 5) * 64 + lane;
#pragma unroll
    for (int d = 0; d < 5; d++) {
      const f32x4 sacc = (own[d] + r1[d * 64]) + (r2[d * 64] + r3[d * 64]);
      u32x2 o;
#pragma unroll
      for (int h = 0; h < 2; h++) {
        const f32x2 y = rb2(rb2((f32x2){sacc[2 * h], sacc[2 * h + 1]}) + up(s2_.b2v[d][h]));
        o[h] = pk(up(s2_.xx[d][h]) + rb2(up(s2_.gg[d][h]) * y));
      }
      *(u32x2*)(out + (size_t)tok * D + 80 * cg + 16 * d + 4 * q) = o;
    }
  }
  MARK(18);
  MARK_FLUSH(ws, 1, 19);
  STAMP_END(ws, 1);
}

extern "C" void kernel_launch(void* const* d_in, const int* in_sizes, int n_in, void* d_out, int out_size, void* d_ws, size_t ws_size,
                              hipStream_t stream) {
  const u16* q = (const u16*)d_in[0]; const u16* k = (const u16*)d_in[1]; const u16* v = (const u16*)d_in[2];
  const u16* shk = (const u16*)d_in[3]; const u16* ssk = (const u16*)d_in[4]; const u16* ssD = (const u16*)d_in[5];
  const u16* w1 = (const u16*)d_in[7]; const u16* b1 = (const u16*)d_in[8]; const u16* w2 = (const u16*)d_in[9];
  const u16* b2 = (const u16*)d_in[10]; const u16* c = (const u16*)d_in[11]; const u16* wmod = (const u16*)d_in[12];
  char* ws = (char*)d_ws;
  (void)hipFuncSetAttribute((const void*)k_main, hipFuncAttributeMaxDynamicSharedMemorySize, L_TOTAL);
  k_prep<<<256, 640, 0, stream>>>(k, v, shk, ssk, ws);
  k_main<<<256, 256, L_TOTAL, stream>>>(q, ssk, ssD, c, wmod, w1, b1, w2, b2, ws, (u16*)d_out);
}
```

```cpp
#include <hip/hip_runtime.h>
#include <stdint.h>

typedef unsigned short u16;
typedef unsigned int u32;
typedef short s16x4 __attribute__((ext_vector_type(4)));
typedef short s16x8 __attribute__((ext_vector_type(8)));
typedef __bf16 bf16x8 __attribute__((ext_vector_type(8)));
typedef __bf16 bf16x2 __attribute__((ext_vector_type(2)));
typedef float f32x4 __attribute__((ext_vector_type(4)));
typedef float f32x2 __attribute__((ext_vector_type(2)));
typedef u32 u32x4 __attribute__((ext_vector_type(4)));
typedef u32 u32x2 __attribute__((ext_vector_type(2)));
#define LAS __attribute__((address_space(3)))
#define GAS __attribute__((address_space(1)))
#define DI __device__ __forceinline__

constexpr int S = 2048, D = 640, F = 1728;
constexpr size_t WS_MODP = 0;
constexpr size_t WS_FLAG = 8192;
constexpr size_t WS_FLAGA = WS_FLAG, WS_FLAGB = WS_FLAG + 16384, WS_GSYNC = WS_FLAG + 32768, WS_CNTW = WS_GSYNC + 2048, WS_FLAG_BYTES = 32768 + 3072;
constexpr size_t WS_U    = 65536;
constexpr size_t WS_Y2B  = WS_U + 2621440;
constexpr size_t WS_X1   = WS_Y2B + 2621440;
constexpr size_t WS_Y3F  = WS_X1 + 2621440;
constexpr size_t WS_HF   = WS_Y3F + 2621440;
constexpr size_t WS_W2F  = WS_HF + 7077888;

#define STAMP_BEGIN(ws)
#define STAMP_END(ws, kid)
#define MARK_DECL
#define MARK(i)
#define MARK_FLUSH(ws, kid, n)

DI float lo16(u32 x) { return __uint_as_float(x << 16); }
DI float hi16(u32 x) { return __uint_as_float(x & 0xffff0000u); }
DI u32 pk(f32x2 v) { return __builtin_bit_cast(u32, __builtin_convertvector(v, bf16x2)); }
DI u32 pk2(float a, float b) { return pk((f32x2){a, b}); }
DI f32x2 up(u32 x) { return (f32x2){lo16(x), hi16(x)}; }
DI f32x2 rb2(f32x2 v) { return up(pk(v)); }
DI float rbf(float a) { return lo16(pk2(a, a)); }
DI int rfl(int x) { return __builtin_amdgcn_readfirstlane(x); }
DI s16x4 trread(u32 ldsaddr) { return __builtin_amdgcn_ds_read_tr16_b64_v4i16((LAS s16x4*)(size_t)ldsaddr); }
DI bf16x8 cat8(s16x4 a, s16x4 b) { return __builtin_bit_cast(bf16x8, __builtin_shufflevector(a, b, 0, 1, 2, 3, 4, 5, 6, 7)); }
DI f32x4 mfma16(bf16x8 a, bf16x8 b, f32x4 c) { return __builtin_amdgcn_mfma_f32_16x16x32_bf16(a, b, c, 0, 0, 0); }
DI u32 ldsaddr(const void* p) { return (u32)(size_t)(LAS const char*)p; }
DI __amdgpu_buffer_rsrc_t mksrd(const void* p, u32 bytes) { return __builtin_amdgcn_make_buffer_rsrc((void*)p, 0, bytes, 0x00020000); }

[[maybe_unused]] DI float wsum64(float x) {
  x += __int_as_float(__builtin_amdgcn_update_dpp(0, __float_as_int(x), 0xB1, 0xf, 0xf, false));
  x += __int_as_float(__builtin_amdgcn_update_dpp(0, __float_as_int(x), 0x4E, 0xf, 0xf, false));
  x += __int_as_float(__builtin_amdgcn_update_dpp(0, __float_as_int(x), 0x141, 0xf, 0xf, false));
  x += __int_as_float(__builtin_amdgcn_update_dpp(0, __float_as_int(x), 0x128, 0xf, 0xf, false));
  { auto r = __builtin_amdgcn_permlane16_swap(__float_as_uint(x), __float_as_uint(x), false, false); x = __uint_as_float(r[0]) + __uint_as_float(r[1]); }
  { auto r = __builtin_amdgcn_permlane32_swap(__float_as_uint(x), __float_as_uint(x), false, false); x = __uint_as_float(r[0]) + __uint_as_float(r[1]); }
  return x;
}
DI void ln_finish(float sum, float sq, float& mean, float& rinv) {
  const float m = sum * (1.f / D);
  const float var = fmaxf(sq * (1.f / D) - m * m, 0.f);
  mean = rbf(m);
  rinv = __builtin_amdgcn_rcpf(rbf(__builtin_amdgcn_sqrtf(rbf(rbf(var) + 1e-5f))));
}

DI void dma4(u32 m0v, u32 voff, __amdgpu_buffer_rsrc_t srd, u32 soff) {
  asm volatile("s_mov_b32 m0, %0\n\ts_nop 0\n\tbuffer_load_dword %1, %2, %3 offen lds" ::"s"(m0v), "v"(voff), "s"(srd), "s"(soff) : "m0");
}
__global__ __launch_bounds__(640) void k_prep(const u16* __restrict__ k, const u16* __restrict__ v, const u16* __restrict__ shk,
                                              const u16* __restrict__ ssk, char* __restrict__ ws) {
  STAMP_BEGIN(ws)
  __shared__ __attribute__((aligned(16))) char sm[64 * 176 + 256];
  const int tid = threadIdx.x, lane = tid & 63, w = rfl(tid >> 6);
  const int b = blockIdx.x;
  const int xcd = b & 7, tt = b >> 3;
  const int mh = tid / 20, cq = tid - 20 * mh;
  const int t0 = 64 * tt + 2 * mh, ch0 = 80 * xcd + 4 * cq;
  u32x2 kr[5], vr[2];
#pragma unroll
  for (int i = 0; i < 5; i++) {
    const int t = t0 - 3 + i;
    u32x2 x = *(const u32x2*)(k + (size_t)(t < 0 ? 0 : t) * D + ch0);
    if (t < 0) x = (u32x2){0u, 0u};
    kr[i] = x;
  }
#pragma unroll
  for (int i = 0; i < 2; i++) vr[i] = *(const u32x2*)(v + (size_t)(t0 + i) * D + ch0);
  const u32x4 s0 = *(const u32x4*)(shk + ch0 * 4), s1 = *(const u32x4*)(shk + ch0 * 4 + 8);
  if (w < 3) {
    const __amdgpu_buffer_rsrc_t srd = mksrd(ssk + (size_t)80 * xcd * S + tt * 5120, 10240);
    dma4(rfl(ldsaddr(sm) + 64 * 176), tid * 64, srd, 0);
  }
  if (b < 35 && tid < 64) *(u32x4*)(ws + WS_FLAG + b * 1024 + tid * 16) = (u32x4){0u, 0u, 0u, 0u};
  const f32x2 sk01[4] = {{lo16(s0[0]), lo16(s0[2])}, {hi16(s0[0]), hi16(s0[2])}, {lo16(s0[1]), lo16(s0[3])}, {hi16(s0[1]), hi16(s0[3])}};
  const f32x2 sk23[4] = {{lo16(s1[0]), lo16(s1[2])}, {hi16(s1[0]), hi16(s1[2])}, {lo16(s1[1]), lo16(s1[3])}, {hi16(s1[1]), hi16(s1[3])}};
#pragma unroll
  for (int r = 0; r < 2; r++) {
    u32x2 o;
#pragma unroll
    for (int h = 0; h < 2; h++) {
      const f32x2* sk = h ? sk23 : sk01;
      f32x2 a = rb2(up(kr[r][h]) * sk[3]);
      a = rb2(a + rb2(up(kr[r + 1][h]) * sk[2]));
      a = rb2(a + rb2(up(kr[r + 2][h]) * sk[1]));
      a = rb2(a + rb2(up(kr[r + 3][h]) * sk[0]));
      o[h] = pk(a * up(vr[r][h]));
    }
    *(u32x2*)(sm + (2 * mh + r) * 176 + 8 * cq) = o;
  }
  __syncthreads();
  const int grp = lane >> 4, i16 = lane & 15, jj = i16 >> 2, p = i16 & 3;
  const int task = 4 * w + grp, hr = task / 5, cgrp = task - 5 * hr, rho = hr >> 1, half = hr & 1;
  const u32 base = ldsaddr(sm) + (4 * (8 * half + jj) + rho) * 176 + (16 * cgrp + 4 * p) * 2;
  const s16x4 a = trread(base), c = trread(base + 16 * 176);
  *(bf16x8*)(ws + WS_U + ((size_t)(80 * xcd + 16 * cgrp + i16) * 32 + tt) * 128 + rho * 32 + half * 16) = cat8(a, c);
  asm volatile("s_waitcnt vmcnt(0)" ::: "memory");
  STAMP_END(ws, 0);
}

template <int IMM> DI void bload(u32x4& dst, u32 voff, __amdgpu_buffer_rsrc_t srd, u32 soff) {
  asm volatile("buffer_load_dwordx4 %0, %1, %2, %3 offen offset:%c4" : "=v"(dst) : "v"(voff), "s"(srd), "s"(soff), "i"(IMM));
}
template <int IMM> DI void bload_sc1(u32x4& dst, u32 voff, __amdgpu_buffer_rsrc_t srd, u32 soff) {
  asm volatile("buffer_load_dwordx4 %0, %1, %2, %3 offen offset:%c4 sc1" : "=v"(dst) : "v"(voff), "s"(srd), "s"(soff), "i"(IMM));
}
template <int IMM> DI void bload_a(u32x4& dst, u32 voff, __amdgpu_buffer_rsrc_t srd, u32 soff) {
  asm volatile("buffer_load_dwordx4 %0, %1, %2, %3 offen offset:%c4" : "=a"(dst) : "v"(voff), "s"(srd), "s"(soff), "i"(IMM));
}
template <int IMM> DI void bload_a_sc1(u32x4& dst, u32 voff, __amdgpu_buffer_rsrc_t srd, u32 soff) {
  asm volatile("buffer_load_dwordx4 %0, %1, %2, %3 offen offset:%c4 sc1" : "=a"(dst) : "v"(voff), "s"(srd), "s"(soff), "i"(IMM));
}
DI void touch(u32& dst, u32 voff, __amdgpu_buffer_rsrc_t srd) {
  asm volatile("buffer_load_dword %0, %1, %2, 0 offen sc1" : "=v"(dst) : "v"(voff), "s"(srd));
}
template <int IMM> DI void bload2_sc1(u32x2& dst, u32 voff, __amdgpu_buffer_rsrc_t srd, u32 soff) {
  asm volatile("buffer_load_dwordx2 %0, %1, %2, %3 offen offset:%c4 sc1" : "=v"(dst) : "v"(voff), "s"(srd), "s"(soff), "i"(IMM));
}
template <int IMM> DI void bload2(u32x2& dst, u32 voff, __amdgpu_buffer_rsrc_t srd, u32 soff) {
  asm volatile("buffer_load_dwordx2 %0, %1, %2, %3 offen offset:%c4" : "=v"(dst) : "v"(voff), "s"(srd), "s"(soff), "i"(IMM));
}
DI void dma16(u32 m0v, u32 voff, __amdgpu_buffer_rsrc_t srd, u32 soff) {
  asm volatile("s_mov_b32 m0, %0\n\ts_nop 0\n\tbuffer_load_dwordx4 %1, %2, %3 offen lds" ::"s"(m0v), "v"(voff), "s"(srd), "s"(soff) : "m0");
}
template <int IMM> DI void dsread64(u32x2& dst, u32 addr) { asm volatile("ds_read_b64 %0, %1 offset:%c2" : "=v"(dst) : "v"(addr), "i"(IMM)); }
typedef u32x4 frag;
DI void mfma_vv(f32x4& acc, const frag& a, const frag& b) { asm volatile("v_mfma_f32_16x16x32_bf16 %0, %1, %2, %0" : "+v"(acc) : "v"(a), "v"(b)); }
DI void mfma_vv0(f32x4& acc, const frag& a, const frag& b) { asm volatile("v_mfma_f32_16x16x32_bf16 %0, %1, %2, 0" : "=&v"(acc) : "v"(a), "v"(b)); }
DI void mfma_av(f32x4& acc, const frag& a, const frag& b) { asm volatile("v_mfma_f32_16x16x32_bf16 %0, %1, %2, %0" : "+v"(acc) : "a"(a), "v"(b)); }
DI void mfma_av0(f32x4& acc, const frag& a, const frag& b) { asm volatile("v_mfma_f32_16x16x32_bf16 %0, %1, %2, 0" : "=&v"(acc) : "a"(a), "v"(b)); }
template <int N> DI void waitv() { asm volatile("s_waitcnt vmcnt(%c0)" ::"i"(N) : "memory"); }
template <int N> DI void waitv2(u32x4& a, u32x4& b) { asm volatile("s_waitcnt vmcnt(%c2)" : "+v"(a), "+v"(b) : "i"(N)); }
template <int N> DI void waitv4(u32x4& a, u32x4& b, u32x4& c, u32x4& d) { asm volatile("s_waitcnt vmcnt(%c4)" : "+v"(a), "+v"(b), "+v"(c), "+v"(d) : "i"(N)); }
DI frag catf(s16x4 a, s16x4 b) { return __builtin_bit_cast(frag, __builtin_shufflevector(a, b, 0, 1, 2, 3, 4, 5, 6, 7)); }
#define SB0 __builtin_amdgcn_sched_barrier(0)

constexpr int CV_RS = 1184;
constexpr int CV_KB = 4128;
constexpr int CV_WB = CV_KB + 4 * CV_RS;
template <int OFF> DI frag cv_ldB(u32 vb) { return catf(*(LAS const s16x4*)(size_t)(vb + OFF), *(volatile LAS const s16x4*)(size_t)(vb + OFF + 8)); }
template <int G> constexpr int cv_off0() { return 2 * (G >> 4) * CV_RS + 64 * (G & 15); }
template <int G> constexpr int cv_off1() { return (2 * (G >> 4) + 1) * CV_RS + 64 * (15 - (G & 15)); }
template <int G, int I> DI void cv_one(f32x4 (&acc)[4][8], const frag (&A)[16], const frag& B0, const frag& B1) {
  constexpr int R = 2 * (G >> 4), N = G & 15;
  constexpr int c0 = N <= 1 ? 0 : N / 2;
  constexpr int n0 = 8 - c0;
  if constexpr (I < n0) {
    constexpr int cb = c0 + I, s = 1 - N + 2 * cb;
    if constexpr (N == 0) mfma_vv0(acc[R][cb], A[s], B0); else mfma_vv(acc[R][cb], A[s], B0);
  } else {
    constexpr int c1 = (15 - N) / 2, cb = c1 + (I - n0), s = N - 14 + 2 * cb;
    static_assert(cb <= 7 && s >= 0 && s <= 15, "");
    if constexpr (s == 0) mfma_vv0(acc[R + 1][cb], A[s], B1); else mfma_vv(acc[R + 1][cb], A[s], B1);
  }
}
struct W1C { u32 vo[7]; u32 m0b, sob; __amdgpu_buffer_rsrc_t srd; };
template <int N, int R> DI void w1_m0(const W1C& c) { asm volatile("s_add_u32 m0, %0, %1" ::"s"(c.m0b), "i"(N * 4 * 7168 + R * 1024) : "scc", "m0"); }
template <int N, int R> DI void w1_ld(const W1C& c) {
  asm volatile("buffer_load_dwordx4 %0, %1, %2 offen lds" ::"v"(c.vo[R]), "s"(c.srd), "s"(c.sob + N * 4 * (32 * F * 2)));
}
template <int N, int R> DI void w1_piece(const W1C& c) { w1_m0<N, R>(c); asm volatile("s_nop 0"); w1_ld<N, R>(c); }
template <int N> DI void w1_step(const W1C& c) {
  w1_piece<N, 0>(c); w1_piece<N, 1>(c); w1_piece<N, 2>(c); w1_piece<N, 3>(c); w1_piece<N, 4>(c); w1_piece<N, 5>(c); w1_piece<N, 6>(c);
}
template <int G> DI void cv_steps(f32x4 (&acc)[4][8], const frag (&A)[16], u32 vb, frag P0, frag P1, frag Q0, frag Q1, const W1C& wc) {
  frag N0, N1;
  cv_one<G, 0>(acc, A, P0, P1); SB0;
  if constexpr (G + 2 < 32) N0 = cv_ldB<cv_off0<G + 2>()>(vb);
  SB0; cv_one<G, 1>(acc, A, P0, P1); cv_one<G, 2>(acc, A, P0, P1); SB0;
  if constexpr (G + 2 < 32) N1 = cv_ldB<cv_off1<G + 2>()>(vb);
  SB0; cv_one<G, 3>(acc, A, P0, P1); cv_one<G, 4>(acc, A, P0, P1);
  if constexpr (G < 28) w1_m0<G / 7, G % 7>(wc);
  cv_one<G, 5>(acc, A, P0, P1);
  if constexpr (G < 28) w1_ld<G / 7, G % 7>(wc);
  cv_one<G, 6>(acc, A, P0, P1); cv_one<G, 7>(acc, A, P0, P1); cv_one<G, 8>(acc, A, P0, P1);
  SB0;
  if constexpr (G + 1 < 32) cv_steps<G + 1>(acc, A, vb, Q0, Q1, N0, N1, wc);
}
constexpr int L_W1 = 0;
constexpr int L_Y = 143360;
constexpr int L_MOD = L_Y + 10240;
constexpr int L_RED = L_MOD + 8192;
constexpr int L_TOTAL = 163840;
constexpr int L_CONV = L_TOTAL - 4 * CV_WB;
static_assert(L_CONV >= 16 * 7168 && (L_CONV & 15) == 0 && L_RED + 1024 <= L_TOTAL, "");

template <int NL> DI void poll_ge(const u32* words, u32 want, int lane) {
  const GAS u32* p = (const GAS u32*)words + (lane & (NL - 1)) * 16;
  for (u32 spins = 0; spins < (1u << 18); spins++) {
    const u32 v = __hip_atomic_load(p, __ATOMIC_RELAXED, __HIP_MEMORY_SCOPE_AGENT);
    if (__all(v >= want)) break;
    __builtin_amdgcn_s_sleep(1);
  }
}
DI void lds_barrier() { asm volatile("s_waitcnt lgkmcnt(0)" ::: "memory"); __builtin_amdgcn_s_barrier(); asm volatile("" ::: "memory"); }

DI void phase_conv(const u16* __restrict__ ssk, const u16* __restrict__ ssD, char* __restrict__ ws, char* smem, int xcd, int j, int w, int lane, const W1C& wc
) {
  const int ch = 80 * xcd + 4 * j + w;
  char* kim = smem + L_CONV + w * CV_WB;
  char* uim = kim + CV_KB;
  u32x4 ru[4], rk[4];
#pragma unroll
  for (int i = 0; i < 4; i++) ru[i] = *(const u32x4*)(ws + WS_U + (size_t)ch * 4096 + (64 * i + lane) * 16);
#pragma unroll
  for (int i = 0; i < 4; i++) rk[i] = *(const u32x4*)(ssk + (size_t)ch * S + 8 * (64 * i + lane));
  float Dch = lo16(ssD[ch]);
  if (lane < 32) *(u32x4*)(uim + (lane >> 3) * CV_RS + 16 * (lane & 7)) = (u32x4){0u, 0u, 0u, 0u};
  if (lane < 2) *(u32x4*)(kim + 16 * lane) = (u32x4){0u, 0u, 0u, 0u};
#pragma unroll
  for (int i = 0; i < 4; i++) *(u32x4*)(kim + 32 + 16 * (64 * i + lane)) = rk[i];
  {
    char* ud = uim + ((lane & 7) >> 1) * CV_RS + 128 + 32 * (lane >> 3) + 16 * (lane & 1);
#pragma unroll
    for (int i = 0; i < 4; i++) *(u32x4*)(ud + 256 * i) = ru[i];
  }
  asm volatile("" : "+v"(Dch));
  MARK(0);
  const int q = lane >> 4, cc = lane & 15, jj = cc >> 2, p = cc & 3;
  const u32 va = ldsaddr(kim) + 224 - 64 * q - 8 * jj + 8 * p;
  const u32 vb = ldsaddr(uim) + 8 + 8 * cc + 16 * q;
  frag A[16];
#pragma unroll
  for (int s = 0; s < 16; s++) A[s] = catf(trread(va + 256 * s + 32), trread(va + 256 * s));
  f32x4 acc[4][8];
  frag P0, P1, Q0, Q1;
  P0 = cv_ldB<cv_off0<0>()>(vb); P1 = cv_ldB<cv_off1<0>()>(vb); Q0 = cv_ldB<cv_off0<1>()>(vb); Q1 = cv_ldB<cv_off1<1>()>(vb);
  MARK(1);
  cv_steps<0>(acc, A, vb, P0, P1, Q0, Q1, wc);
  asm volatile("s_nop 7\n\ts_nop 1");
#pragma unroll
  for (int r = 0; r < 4; r++)
#pragma unroll
    for (int cb = 0; cb < 8; cb += 4) asm volatile("" : "+v"(acc[r][cb]), "+v"(acc[r][cb + 1]), "+v"(acc[r][cb + 2]), "+v"(acc[r][cb + 3]));
  MARK(2);
  const int idx = 4 * (lane == 0 ? 63 : (lane >= 16 ? lane - 16 : lane + 47));
  const u32 ub = ldsaddr(uim) + 2 * (64 + 4 * cc + q);
  char* yl = kim + 32 * cc + 8 * q;
  const f32x2 DD = {Dch, Dch};
  float o[8][4], cy[8][3];
  u32 u01[8], u23[8];
#pragma unroll
  for (int cb = 0; cb < 8; cb++) {
    const LAS u16* up_ = (const LAS u16*)(size_t)(ub + 128 * cb);
    u01[cb] = (u32)up_[0] | ((u32)up_[CV_RS / 2] << 16);
    u23[cb] = (u32)up_[CV_RS] | ((u32)up_[3 * CV_RS / 2] << 16);
  }
#pragma unroll
  for (int cb = 0; cb < 8; cb++) {
    const f32x4 z0 = acc[0][cb], z1 = acc[1][cb], z2 = acc[2][cb], z3 = acc[3][cb];
    o[cb][0] = z0[0]; o[cb][1] = z0[1] + z1[0]; o[cb][2] = z0[2] + z1[1] + z2[0]; o[cb][3] = (z0[3] + z1[2]) + (z2[1] + z3[0]);
    cy[cb][0] = z1[3] + z2[2] + z3[1]; cy[cb][1] = z2[3] + z3[2]; cy[cb][2] = z3[3];
  }
#pragma unroll
  for (int cb = 0; cb < 8; cb++)
#pragma unroll
    for (int e = 0; e < 3; e++) {
      const float t = lane == 63 ? (cb ? cy[cb - 1][e] : 0.f) : cy[cb][e];
      o[cb][e] += __int_as_float(__builtin_amdgcn_ds_bpermute(idx, __float_as_int(t)));
    }
#pragma unroll
  for (int cb = 0; cb < 8; cb++) {
    const f32x2 y01 = rb2((f32x2){o[cb][0], o[cb][1]}) + rb2(up(u01[cb]) * DD), y23 = rb2((f32x2){o[cb][2], o[cb][3]}) + rb2(up(u23[cb]) * DD);
    *(u32x2*)(yl + 512 * cb) = (u32x2){pk(y01), pk(y23)};
  }
  lds_barrier();
  {
    const __amdgpu_buffer_rsrc_t sY = mksrd(ws + WS_Y2B, 2621440);
    const int c4 = (lane >> 1) & 3, tgl = 32 * w + (lane >> 3), hf = lane & 1;
    const char* src = smem + L_CONV + c4 * CV_WB + tgl * 32 + 16 * hf;
    const u32 yo = (tgl * D + 80 * xcd + 4 * j + c4) * 32 + 16 * hf;
    u32x4 v[4];
#pragma unroll
    for (int i = 0; i < 4; i++) v[i] = *(const u32x4*)(src + 256 * i);
#pragma unroll
    for (int i = 0; i < 4; i++) __builtin_amdgcn_raw_buffer_store_b128(v[i], sY, yo + i * (8 * D * 32), 0, 16);
  }
  MARK(3);
}

DI void phase_spare(const u16* __restrict__ c, const u16* __restrict__ wmod, const u16* __restrict__ w2,
                    char* __restrict__ ws, char* smem, int xcd, int j, int w, int tid, int lane) {
  const int r0 = (j - 20) * 8 + xcd;
  const __amdgpu_buffer_rsrc_t sWS = mksrd(ws, 65536);
  if (r0 < 60) {
    float* sc = (float*)(smem + L_Y);
    float* red = sc + 640;
    const int col0 = 64 * r0, cl = tid & 7, rg = tid >> 3;
    u32x4 r[20];
#pragma unroll
    for (int i = 0; i < 20; i++) r[i] = *(const u32x4*)(wmod + (size_t)(rg + 32 * i) * (6 * D) + col0 + 8 * cl);
    for (int i = tid; i < D; i += 256) { float x = lo16(c[i]); sc[i] = rbf(x / (1.f + __expf(-x))); }
    __syncthreads();
    f32x2 a[4] = {{0.f, 0.f}, {0.f, 0.f}, {0.f, 0.f}, {0.f, 0.f}};
#pragma unroll
    for (int i = 0; i < 20; i++) {
      const float s = sc[rg + 32 * i];
#pragma unroll
      for (int e = 0; e < 4; e++) a[e] += up(r[i][e]) * (f32x2){s, s};
    }
    float af[8] = {a[0][0], a[0][1], a[1][0], a[1][1], a[2][0], a[2][1], a[3][0], a[3][1]};
#pragma unroll
    for (int e = 0; e < 8; e++) af[e] += __int_as_float(__builtin_amdgcn_update_dpp(0, __float_as_int(af[e]), 0x128, 0xf, 0xf, false));
#pragma unroll
    for (int e = 0; e < 8; e++) { auto rr = __builtin_amdgcn_permlane16_swap(__float_as_uint(af[e]), __float_as_uint(af[e]), false, false); af[e] = __uint_as_float(rr[0]) + __uint_as_float(rr[1]); }
#pragma unroll
    for (int e = 0; e < 8; e++) { auto rr = __builtin_amdgcn_permlane32_swap(__float_as_uint(af[e]), __float_as_uint(af[e]), false, false); af[e] = __uint_as_float(rr[0]) + __uint_as_float(rr[1]); }
    if (lane < 8) { *(f32x4*)(red + w * 64 + 8 * lane) = (f32x4){af[0], af[1], af[2], af[3]}; *(f32x4*)(red + w * 64 + 8 * lane + 4) = (f32x4){af[4], af[5], af[6], af[7]}; }
    __syncthreads();
    if (tid < 32) {
      const int c0 = 2 * tid;
      float s0 = rbf((red[c0] + red[64 + c0]) + (red[128 + c0] + red[192 + c0]));
      float s1 = rbf((red[c0 + 1] + red[65 + c0]) + (red[129 + c0] + red[193 + c0]));
      const int jc = col0 + c0, vv = jc / D, ch = jc - vv * D;
      if (vv == 1 || vv == 4) { s0 += 1.f; s1 += 1.f; }
      const int slot = vv == 0 ? 1 : vv == 1 ? 0 : vv == 2 ? 2 : vv == 3 ? 4 : vv == 4 ? 3 : 5;
      __builtin_amdgcn_raw_buffer_store_b32(pk2(s0, s1), sWS, WS_MODP + ((ch >> 2) * 24 + slot * 4 + (ch & 3)) * 2, 0, 16);
    }
  } else {
    const int wv = (r0 - 60) * 4 + w;
    char* mb = smem + L_Y + w * (32 * 144);
    u32x4 r[4][4];
#pragma unroll
    for (int n = 0; n < 4; n++) {
      const int u = wv + 144 * n, uc = u < 540 ? u : 539, ks = uc / 10, cc = uc - 10 * ks;
#pragma unroll
      for (int i = 0; i < 4; i++) r[n][i] = *(const u32x4*)(w2 + (size_t)(32 * ks + 8 * i + (lane >> 3)) * D + 64 * cc + 8 * (lane & 7));
    }
    const int q = lane >> 4, i16 = lane & 15, jj = i16 >> 2, p = i16 & 3;
    const __amdgpu_buffer_rsrc_t sO = mksrd(ws + WS_W2F, 2211840);
    const u32 a0 = ldsaddr(mb) + (4 * q + jj) * 144 + 8 * p;
#pragma unroll
    for (int n = 0; n < 4; n++) {
      const int u = wv + 144 * n, uc = u < 540 ? u : 539, ks = uc / 10, cc = uc - 10 * ks;
#pragma unroll
      for (int i = 0; i < 4; i++) *(u32x4*)(mb + (8 * i + (lane >> 3)) * 144 + 16 * (lane & 7)) = r[n][i];
      frag v[4];
#pragma unroll
      for (int f = 0; f < 4; f++) v[f] = catf(trread(a0 + 32 * f), trread(a0 + 32 * f + 16 * 144));
#pragma unroll
      for (int f = 0; f < 4; f++) {
        const int ft = 4 * cc + f;
        if (u < 540) __builtin_amdgcn_raw_buffer_store_b128(v[f], sO, (((ft / 5) * 54 + ks) * 5 + (ft % 5)) * 1024 + lane * 16, 0, 16);
      }
    }
  }
}

DI float xsum8_16_32(float x) {
  x += __int_as_float(__builtin_amdgcn_update_dpp(0, __float_as_int(x), 0x128, 0xf, 0xf, false));
  { auto r = __builtin_amdgcn_permlane16_swap(__float_as_uint(x), __float_as_uint(x), false, false); x = __uint_as_float(r[0]) + __uint_as_float(r[1]); }
  { auto r = __builtin_amdgcn_permlane32_swap(__float_as_uint(x), __float_as_uint(x), false, false); x = __uint_as_float(r[0]) + __uint_as_float(r[1]); }
  return x;
}
DI void ln_stats(const f32x2 (&x)[10], f32x2* red, int w, int lane, float& mean, float& rinv) {
  f32x2 a = ((x[0] + x[1]) + (x[2] + x[3])) + ((x[4] + x[5]) + (x[6] + x[7])) + (x[8] + x[9]);
  f32x2 b0 = x[0] * x[0], b1 = x[1] * x[1];
#pragma unroll
  for (int h = 2; h < 10; h += 2) { b0 += x[h] * x[h]; b1 += x[h + 1] * x[h + 1]; }
  b0 += b1;
  const float s1 = xsum8_16_32(a[0] + a[1]), s2 = xsum8_16_32(b0[0] + b0[1]);
  if (lane < 8) red[w * 8 + lane] = (f32x2){s1, s2};
  lds_barrier();
  const int tk = lane & 7;
  const f32x2 t = (red[tk] + red[8 + tk]) + (red[16 + tk] + red[24 + tk]);
  ln_finish(t[0], t[1], mean, rinv);
}
DI f32x2 gelu2(f32x2 x) {
  const float c0 = -2.f * 0.7978845608028654f * 1.4426950408889634f, c1 = c0 * 0.044715f;
  const f32x2 z = x * (x * x * (f32x2){c1, c1} + (f32x2){c0, c0});
  const f32x2 d = (f32x2){__builtin_amdgcn_exp2f(z[0]), __builtin_amdgcn_exp2f(z[1])} + (f32x2){1.f, 1.f};
  return x * (f32x2){__builtin_amdgcn_rcpf(d[0]), __builtin_amdgcn_rcpf(d[1])};
}
DI int rotk(int n, int rot, int m) { int k = n + rot; return k >= m ? k - m : k; }
struct W2C { u32 l16; int rot2; __amdgpu_buffer_rsrc_t srd; };
template <int I> DI void w2_load1(frag (&W2)[60], const W2C& c) {
  const u32 vo = c.l16 + rotk(I / 5, c.rot2, 14) * 5120;
  if constexpr (I % 5 < 4) bload_a_sc1<(I % 5) * 1024>(W2[I], vo, c.srd, 0); else bload_a_sc1<0>(W2[I], vo + 4096, c.srd, 0);
}
template <int I, int N> DI void w2_loads(frag (&W2)[60], const W2C& c) {
  w2_load1<I>(W2, c);
  if constexpr (N > 1) w2_loads<I + 1, N - 1>(W2, c);
}
#define W2_M0V 8
#define W2_M1V 20
#define W2_G1V 20
constexpr int W2_M0 = W2_M0V, W2_M1 = W2_M1V, W2_G1 = W2_G1V;

constexpr int G1R = 6;
struct G1S {
  frag Y[G1R][2];
  frag A0[7], A1[7];
  f32x4 acc[7][2];
};
template <int FI> DI frag g1_lda(u32 ab) { return catf(trread(ab + 32 * FI), trread(ab + 32 * FI + 1792)); }
struct G1C { u32 b0, l16, ybase; int rot1; __amdgpu_buffer_rsrc_t srdY; };
template <int KS, int FI>
DI void g1_f(G1S& s, frag (&Ac)[7], frag (&An)[7], frag (&W2)[60], const G1C& c, const W2C& c2, u32 ab, u32 yo) {
  mfma_vv(s.acc[FI][0], Ac[FI], s.Y[KS % G1R][0]);
  SB0;
  if constexpr (KS < 19) An[FI] = g1_lda<FI>(ab);
  SB0;
  mfma_vv(s.acc[FI][1], Ac[FI], s.Y[KS % G1R][1]);
  if constexpr (KS >= 1 && KS - 1 + G1R < 20 && (FI == 1 || FI == 4)) bload_sc1<(FI == 4) * 1024>(s.Y[(KS - 1) % G1R][FI == 4], c.l16, c.srdY, yo);
  if constexpr (KS < W2_G1 && FI == 6) w2_load1<W2_M0 + W2_M1 + KS>(W2, c2);
  SB0;
  if constexpr (FI < 6) g1_f<KS, FI + 1>(s, Ac, An, W2, c, c2, ab, yo);
}
template <int KS> constexpr int g1_younger() {
  int cnt = 0; bool seen = false;
  for (int k = 0; k < G1R; k++) { if (seen) cnt += 2; if (k == KS) seen = true; }
  for (int n = 0; n < KS; n++) {
    if (n >= 1 && n - 1 + G1R < 20) { if (seen) cnt += 2; if (n - 1 + G1R == KS) seen = true; }
    if (n < W2_G1 && seen) cnt += 1;
  }
  return cnt;
}
template <int KS>
DI void g1_step(G1S& s, frag (&W2)[60], const G1C& c, const W2C& c2) {
  waitv2<g1_younger<KS>()>(s.Y[KS % G1R][0], s.Y[KS % G1R][1]);
  const u32 ab = c.b0 + rotk(KS + 1, c.rot1, 20) * 7168;
  const u32 yo = c.ybase + rotk(KS - 1 + G1R, c.rot1, 20) * 2048;
  if constexpr (KS & 1) g1_f<KS, 0>(s, s.A1, s.A0, W2, c, c2, ab, yo);
  else                  g1_f<KS, 0>(s, s.A0, s.A1, W2, c, c2, ab, yo);
  if constexpr (KS < 19) g1_step<KS + 1>(s, W2, c, c2);
}
struct G2S {
  frag H[4][4];
  frag WX[10];
  f32x4 acc[5][4];
  u32x2 xx[5], gg[5], b2v[5];
};
struct G2C { u32 l16, vx, vb2, vg; int rot2; __amdgpu_buffer_rsrc_t srdH, srdX, srdB2, srdMD; };
template <int N, int T> DI void g2_hload1(G2S& s, const G2C& c, u32 vo) { bload_sc1<T * 1024>(s.H[N & 3][T], vo, c.srdH, 0); }
template <int N> DI void g2_hload(G2S& s, const G2C& c) {
  const u32 vo = c.l16 + rotk(N, c.rot2, 14) * 4096;
  g2_hload1<N, 0>(s, c, vo); g2_hload1<N, 1>(s, c, vo); g2_hload1<N, 2>(s, c, vo); g2_hload1<N, 3>(s, c, vo);
}
template <int I> DI void g2_eload(G2S& s, const G2C& c) {
  if constexpr (I < 5) bload2_sc1<32 * I>(s.xx[I], c.vx, c.srdX, 0);
  else if constexpr (I < 10) bload2<32 * (I - 5)>(s.b2v[I - 5], c.vb2, c.srdB2, 0);
  else bload2_sc1<192 * (I - 10)>(s.gg[I - 10], c.vg, c.srdMD, 0);
}
template <int N, int M>
DI void g2_m(G2S& s, frag (&W2)[60], const G2C& c, u32 vo) {
  constexpr int d = M >> 2, t = M & 3;
  if constexpr (N == 0) mfma_av0(s.acc[d][t], W2[d], s.H[0][t]);
  else if constexpr (N < 12) mfma_av(s.acc[d][t], W2[N * 5 + d], s.H[N & 3][t]);
  else mfma_vv(s.acc[d][t], s.WX[(N - 12) * 5 + d], s.H[N & 3][t]);
  if constexpr (N + 3 < 14 && (M == 1 || M == 6 || M == 11 || M == 16)) g2_hload1<N + 3, (M - 1) / 5>(s, c, vo);
  if constexpr (N >= 11 && (M == 1 || M == 5 || M == 9 || M == 13 || M == 17)) g2_eload<5 * (N - 11) + (M - 1) / 4>(s, c);
  if constexpr (M < 19) g2_m<N, M + 1>(s, W2, c, vo);
}
template <int N> constexpr int g2_younger() {
  int cnt = 0; bool seen = false;
  for (int h = 0; h < 3; h++) { if (seen) cnt += 4; if (h == N) seen = true; }
  for (int n = 0; n < N; n++) {
    if (n + 3 < 14) { if (seen) cnt += 4; if (n + 3 == N) seen = true; }
    if (n >= 11 && seen) cnt += 5;
  }
  return cnt;
}
static_assert(g2_younger<0>() == 8 && g2_younger<3>() == 8 && g2_younger<11>() == 8 && g2_younger<12>() == 9 && g2_younger<13>() == 10, "");
template <int N>
DI void g2_step(G2S& s, frag (&W2)[60], const G2C& c) {
  waitv4<g2_younger<N>()>(s.H[N & 3][0], s.H[N & 3][1], s.H[N & 3][2], s.H[N & 3][3]);
  const u32 vo = c.l16 + rotk(N + 3, c.rot2, 14) * 4096;
  g2_m<N, 0>(s, W2, c, vo);
  if constexpr (N < 13) g2_step<N + 1>(s, W2, c);
}

__global__ __launch_bounds__(256, 1) void k_main(const u16* __restrict__ qin, const u16* __restrict__ ssk, const u16* __restrict__ ssD,
                                                 const u16* __restrict__ cin, const u16* __restrict__ wmod, const u16* __restrict__ w1,
                                                 const u16* __restrict__ b1, const u16* __restrict__ w2, const u16* __restrict__ b2,
                                                 char* __restrict__ ws, u16* __restrict__ out) {
  STAMP_BEGIN(ws)
  MARK_DECL
  extern __shared__ __attribute__((aligned(16))) char smem[];
  const int tid = threadIdx.x, lane = tid & 63, w = rfl(tid >> 6);
  const int b = blockIdx.x, xcd = b & 7, j = b >> 3;
  const int tile = 4 * (xcd >> 1) + (j >> 3), g = 8 * (xcd & 1) + (j & 7);
  const int TT = 4 * xcd + (j >> 3), cg = j & 7;
  const u32 l16 = lane * 16, lds0 = ldsaddr(smem);
  W1C wc;
#pragma unroll
  for (int r = 0; r < 7; r++) { const u32 idx = 64 * r + lane, row = (idx * 4682u) >> 16; wc.vo[r] = row * (F * 2 - 224) + idx * 16; }
  wc.m0b = lds0 + L_W1 + w * 7168; wc.sob = w * (32 * F * 2) + 224 * g; wc.srd = mksrd(w1, D * F * 2);
  const int tg = 16 * tile + g, t0 = 8 * tg;
  const int tk = lane & 7, slot = lane >> 3, q = lane >> 4;
  u32x2 xr[5];
  {
    const __amdgpu_buffer_rsrc_t srdQ = mksrd(qin + (size_t)t0 * D, 10240);
    const u32 vq = tk * 1280 + (40 * w + slot) * 8;
    asm volatile("s_nop 4");
    bload2<0>(xr[0], vq, srdQ, 0); bload2<64>(xr[1], vq, srdQ, 0); bload2<128>(xr[2], vq, srdQ, 0); bload2<192>(xr[3], vq, srdQ, 0); bload2<256>(xr[4], vq, srdQ, 0);
  }
  if (j < 20) {
    phase_conv(ssk, ssD, ws, smem, xcd, j, w, lane, wc
    );
  } else {
    phase_spare(cin, wmod, w2, ws, smem, xcd, j, w, tid, lane);
  }
  asm volatile("s_waitcnt vmcnt(0)" : "+v"(xr[0]), "+v"(xr[1]), "+v"(xr[2]), "+v"(xr[3]), "+v"(xr[4]) :: "memory");
  __builtin_amdgcn_s_barrier();
  MARK(4);
  u32* gsync = (u32*)(ws + WS_GSYNC);
  u32* cntw = (u32*)(ws + WS_CNTW);
  if (tid < 4) {
    __hip_atomic_fetch_add((GAS u32*)gsync + (tid * 8 + xcd) * 16, 1u, __ATOMIC_RELAXED, __HIP_MEMORY_SCOPE_AGENT);
    if ((j - 20) * 8 + xcd >= 60) __hip_atomic_fetch_add((GAS u32*)cntw + tid * 16, 1u, __ATOMIC_RELAXED, __HIP_MEMORY_SCOPE_AGENT);
  }
  const int ks0 = w < 2 ? 14 * w : 28 + 13 * (w - 2), nks = w < 2 ? 14 : 13;
  W2C c2;
  c2.l16 = l16; c2.srd = mksrd(ws + WS_W2F + (size_t)cg * 276480 + ks0 * 5120, nks * 5120);
  { int r2 = 2 * cg + 4 * (j >> 3); r2 = r2 >= 14 ? r2 - 14 : r2; c2.rot2 = r2 >= 14 ? r2 - 14 : r2; }
  frag W2[60];
  f32x2 x[10], t[10];
  f32x2* red = (f32x2*)(smem + L_RED);
  {
    if (j >= 20) { w1_step<0>(wc); w1_step<1>(wc); }
#pragma unroll
    for (int i = 0; i < 5; i++) { x[2 * i] = up(xr[i][0]); x[2 * i + 1] = up(xr[i][1]); }
    if (w == 0) poll_ge<1>(cntw + (j & 3) * 16, 36u, lane);
    float mean, rinv;
    ln_stats(x, red + 64, w, lane, mean, rinv);
    const f32x2 mm = {mean, mean}, rr = {rinv, rinv};
#pragma unroll
    for (int h = 0; h < 10; h++) t[h] = rb2(x[h] - mm);
#pragma unroll
    for (int h = 0; h < 10; h++) t[h] = rb2(t[h] * rr);
#pragma unroll
    for (int h = 0; h < 10; h++) asm volatile("" : "+v"(x[h]), "+v"(t[h]));
  }
  if (j >= 20) { w1_step<2>(wc); w1_step<3>(wc); }
  w2_loads<0, W2_M0>(W2, c2);
  if (w == 0) poll_ge<8>(gsync + (j & 3) * 128, 32u, lane);
  __builtin_amdgcn_s_barrier();
  MARK(5);
  const __amdgpu_buffer_rsrc_t srdMD = mksrd(ws + WS_MODP, 7680);
  u32x2 bb[7];
  {
    const __amdgpu_buffer_rsrc_t srdYB = mksrd(ws + WS_Y2B + (size_t)(tg >> 1) * 20480, 20480);
    const __amdgpu_buffer_rsrc_t srdB1 = mksrd(b1, F * 2);
    u32x4 yr[3], mr[2];
    const u32 v16 = tid * 16, vb = (16 * 7 * g + 4 * q) * 2;
    asm volatile("s_nop 4");
    MARK(20);
    const u32 vy = tid * 32 + 16 * (tg & 1);
    bload_sc1<0>(yr[0], vy, srdYB, 0); bload_sc1<0>(yr[1], vy + 8192, srdYB, 0); bload_sc1<0>(yr[2], vy + 16384, srdYB, 0);
    bload_sc1<0>(mr[0], v16, srdMD, 0); bload_sc1<0>(mr[1], v16 + 4096, srdMD, 0);
    w1_step<4>(wc);
    asm volatile("s_waitcnt vmcnt(7)" : "+v"(yr[0]), "+v"(yr[1]), "+v"(yr[2]), "+v"(mr[0]), "+v"(mr[1]));
    MARK(19);
    *(u32x4*)(smem + L_Y + v16) = yr[0]; *(u32x4*)(smem + L_Y + 4096 + v16) = yr[1];
    if (tid < 128) *(u32x4*)(smem + L_Y + 8192 + v16) = yr[2];
    *(u32x4*)(smem + L_MOD + v16) = mr[0]; *(u32x4*)(smem + L_MOD + 4096 + v16) = mr[1];
    lds_barrier();
    MARK(6);
    const int i16 = lane & 15, jj = i16 >> 2, p = i16 & 3;
    const u32 ya = lds0 + L_Y + (4 * (40 * w + 2 * q + (p >> 1)) + jj) * 16 + 8 * (p & 1);
    const char* mdp = smem + L_MOD + (40 * w + slot) * 48;
    u32 yy[10], w1v[10], sav[10], gav[10];
#pragma unroll
    for (int i = 0; i < 5; i++) {
      const u32x2 yu = __builtin_bit_cast(u32x2, trread(ya + i * 8 * 64));
      const u32x4 m0 = *(const u32x4*)(mdp + i * 384);
      const u32x2 m1 = *(const u32x2*)(mdp + i * 384 + 16);
      yy[2 * i] = yu[0]; yy[2 * i + 1] = yu[1]; w1v[2 * i] = m0[0]; w1v[2 * i + 1] = m0[1]; sav[2 * i] = m0[2]; sav[2 * i + 1] = m0[3]; gav[2 * i] = m1[0]; gav[2 * i + 1] = m1[1];
    }
    u32 o[10];
#define STAGE(EXPR) _Pragma("unroll") for (int h = 0; h < 10; h++) { EXPR; }
    STAGE(t[h] = rb2(t[h] * up(w1v[h])))
    STAGE(t[h] = rb2(t[h] + up(sav[h])))
    STAGE(t[h] = rb2(t[h] * up(yy[h])))
    STAGE(t[h] = rb2(up(gav[h]) * t[h]))
    STAGE(o[h] = pk(x[h] + t[h]))
    const __amdgpu_buffer_rsrc_t srdX1 = mksrd(ws + WS_X1, 2621440);
    const u32 xo = (t0 + tk) * 1280 + (40 * w + slot) * 8;
#pragma unroll
    for (int i = 0; i < 5; i++) __builtin_amdgcn_raw_buffer_store_b64((u32x2){o[2 * i], o[2 * i + 1]}, srdX1, xo + i * 64, 0, 16);
    STAGE(x[h] = up(o[h]))
    MARK(7);
    float mean, rinv;
    SB0;
    ln_stats(x, red, w, lane, mean, rinv);
    {
      const f32x2 m2 = {mean, mean}, r2 = {rinv, rinv};
#pragma unroll
      for (int i = 0; i < 5; i++) {
        const u32x4 m0 = *(const u32x4*)(mdp + i * 384 + 16);
        const u32x2 m1 = *(const u32x2*)(mdp + i * 384 + 32);
        x[2 * i] = (x[2 * i] - m2) * r2 * up(m0[2]) + up(m1[0]);
        x[2 * i + 1] = (x[2 * i + 1] - m2) * r2 * up(m0[3]) + up(m1[1]);
      }
    }
    MARK(8);
    SB0;
    {
      bload2<0>(bb[0], vb, srdB1, 0); bload2<32>(bb[1], vb, srdB1, 0); bload2<64>(bb[2], vb, srdB1, 0); bload2<96>(bb[3], vb, srdB1, 0);
      bload2<128>(bb[4], vb, srdB1, 0); bload2<160>(bb[5], vb, srdB1, 0); bload2<192>(bb[6], vb, srdB1, 0);
    }
    SB0;
    ln_stats(x, red + 32, w, lane, mean, rinv);
    {
      const f32x2 m3 = {mean, mean}, r3 = {rinv, rinv};
      STAGE(o[h] = pk((x[h] - m3) * r3))
      const int rt = tg >> 1, r = 8 * (tg & 1) + tk;
      const __amdgpu_buffer_rsrc_t srdY3 = mksrd(ws + WS_Y3F, 2621440);
      const u32 yo = (rt >> 1) * 40960 + (rt & 1) * 1024 + ((2 * (slot >> 2) + (slot & 1)) * 16 + r) * 16 + 8 * ((slot >> 1) & 1) + 5 * w * 2048;
#pragma unroll
      for (int i = 0; i < 5; i++) __builtin_amdgcn_raw_buffer_store_b64((u32x2){o[2 * i], o[2 * i + 1]}, srdY3, yo + i * 2048, 0, 16);
    }
#undef STAGE
  }
  MARK(9);
  asm volatile("s_waitcnt vmcnt(0)" : "+v"(bb[0]), "+v"(bb[1]), "+v"(bb[2]), "+v"(bb[3]), "+v"(bb[4]), "+v"(bb[5]), "+v"(bb[6]) :: "memory");
  __builtin_amdgcn_s_barrier();
  u32* flagA = (u32*)(ws + WS_FLAGA) + tile * 256;
  if (tid == 0) __hip_atomic_store((GAS u32*)flagA + g * 16, 1u, __ATOMIC_RELAXED, __HIP_MEMORY_SCOPE_AGENT);
  MARK(10);
  w2_loads<W2_M0, W2_M1>(W2, c2);
  if (w == 0) poll_ge<16>(flagA, 1u, lane);
  __builtin_amdgcn_s_barrier();
  MARK(11);
  f32x4 hacc[7][2];
  {
    G1C c1;
    const int i16 = lane & 15, jj = i16 >> 2, p = i16 & 3;
    c1.srdY = mksrd(ws + WS_Y3F, 2621440); c1.l16 = l16;
    c1.rot1 = (5 * (j & 7)) >> 1;
    c1.ybase = (tile * 4 + w) * 40960;
    c1.b0 = lds0 + L_W1 + (16 * (q >> 1) + 4 * (q & 1) + jj) * 224 + 8 * p;
    G1S s;
    asm volatile("s_nop 4");
#pragma unroll
    for (int k = 0; k < G1R; k++) { const u32 yo = c1.ybase + rotk(k, c1.rot1, 20) * 2048; bload_sc1<0>(s.Y[k][0], l16, c1.srdY, yo); bload_sc1<1024>(s.Y[k][1], l16, c1.srdY, yo); }
#pragma unroll
    for (int f = 0; f < 7; f++) {
      const f32x4 bv = {lo16(bb[f][0]), hi16(bb[f][0]), lo16(bb[f][1]), hi16(bb[f][1])};
      s.acc[f][0] = bv; s.acc[f][1] = bv;
      asm volatile("" : "+v"(s.acc[f][0]), "+v"(s.acc[f][1]));
    }
    const u32 ab0 = c1.b0 + c1.rot1 * 7168;
    s.A0[0] = g1_lda<0>(ab0); s.A0[1] = g1_lda<1>(ab0); s.A0[2] = g1_lda<2>(ab0); s.A0[3] = g1_lda<3>(ab0);
    s.A0[4] = g1_lda<4>(ab0); s.A0[5] = g1_lda<5>(ab0); s.A0[6] = g1_lda<6>(ab0);
    SB0;
    g1_step<0>(s, W2, c1, c2);
    asm volatile("s_nop 7\n\ts_nop 1");
#pragma unroll
    for (int f = 0; f < 7; f++) { asm volatile("" : "+v"(s.acc[f][0]), "+v"(s.acc[f][1])); hacc[f][0] = s.acc[f][0]; hacc[f][1] = s.acc[f][1]; }
  }
  MARK(12);
  G2S s2_;
  {
    const __amdgpu_buffer_rsrc_t srdH = mksrd(ws + WS_HF, 7077888);
    const int rt0 = 8 * tile + 2 * w;
    const u32 ho = ((rt0 >> 2) * 54 * 4 + (rt0 & 3)) * 1024 + l16 + ((7 * g) >> 1) * 4096;
    auto gel = [&](int f, int tt) -> u32x2 {
      const f32x4 a = hacc[f][tt];
      return (u32x2){pk(gelu2((f32x2){a[0], a[1]})), pk(gelu2((f32x2){a[2], a[3]}))};
    };
    if (g & 1) {
      { const u32x2 a = gel(0, 0), c = gel(0, 1);
        __builtin_amdgcn_raw_buffer_store_b64(a, srdH, ho + 8, 0, 16); __builtin_amdgcn_raw_buffer_store_b64(c, srdH, ho + 1024 + 8, 0, 16); }
#pragma unroll
      for (int i = 0; i < 3; i++) {
        const u32x2 a0 = gel(2 * i + 1, 0), a1 = gel(2 * i + 2, 0), c0 = gel(2 * i + 1, 1), c1 = gel(2 * i + 2, 1);
        if (7 * g + 2 * i + 2 < 108) {
          __builtin_amdgcn_raw_buffer_store_b128((u32x4){a0[0], a0[1], a1[0], a1[1]}, srdH, ho + (1 + i) * 4096, 0, 16);
          __builtin_amdgcn_raw_buffer_store_b128((u32x4){c0[0], c0[1], c1[0], c1[1]}, srdH, ho + (1 + i) * 4096 + 1024, 0, 16);
        }
      }
    } else {
#pragma unroll
      for (int i = 0; i < 3; i++) {
        const u32x2 a0 = gel(2 * i, 0), a1 = gel(2 * i + 1, 0), c0 = gel(2 * i, 1), c1 = gel(2 * i + 1, 1);
        __builtin_amdgcn_raw_buffer_store_b128((u32x4){a0[0], a0[1], a1[0], a1[1]}, srdH, ho + i * 4096, 0, 16);
        __builtin_amdgcn_raw_buffer_store_b128((u32x4){c0[0], c0[1], c1[0], c1[1]}, srdH, ho + i * 4096 + 1024, 0, 16);
      }
      { const u32x2 a = gel(6, 0), c = gel(6, 1);
        __builtin_amdgcn_raw_buffer_store_b64(a, srdH, ho + 3 * 4096, 0, 16); __builtin_amdgcn_raw_buffer_store_b64(c, srdH, ho + 3 * 4096 + 1024, 0, 16); }
    }
  }
  MARK(13);
  waitv<0>();
  __builtin_amdgcn_s_barrier();
  u32* flagB = (u32*)(ws + WS_FLAGB);
  if (tid == 0) __hip_atomic_store((GAS u32*)flagB + tile * 256 + g * 16, 1u, __ATOMIC_RELAXED, __HIP_MEMORY_SCOPE_AGENT);
  {
    const u32 vo = l16 + rotk(12, c2.rot2, 14) * 5120, v1 = l16 + rotk(13, c2.rot2, 14) * 5120;
    if constexpr (W2_M0 + W2_M1 + W2_G1 < 60) w2_loads<W2_M0 + W2_M1 + W2_G1, 60 - W2_M0 - W2_M1 - W2_G1>(W2, c2);
    bload_sc1<0>(s2_.WX[0], vo, c2.srd, 0); bload_sc1<1024>(s2_.WX[1], vo, c2.srd, 0); bload_sc1<2048>(s2_.WX[2], vo, c2.srd, 0); bload_sc1<3072>(s2_.WX[3], vo, c2.srd, 0);
    bload_sc1<0>(s2_.WX[4], vo + 4096, c2.srd, 0);
    bload_sc1<0>(s2_.WX[5], v1, c2.srd, 0); bload_sc1<1024>(s2_.WX[6], v1, c2.srd, 0); bload_sc1<2048>(s2_.WX[7], v1, c2.srd, 0); bload_sc1<3072>(s2_.WX[8], v1, c2.srd, 0);
    bload_sc1<0>(s2_.WX[9], v1 + 4096, c2.srd, 0);
  }
  MARK(14);
  const int cc = lane & 15, tok = 64 * TT + 16 * w + cc;
  G2C cg2;
  cg2.l16 = l16; cg2.rot2 = c2.rot2;
  cg2.srdH = mksrd(ws + WS_HF + (size_t)TT * 221184 + ks0 * 4096, nks * 4096);
  cg2.srdX = mksrd(ws + WS_X1, 2621440); cg2.srdB2 = mksrd(b2, D * 2); cg2.srdMD = srdMD;
  cg2.vx = tok * 1280 + (80 * cg + 4 * q) * 2; cg2.vb2 = (80 * cg + 4 * q) * 2; cg2.vg = (20 * cg + q) * 48 + 40;
  if (w == 0) poll_ge<16>(flagB + (TT >> 1) * 256, 1u, lane);
  __builtin_amdgcn_s_barrier();
  MARK(15);
  asm volatile("s_nop 4");
  g2_hload<0>(s2_, cg2); g2_hload<1>(s2_, cg2); g2_hload<2>(s2_, cg2);
  asm volatile("s_waitcnt vmcnt(12)" : "+v"(s2_.WX[0]), "+v"(s2_.WX[1]), "+v"(s2_.WX[2]), "+v"(s2_.WX[3]), "+v"(s2_.WX[4]), "+v"(s2_.WX[5]), "+v"(s2_.WX[6]), "+v"(s2_.WX[7]), "+v"(s2_.WX[8]), "+v"(s2_.WX[9]));
  MARK(21);
  g2_step<0>(s2_, W2, cg2);
  asm volatile("s_nop 7\n\ts_nop 1");
#pragma unroll
  for (int d = 0; d < 5; d++) asm volatile("" : "+v"(s2_.acc[d][0]), "+v"(s2_.acc[d][1]), "+v"(s2_.acc[d][2]), "+v"(s2_.acc[d][3]));
  MARK(16);
  f32x4* redq = (f32x4*)smem;
  f32x4 own[5];
#define G2_OWN(T) { _Pragma("unroll") for (int d = 0; d < 5; d++) { own[d] = s2_.acc[d][T]; \
    _Pragma("unroll") for (int t = 0; t < 4; t++) if (t != T) redq[((T * 4 + t) * 5 + d) * 64 + lane] = s2_.acc[d][t]; } }
  if (w == 0) G2_OWN(0) else if (w == 1) G2_OWN(1) else if (w == 2) G2_OWN(2) else G2_OWN(3)
#undef G2_OWN
  lds_barrier();
  asm volatile("s_waitcnt vmcnt(0)" : "+v"(s2_.xx[0]), "+v"(s2_.xx[1]), "+v"(s2_.xx[2]), "+v"(s2_.xx[3]), "+v"(s2_.xx[4]), "+v"(s2_.b2v[0]), "+v"(s2_.b2v[1]), "+v"(s2_.b2v[2]), "+v"(s2_.b2v[3]), "+v"(s2_.b2v[4]),
               "+v"(s2_.gg[0]), "+v"(s2_.gg[1]), "+v"(s2_.gg[2]), "+v"(s2_.gg[3]), "+v"(s2_.gg[4]));
  MARK(17);
  {
    const f32x4* r1 = redq + ((((w + 1) & 3) * 4 + w) * 5) * 64 + lane;
    const f32x4* r2 = redq + ((((w + 2) & 3) * 4 + w) * 5) * 64 + lane;
    const f32x4* r3 = redq + ((((w + 3) & 3) * 4 + w) * 5) * 64 + lane;
#pragma unroll
    for (int d = 0; d < 5; d++) {
      const f32x4 sacc = (own[d] + r1[d * 64]) + (r2[d * 64] + r3[d * 64]);
      u32x2 o;
#pragma unroll
      for (int h = 0; h < 2; h++) {
        const f32x2 y = rb2(rb2((f32x2){sacc[2 * h], sacc[2 * h + 1]}) + up(s2_.b2v[d][h]));
        o[h] = pk(up(s2_.xx[d][h]) + rb2(up(s2_.gg[d][h]) * y));
      }
      *(u32x2*)(out + (size_t)tok * D + 80 * cg + 16 * d + 4 * q) = o;
    }
  }
  MARK(18);
  MARK_FLUSH(ws, 1, 22);
  STAMP_END(ws, 1);
}

extern "C" void kernel_launch(void* const* d_in, const int* in_sizes, int n_in, void* d_out, int out_size, void* d_ws, size_t ws_size,
                              hipStream_t stream) {
  const u16* q = (const u16*)d_in[0]; const u16* k = (const u16*)d_in[1]; const u16* v = (const u16*)d_in[2];
  const u16* shk = (const u16*)d_in[3]; const u16* ssk = (const u16*)d_in[4]; const u16* ssD = (const u16*)d_in[5];
  const u16* w1 = (const u16*)d_in[7]; const u16* b1 = (const u16*)d_in[8]; const u16* w2 = (const u16*)d_in[9];
  const u16* b2 = (const u16*)d_in[10]; const u16* c = (const u16*)d_in[11]; const u16* wmod = (const u16*)d_in[12];
  char* ws = (char*)d_ws;
  (void)hipFuncSetAttribute((const void*)k_main, hipFuncAttributeMaxDynamicSharedMemorySize, L_TOTAL);
  k_prep<<<256, 640, 0, stream>>>(k, v, shk, ssk, ws);
  k_main<<<256, 256, L_TOTAL, stream>>>(q, ssk, ssD, c, wmod, w1, b1, w2, b2, ws, (u16*)d_out);
}
```

```cpp
#include <hip/hip_runtime.h>
#include <stdint.h>

typedef unsigned short u16;
typedef unsigned int u32;
typedef short s16x4 __attribute__((ext_vector_type(4)));
typedef short s16x8 __attribute__((ext_vector_type(8)));
typedef __bf16 bf16x8 __attribute__((ext_vector_type(8)));
typedef __bf16 bf16x2 __attribute__((ext_vector_type(2)));
typedef float f32x4 __attribute__((ext_vector_type(4)));
typedef float f32x2 __attribute__((ext_vector_type(2)));
typedef u32 u32x4 __attribute__((ext_vector_type(4)));
typedef u32 u32x2 __attribute__((ext_vector_type(2)));
#define LAS __attribute__((address_space(3)))
#define GAS __attribute__((address_space(1)))
#define DI __device__ __forceinline__

constexpr int S = 2048, D = 640, F = 1728;
constexpr size_t WS_MODP = 0;
constexpr size_t WS_EPOCH = 8192;
constexpr size_t WS_FU = 16384;
constexpr size_t WS_FA = WS_FU + 16384;
constexpr size_t WS_FB = WS_FA + 16384;
constexpr size_t WS_FG = WS_FB + 16384;
constexpr size_t WS_FW = WS_FG + 16384;
constexpr size_t WS_U    = 131072;
constexpr size_t WS_Y2B  = WS_U + 2621440;
constexpr size_t WS_X1   = WS_Y2B + 2621440;
constexpr size_t WS_Y3F  = WS_X1 + 2621440;
constexpr size_t WS_HF   = WS_Y3F + 2621440;
constexpr size_t WS_W2F  = WS_HF + 7077888;

#define STAMP_BEGIN(ws)
#define STAMP_END(ws, kid)
#define MARK_DECL
#define MARK(i)
#define MARK_FLUSH(ws, kid, n)

DI float lo16(u32 x) { return __uint_as_float(x << 16); }
DI float hi16(u32 x) { return __uint_as_float(x & 0xffff0000u); }
DI u32 pk(f32x2 v) { return __builtin_bit_cast(u32, __builtin_convertvector(v, bf16x2)); }
DI u32 pk2(float a, float b) { return pk((f32x2){a, b}); }
DI f32x2 up(u32 x) { return (f32x2){lo16(x), hi16(x)}; }
DI f32x2 rb2(f32x2 v) { return up(pk(v)); }
DI float rbf(float a) { return lo16(pk2(a, a)); }
DI int rfl(int x) { return __builtin_amdgcn_readfirstlane(x); }
DI s16x4 trread(u32 ldsaddr) { return __builtin_amdgcn_ds_read_tr16_b64_v4i16((LAS s16x4*)(size_t)ldsaddr); }
DI bf16x8 cat8(s16x4 a, s16x4 b) { return __builtin_bit_cast(bf16x8, __builtin_shufflevector(a, b, 0, 1, 2, 3, 4, 5, 6, 7)); }
DI f32x4 mfma16(bf16x8 a, bf16x8 b, f32x4 c) { return __builtin_amdgcn_mfma_f32_16x16x32_bf16(a, b, c, 0, 0, 0); }
DI u32 ldsaddr(const void* p) { return (u32)(size_t)(LAS const char*)p; }
DI __amdgpu_buffer_rsrc_t mksrd(const void* p, u32 bytes) { return __builtin_amdgcn_make_buffer_rsrc((void*)p, 0, bytes, 0x00020000); }

[[maybe_unused]] DI float wsum64(float x) {
  x += __int_as_float(__builtin_amdgcn_update_dpp(0, __float_as_int(x), 0xB1, 0xf, 0xf, false));
  x += __int_as_float(__builtin_amdgcn_update_dpp(0, __float_as_int(x), 0x4E, 0xf, 0xf, false));
  x += __int_as_float(__builtin_amdgcn_update_dpp(0, __float_as_int(x), 0x141, 0xf, 0xf, false));
  x += __int_as_float(__builtin_amdgcn_update_dpp(0, __float_as_int(x), 0x128, 0xf, 0xf, false));
  { auto r = __builtin_amdgcn_permlane16_swap(__float_as_uint(x), __float_as_uint(x), false, false); x = __uint_as_float(r[0]) + __uint_as_float(r[1]); }
  { auto r = __builtin_amdgcn_permlane32_swap(__float_as_uint(x), __float_as_uint(x), false, false); x = __uint_as_float(r[0]) + __uint_as_float(r[1]); }
  return x;
}
DI void ln_finish(float sum, float sq, float& mean, float& rinv) {
  const float m = sum * (1.f / D);
  const float var = fmaxf(sq * (1.f / D) - m * m, 0.f);
  mean = rbf(m);
  rinv = __builtin_amdgcn_rcpf(rbf(__builtin_amdgcn_sqrtf(rbf(rbf(var) + 1e-5f))));
}

template <int IMM> DI void bload(u32x4& dst, u32 voff, __amdgpu_buffer_rsrc_t srd, u32 soff) {
  asm volatile("buffer_load_dwordx4 %0, %1, %2, %3 offen offset:%c4" : "=v"(dst) : "v"(voff), "s"(srd), "s"(soff), "i"(IMM));
}
template <int IMM> DI void bload_sc1(u32x4& dst, u32 voff, __amdgpu_buffer_rsrc_t srd, u32 soff) {
  asm volatile("buffer_load_dwordx4 %0, %1, %2, %3 offen offset:%c4 sc1" : "=v"(dst) : "v"(voff), "s"(srd), "s"(soff), "i"(IMM));
}
template <int IMM> DI void bload_a(u32x4& dst, u32 voff, __amdgpu_buffer_rsrc_t srd, u32 soff) {
  asm volatile("buffer_load_dwordx4 %0, %1, %2, %3 offen offset:%c4" : "=a"(dst) : "v"(voff), "s"(srd), "s"(soff), "i"(IMM));
}
template <int IMM> DI void bload_a_sc1(u32x4& dst, u32 voff, __amdgpu_buffer_rsrc_t srd, u32 soff) {
  asm volatile("buffer_load_dwordx4 %0, %1, %2, %3 offen offset:%c4 sc1" : "=a"(dst) : "v"(voff), "s"(srd), "s"(soff), "i"(IMM));
}
DI void touch(u32& dst, u32 voff, __amdgpu_buffer_rsrc_t srd) {
  asm volatile("buffer_load_dword %0, %1, %2, 0 offen sc1" : "=v"(dst) : "v"(voff), "s"(srd));
}
template <int IMM> DI void bload2_sc1(u32x2& dst, u32 voff, __amdgpu_buffer_rsrc_t srd, u32 soff) {
  asm volatile("buffer_load_dwordx2 %0, %1, %2, %3 offen offset:%c4 sc1" : "=v"(dst) : "v"(voff), "s"(srd), "s"(soff), "i"(IMM));
}
template <int IMM> DI void bload2(u32x2& dst, u32 voff, __amdgpu_buffer_rsrc_t srd, u32 soff) {
  asm volatile("buffer_load_dwordx2 %0, %1, %2, %3 offen offset:%c4" : "=v"(dst) : "v"(voff), "s"(srd), "s"(soff), "i"(IMM));
}
DI void dma16(u32 m0v, u32 voff, __amdgpu_buffer_rsrc_t srd, u32 soff) {
  asm volatile("s_mov_b32 m0, %0\n\ts_nop 0\n\tbuffer_load_dwordx4 %1, %2, %3 offen lds" ::"s"(m0v), "v"(voff), "s"(srd), "s"(soff) : "m0");
}
template <int IMM> DI void dsread64(u32x2& dst, u32 addr) { asm volatile("ds_read_b64 %0, %1 offset:%c2" : "=v"(dst) : "v"(addr), "i"(IMM)); }
typedef u32x4 frag;
DI void mfma_vv(f32x4& acc, const frag& a, const frag& b) { asm volatile("v_mfma_f32_16x16x32_bf16 %0, %1, %2, %0" : "+v"(acc) : "v"(a), "v"(b)); }
DI void mfma_vv0(f32x4& acc, const frag& a, const frag& b) { asm volatile("v_mfma_f32_16x16x32_bf16 %0, %1, %2, 0" : "=&v"(acc) : "v"(a), "v"(b)); }
DI void mfma_av(f32x4& acc, const frag& a, const frag& b) { asm volatile("v_mfma_f32_16x16x32_bf16 %0, %1, %2, %0" : "+v"(acc) : "a"(a), "v"(b)); }
DI void mfma_av0(f32x4& acc, const frag& a, const frag& b) { asm volatile("v_mfma_f32_16x16x32_bf16 %0, %1, %2, 0" : "=&v"(acc) : "a"(a), "v"(b)); }
template <int N> DI void waitv() { asm volatile("s_waitcnt vmcnt(%c0)" ::"i"(N) : "memory"); }
template <int N> DI void waitv2(u32x4& a, u32x4& b) { asm volatile("s_waitcnt vmcnt(%c2)" : "+v"(a), "+v"(b) : "i"(N)); }
template <int N> DI void waitv4(u32x4& a, u32x4& b, u32x4& c, u32x4& d) { asm volatile("s_waitcnt vmcnt(%c4)" : "+v"(a), "+v"(b), "+v"(c), "+v"(d) : "i"(N)); }
DI frag catf(s16x4 a, s16x4 b) { return __builtin_bit_cast(frag, __builtin_shufflevector(a, b, 0, 1, 2, 3, 4, 5, 6, 7)); }
#define SB0 __builtin_amdgcn_sched_barrier(0)

constexpr int CV_RS = 1184;
constexpr int CV_KB = 4128;
constexpr int CV_WB = CV_KB + 4 * CV_RS;
template <int OFF> DI frag cv_ldB(u32 vb) { return catf(*(LAS const s16x4*)(size_t)(vb + OFF), *(volatile LAS const s16x4*)(size_t)(vb + OFF + 8)); }
template <int G> constexpr int cv_off0() { return 2 * (G >> 4) * CV_RS + 64 * (G & 15); }
template <int G> constexpr int cv_off1() { return (2 * (G >> 4) + 1) * CV_RS + 64 * (15 - (G & 15)); }
template <int G, int I> DI void cv_one(f32x4 (&acc)[4][8], const frag (&A)[16], const frag& B0, const frag& B1) {
  constexpr int R = 2 * (G >> 4), N = G & 15;
  constexpr int c0 = N <= 1 ? 0 : N / 2;
  constexpr int n0 = 8 - c0;
  if constexpr (I < n0) {
    constexpr int cb = c0 + I, s = 1 - N + 2 * cb;
    if constexpr (N == 0) mfma_vv0(acc[R][cb], A[s], B0); else mfma_vv(acc[R][cb], A[s], B0);
  } else {
    constexpr int c1 = (15 - N) / 2, cb = c1 + (I - n0), s = N - 14 + 2 * cb;
    static_assert(cb <= 7 && s >= 0 && s <= 15, "");
    if constexpr (s == 0) mfma_vv0(acc[R + 1][cb], A[s], B1); else mfma_vv(acc[R + 1][cb], A[s], B1);
  }
}
struct W1C { u32 vo[7]; u32 m0b, sob; __amdgpu_buffer_rsrc_t srd; };
template <int N, int R> DI void w1_m0(const W1C& c) { asm volatile("s_add_u32 m0, %0, %1" ::"s"(c.m0b), "i"(N * 4 * 7168 + R * 1024) : "scc", "m0"); }
template <int N, int R> DI void w1_ld(const W1C& c) {
  asm volatile("buffer_load_dwordx4 %0, %1, %2 offen lds" ::"v"(c.vo[R]), "s"(c.srd), "s"(c.sob + N * 4 * (32 * F * 2)));
}
template <int N, int R> DI void w1_piece(const W1C& c) { w1_m0<N, R>(c); asm volatile("s_nop 0"); w1_ld<N, R>(c); }
template <int N> DI void w1_step(const W1C& c) {
  w1_piece<N, 0>(c); w1_piece<N, 1>(c); w1_piece<N, 2>(c); w1_piece<N, 3>(c); w1_piece<N, 4>(c); w1_piece<N, 5>(c); w1_piece<N, 6>(c);
}
template <int G> DI void cv_steps(f32x4 (&acc)[4][8], const frag (&A)[16], u32 vb, frag P0, frag P1, frag Q0, frag Q1, const W1C& wc) {
  frag N0, N1;
  cv_one<G, 0>(acc, A, P0, P1); SB0;
  if constexpr (G + 2 < 32) N0 = cv_ldB<cv_off0<G + 2>()>(vb);
  SB0; cv_one<G, 1>(acc, A, P0, P1); cv_one<G, 2>(acc, A, P0, P1); SB0;
  if constexpr (G + 2 < 32) N1 = cv_ldB<cv_off1<G + 2>()>(vb);
  SB0; cv_one<G, 3>(acc, A, P0, P1); cv_one<G, 4>(acc, A, P0, P1);
  if constexpr (G < 28) w1_m0<G / 7, G % 7>(wc);
  cv_one<G, 5>(acc, A, P0, P1);
  if constexpr (G < 28) w1_ld<G / 7, G % 7>(wc);
  cv_one<G, 6>(acc, A, P0, P1); cv_one<G, 7>(acc, A, P0, P1); cv_one<G, 8>(acc, A, P0, P1);
  SB0;
  if constexpr (G + 1 < 32) cv_steps<G + 1>(acc, A, vb, Q0, Q1, N0, N1, wc);
}
constexpr int L_W1 = 0;
constexpr int L_Y = 143360;
constexpr int L_MOD = L_Y + 10240;
constexpr int L_RED = L_MOD + 8192;
constexpr int L_TOTAL = 163840;
constexpr int L_CONV = L_TOTAL - 4 * CV_WB;
static_assert(L_CONV >= 16 * 7168 && (L_CONV & 15) == 0 && L_RED + 1024 <= L_TOTAL, "");

typedef unsigned long long u64;
DI u64 call_tag(u32 epoch) { const u32 t = epoch + 1u; return (u64)t | ((u64)(~t) << 32); }
DI void signal(char* ws, size_t off, u64 tag) { __hip_atomic_store((GAS u64*)(ws + off), tag, __ATOMIC_RELAXED, __HIP_MEMORY_SCOPE_AGENT); }
DI void poll_tags(const char* base, int stride, int n, u64 tag, int lane) {
  const GAS u64* p = (const GAS u64*)(base + (lane < n ? lane : n - 1) * stride);
  for (u32 spins = 0; spins < (1u << 18); spins++) {
    const u64 v = __hip_atomic_load(p, __ATOMIC_RELAXED, __HIP_MEMORY_SCOPE_AGENT);
    if (__all(v == tag)) break;
    __builtin_amdgcn_s_sleep(1);
  }
}
DI void poll_tags256(const char* base, u64 tag, int lane) {
  const __amdgpu_buffer_rsrc_t srd = mksrd(base, 2048);
  const u32 lo = (u32)tag, hi = (u32)(tag >> 32);
  for (u32 spins = 0; spins < (1u << 18); spins++) {
    asm volatile("" ::: "memory");
    const u32x4 a = __builtin_amdgcn_raw_buffer_load_b128(srd, lane * 32, 0, 16), b = __builtin_amdgcn_raw_buffer_load_b128(srd, lane * 32 + 16, 0, 16);
    const bool ok = a[0] == lo && a[1] == hi && a[2] == lo && a[3] == hi && b[0] == lo && b[1] == hi && b[2] == lo && b[3] == hi;
    if (__all(ok)) break;
    __builtin_amdgcn_s_sleep(1);
  }
}
DI void lds_barrier() { asm volatile("s_waitcnt lgkmcnt(0)" ::: "memory"); __builtin_amdgcn_s_barrier(); asm volatile("" ::: "memory"); }

struct KVI { u32x2 k[7], v[4]; u32x4 s0, s1; };
template <int NT> DI void kv_item(const u32x2* kr, const u32x2* vr, const u32x4& s0, const u32x4& s1, char* dst) {
  const f32x2 sk[2][4] = {{{lo16(s0[0]), lo16(s0[2])}, {hi16(s0[0]), hi16(s0[2])}, {lo16(s0[1]), lo16(s0[3])}, {hi16(s0[1]), hi16(s0[3])}},
                          {{lo16(s1[0]), lo16(s1[2])}, {hi16(s1[0]), hi16(s1[2])}, {lo16(s1[1]), lo16(s1[3])}, {hi16(s1[1]), hi16(s1[3])}}};
  f32x2 kf[NT + 3][2], a[NT][2];
#pragma unroll
  for (int i = 0; i < NT + 3; i++) { kf[i][0] = up(kr[i][0]); kf[i][1] = up(kr[i][1]); }
#define KVS(EXPR) _Pragma("unroll") for (int r = 0; r < NT; r++) { _Pragma("unroll") for (int h = 0; h < 2; h++) { EXPR; } }
  KVS(a[r][h] = rb2(kf[r][h] * sk[h][3]))
  f32x2 pqt[NT][2];
  KVS(pqt[r][h] = rb2(kf[r + 1][h] * sk[h][2]))
  KVS(a[r][h] = rb2(a[r][h] + pqt[r][h]))
  KVS(pqt[r][h] = rb2(kf[r + 2][h] * sk[h][1]))
  KVS(a[r][h] = rb2(a[r][h] + pqt[r][h]))
  KVS(pqt[r][h] = rb2(kf[r + 3][h] * sk[h][0]))
  KVS(a[r][h] = rb2(a[r][h] + pqt[r][h]))
#undef KVS
#pragma unroll
  for (int r = 0; r < NT; r++) *(u32x2*)(dst + r * 176) = (u32x2){pk(a[r][0] * up(vr[r][0])), pk(a[r][1] * up(vr[r][1]))};
}
DI void phase_prep(const u16* __restrict__ k, const u16* __restrict__ v, const u16* __restrict__ shk, char* __restrict__ ws, char* smem,
                   int xcd, int tt, int tid, int lane, int w) {
  const __amdgpu_buffer_rsrc_t sK = mksrd(k, S * D * 2), sV = mksrd(v, S * D * 2), sS = mksrd(shk, D * 8);
  const u32 mgA = ((u32)tid * 3277u) >> 16, cqA = tid - 20 * mgA;
  const u32 i2 = 256 + (tid >> 2), mgB = (i2 * 3277u) >> 16, cqB = i2 - 20 * mgB, tB = 4 * mgB + (tid & 3);
  const u32 oA = ((64 * tt + 4 * mgA) * D + 80 * xcd + 4 * cqA) * 2, oB = ((64 * tt + tB - 3) * D + 80 * xcd + 4 * cqB) * 2;
  u32x2 kA[7], vA[4], kB[4], vB[1];
#pragma unroll
  for (int i = 0; i < 3; i++) {
    u32 o = oA - (3 - i) * (D * 2);
    asm volatile("" : "+v"(o));
    kA[i] = __builtin_amdgcn_raw_buffer_load_b64(sK, o, 0, 0);
  }
#pragma unroll
  for (int i = 0; i < 4; i++) kA[3 + i] = __builtin_amdgcn_raw_buffer_load_b64(sK, oA + i * (D * 2), 0, 0);
#pragma unroll
  for (int i = 0; i < 4; i++) vA[i] = __builtin_amdgcn_raw_buffer_load_b64(sV, oA + i * (D * 2), 0, 0);
  const u32x4 sA0 = __builtin_amdgcn_raw_buffer_load_b128(sS, (80 * xcd + 4 * cqA) * 8, 0, 0), sA1 = __builtin_amdgcn_raw_buffer_load_b128(sS, (80 * xcd + 4 * cqA) * 8 + 16, 0, 0);
#pragma unroll
  for (int i = 0; i < 4; i++) kB[i] = __builtin_amdgcn_raw_buffer_load_b64(sK, oB + i * (D * 2), 0, 0);
  vB[0] = __builtin_amdgcn_raw_buffer_load_b64(sV, oB + 3 * (D * 2), 0, 0);
  const u32x4 sB0 = __builtin_amdgcn_raw_buffer_load_b128(sS, (80 * xcd + 4 * cqB) * 8, 0, 0), sB1 = __builtin_amdgcn_raw_buffer_load_b128(sS, (80 * xcd + 4 * cqB) * 8 + 16, 0, 0);
  SB0;
  kv_item<4>(kA, vA, sA0, sA1, smem + 4 * mgA * 176 + 8 * cqA);
  kv_item<1>(kB, vB, sB0, sB1, smem + tB * 176 + 8 * cqB);
  lds_barrier();
  char* us = smem + 12288;
  {
    const int grp = lane >> 4, i16 = lane & 15, jj = i16 >> 2, p = i16 & 3;
#pragma unroll
    for (int n = 0; n < 3; n++) {
      const u32 task = 4 * (w + 4 * n) + grp;
      if (n < 2 || w < 2) {
        const u32 hr = (task * 52429u) >> 18, cgrp = task - 5 * hr, rho = hr >> 1, half = hr & 1;
        const u32 base = ldsaddr(smem) + (4 * (8 * half + jj) + rho) * 176 + (16 * cgrp + 4 * p) * 2;
        *(frag*)(us + (16 * cgrp + i16) * 144 + rho * 32 + half * 16) = catf(trread(base), trread(base + 16 * 176));
      }
    }
  }
  lds_barrier();
  {
    const __amdgpu_buffer_rsrc_t sU = mksrd(ws + WS_U, 2621440);
    const u32 uo = (tt * D + 80 * xcd + (lane >> 3)) * 128 + (lane & 7) * 16;
#pragma unroll
    for (int n = 0; n < 3; n++) {
      const int c8 = w + 4 * n;
      if (n < 2 || w < 2) __builtin_amdgcn_raw_buffer_store_b128(*(const u32x4*)(us + (8 * c8 + (lane >> 3)) * 144 + (lane & 7) * 16), sU, uo + c8 * 1024, 0, 16);
    }
  }
}

DI void phase_conv(const u32x4 (&rk)[4], u32 Draw, char* __restrict__ ws, char* smem, int xcd, int j, int w, int lane, u64 tag, const W1C& wc
) {
  const int ch = 80 * xcd + 4 * j + w;
  char* kim = smem + L_CONV + w * CV_WB;
  char* uim = kim + CV_KB;
  if (lane < 32) *(u32x4*)(uim + (lane >> 3) * CV_RS + 16 * (lane & 7)) = (u32x4){0u, 0u, 0u, 0u};
  if (lane < 2) *(u32x4*)(kim + 16 * lane) = (u32x4){0u, 0u, 0u, 0u};
#pragma unroll
  for (int i = 0; i < 4; i++) *(u32x4*)(kim + 32 + 16 * (64 * i + lane)) = rk[i];
  frag A[16];
  {
    const int q = lane >> 4, cc = lane & 15;
    const u32 va = ldsaddr(kim) + 224 - 64 * q - 8 * (cc >> 2) + 8 * (cc & 3);
#pragma unroll
    for (int s = 0; s < 16; s++) A[s] = catf(trread(va + 256 * s + 32), trread(va + 256 * s));
  }
  if (w == 0) poll_tags(ws + WS_FU + xcd * 2048, 64, 32, tag, lane);
  __builtin_amdgcn_s_barrier();
  MARK(22);
  {
    const __amdgpu_buffer_rsrc_t sU = mksrd(ws + WS_U, 2621440);
    const u32 uo = ((lane >> 3) * D + ch) * 128 + (lane & 7) * 16;
    u32x4 ru[4];
#pragma unroll
    for (int i = 0; i < 4; i++) ru[i] = __builtin_amdgcn_raw_buffer_load_b128(sU, uo + i * (8 * D * 128), 0, 16);
    char* ud = uim + ((lane & 7) >> 1) * CV_RS + 128 + 32 * (lane >> 3) + 16 * (lane & 1);
#pragma unroll
    for (int i = 0; i < 4; i++) *(u32x4*)(ud + 256 * i) = ru[i];
  }
  MARK(0);
  const int q = lane >> 4, cc = lane & 15;
  const u32 vb = ldsaddr(uim) + 8 + 8 * cc + 16 * q;
  f32x4 acc[4][8];
  frag P0, P1, Q0, Q1;
  P0 = cv_ldB<cv_off0<0>()>(vb); P1 = cv_ldB<cv_off1<0>()>(vb); Q0 = cv_ldB<cv_off0<1>()>(vb); Q1 = cv_ldB<cv_off1<1>()>(vb);
  MARK(1);
  cv_steps<0>(acc, A, vb, P0, P1, Q0, Q1, wc);
  asm volatile("s_nop 7\n\ts_nop 1");
#pragma unroll
  for (int r = 0; r < 4; r++)
#pragma unroll
    for (int cb = 0; cb < 8; cb += 4) asm volatile("" : "+v"(acc[r][cb]), "+v"(acc[r][cb + 1]), "+v"(acc[r][cb + 2]), "+v"(acc[r][cb + 3]));
  MARK(2);
  const int idx = 4 * (lane == 0 ? 63 : (lane >= 16 ? lane - 16 : lane + 47));
  const u32 ub = ldsaddr(uim) + 2 * (64 + 4 * cc + q);
  char* yl = kim + 32 * cc + 8 * q;
  const float Dch = (w & 1) ? hi16(Draw) : lo16(Draw);
  const f32x2 DD = {Dch, Dch};
  float o[8][4], cy[8][3];
  u32 u01[8], u23[8];
#pragma unroll
  for (int cb = 0; cb < 8; cb++) {
    const LAS u16* up_ = (const LAS u16*)(size_t)(ub + 128 * cb);
    u01[cb] = (u32)up_[0] | ((u32)up_[CV_RS / 2] << 16);
    u23[cb] = (u32)up_[CV_RS] | ((u32)up_[3 * CV_RS / 2] << 16);
  }
#pragma unroll
  for (int cb = 0; cb < 8; cb++) {
    const f32x4 z0 = acc[0][cb], z1 = acc[1][cb], z2 = acc[2][cb], z3 = acc[3][cb];
    o[cb][0] = z0[0]; o[cb][1] = z0[1] + z1[0]; o[cb][2] = z0[2] + z1[1] + z2[0]; o[cb][3] = (z0[3] + z1[2]) + (z2[1] + z3[0]);
    cy[cb][0] = z1[3] + z2[2] + z3[1]; cy[cb][1] = z2[3] + z3[2]; cy[cb][2] = z3[3];
  }
#pragma unroll
  for (int cb = 0; cb < 8; cb++)
#pragma unroll
    for (int e = 0; e < 3; e++) {
      const float t = lane == 63 ? (cb ? cy[cb - 1][e] : 0.f) : cy[cb][e];
      o[cb][e] += __int_as_float(__builtin_amdgcn_ds_bpermute(idx, __float_as_int(t)));
    }
#pragma unroll
  for (int cb = 0; cb < 8; cb++) {
    const f32x2 y01 = rb2((f32x2){o[cb][0], o[cb][1]}) + rb2(up(u01[cb]) * DD), y23 = rb2((f32x2){o[cb][2], o[cb][3]}) + rb2(up(u23[cb]) * DD);
    *(u32x2*)(yl + 512 * cb) = (u32x2){pk(y01), pk(y23)};
  }
  lds_barrier();
  {
    const __amdgpu_buffer_rsrc_t sY = mksrd(ws + WS_Y2B, 2621440);
    const int c4 = (lane >> 1) & 3, tgl = 32 * w + (lane >> 3), hf = lane & 1;
    const char* src = smem + L_CONV + c4 * CV_WB + tgl * 32 + 16 * hf;
    const u32 yo = (tgl * D + 80 * xcd + 4 * j + c4) * 32 + 16 * hf;
    u32x4 v[4];
#pragma unroll
    for (int i = 0; i < 4; i++) v[i] = *(const u32x4*)(src + 256 * i);
#pragma unroll
    for (int i = 0; i < 4; i++) __builtin_amdgcn_raw_buffer_store_b128(v[i], sY, yo + i * (8 * D * 32), 0, 16);
  }
  MARK(3);
}

DI void phase_spare(const u16* __restrict__ c, const u16* __restrict__ wmod, const u16* __restrict__ w2,
                    char* __restrict__ ws, char* smem, int xcd, int j, int w, int tid, int lane) {
  const int r0 = (j - 20) * 8 + xcd;
  const __amdgpu_buffer_rsrc_t sWS = mksrd(ws, 65536);
  if (r0 < 60) {
    float* sc = (float*)(smem + L_Y);
    float* red = sc + 640;
    const int col0 = 64 * r0, cl = tid & 7, rg = tid >> 3;
    u32x4 r[20];
#pragma unroll
    for (int i = 0; i < 20; i++) r[i] = *(const u32x4*)(wmod + (size_t)(rg + 32 * i) * (6 * D) + col0 + 8 * cl);
    SB0;
    for (int i = tid; i < D; i += 256) { float x = lo16(c[i]); sc[i] = rbf(x / (1.f + __expf(-x))); }
    __syncthreads();
    f32x2 a[4] = {{0.f, 0.f}, {0.f, 0.f}, {0.f, 0.f}, {0.f, 0.f}};
#pragma unroll
    for (int i = 0; i < 20; i++) {
      const float s = sc[rg + 32 * i];
#pragma unroll
      for (int e = 0; e < 4; e++) a[e] += up(r[i][e]) * (f32x2){s, s};
    }
    float af[8] = {a[0][0], a[0][1], a[1][0], a[1][1], a[2][0], a[2][1], a[3][0], a[3][1]};
#pragma unroll
    for (int e = 0; e < 8; e++) af[e] += __int_as_float(__builtin_amdgcn_update_dpp(0, __float_as_int(af[e]), 0x128, 0xf, 0xf, false));
#pragma unroll
    for (int e = 0; e < 8; e++) { auto rr = __builtin_amdgcn_permlane16_swap(__float_as_uint(af[e]), __float_as_uint(af[e]), false, false); af[e] = __uint_as_float(rr[0]) + __uint_as_float(rr[1]); }
#pragma unroll
    for (int e = 0; e < 8; e++) { auto rr = __builtin_amdgcn_permlane32_swap(__float_as_uint(af[e]), __float_as_uint(af[e]), false, false); af[e] = __uint_as_float(rr[0]) + __uint_as_float(rr[1]); }
    if (lane < 8) { *(f32x4*)(red + w * 64 + 8 * lane) = (f32x4){af[0], af[1], af[2], af[3]}; *(f32x4*)(red + w * 64 + 8 * lane + 4) = (f32x4){af[4], af[5], af[6], af[7]}; }
    __syncthreads();
    if (tid < 32) {
      const int c0 = 2 * tid;
      float s0 = rbf((red[c0] + red[64 + c0]) + (red[128 + c0] + red[192 + c0]));
      float s1 = rbf((red[c0 + 1] + red[65 + c0]) + (red[129 + c0] + red[193 + c0]));
      const int jc = col0 + c0, vv = jc / D, ch = jc - vv * D;
      if (vv == 1 || vv == 4) { s0 += 1.f; s1 += 1.f; }
      const int slot = vv == 0 ? 1 : vv == 1 ? 0 : vv == 2 ? 2 : vv == 3 ? 4 : vv == 4 ? 3 : 5;
      __builtin_amdgcn_raw_buffer_store_b32(pk2(s0, s1), sWS, WS_MODP + ((ch >> 2) * 24 + slot * 4 + (ch & 3)) * 2, 0, 16);
    }
  } else {
    const int wv = (r0 - 60) * 4 + w;
    char* mb = smem + L_Y + w * (32 * 144);
    u32x4 r[4][4];
#pragma unroll
    for (int n = 0; n < 4; n++) {
      const int u = wv + 144 * n, uc = u < 540 ? u : 539, ks = uc / 10, cc = uc - 10 * ks;
#pragma unroll
      for (int i = 0; i < 4; i++) r[n][i] = *(const u32x4*)(w2 + (size_t)(32 * ks + 8 * i + (lane >> 3)) * D + 64 * cc + 8 * (lane & 7));
    }
    SB0;
    const int q = lane >> 4, i16 = lane & 15, jj = i16 >> 2, p = i16 & 3;
    const __amdgpu_buffer_rsrc_t sO = mksrd(ws + WS_W2F, 2211840);
    const u32 a0 = ldsaddr(mb) + (4 * q + jj) * 144 + 8 * p;
#pragma unroll
    for (int n = 0; n < 4; n++) {
      const int u = wv + 144 * n, uc = u < 540 ? u : 539, ks = uc / 10, cc = uc - 10 * ks;
#pragma unroll
      for (int i = 0; i < 4; i++) *(u32x4*)(mb + (8 * i + (lane >> 3)) * 144 + 16 * (lane & 7)) = r[n][i];
      frag v[4];
#pragma unroll
      for (int f = 0; f < 4; f++) v[f] = catf(trread(a0 + 32 * f), trread(a0 + 32 * f + 16 * 144));
#pragma unroll
      for (int f = 0; f < 4; f++) {
        const int ft = 4 * cc + f;
        if (u < 540) __builtin_amdgcn_raw_buffer_store_b128(v[f], sO, (((ft / 5) * 54 + ks) * 5 + (ft % 5)) * 1024 + lane * 16, 0, 16);
      }
    }
  }
}

DI float xsum8_16_32(float x) {
  x += __int_as_float(__builtin_amdgcn_update_dpp(0, __float_as_int(x), 0x128, 0xf, 0xf, false));
  { auto r = __builtin_amdgcn_permlane16_swap(__float_as_uint(x), __float_as_uint(x), false, false); x = __uint_as_float(r[0]) + __uint_as_float(r[1]); }
  { auto r = __builtin_amdgcn_permlane32_swap(__float_as_uint(x), __float_as_uint(x), false, false); x = __uint_as_float(r[0]) + __uint_as_float(r[1]); }
  return x;
}
DI void ln_stats(const f32x2 (&x)[10], f32x2* red, int w, int lane, float& mean, float& rinv) {
  f32x2 a = ((x[0] + x[1]) + (x[2] + x[3])) + ((x[4] + x[5]) + (x[6] + x[7])) + (x[8] + x[9]);
  f32x2 b0 = x[0] * x[0], b1 = x[1] * x[1];
#pragma unroll
  for (int h = 2; h < 10; h += 2) { b0 += x[h] * x[h]; b1 += x[h + 1] * x[h + 1]; }
  b0 += b1;
  const float s1 = xsum8_16_32(a[0] + a[1]), s2 = xsum8_16_32(b0[0] + b0[1]);
  if (lane < 8) red[w * 8 + lane] = (f32x2){s1, s2};
  lds_barrier();
  const int tk = lane & 7;
  const f32x2 t = (red[tk] + red[8 + tk]) + (red[16 + tk] + red[24 + tk]);
  ln_finish(t[0], t[1], mean, rinv);
}
DI f32x2 gelu2(f32x2 x) {
  const float c0 = -2.f * 0.7978845608028654f * 1.4426950408889634f, c1 = c0 * 0.044715f;
  const f32x2 z = x * (x * x * (f32x2){c1, c1} + (f32x2){c0, c0});
  const f32x2 d = (f32x2){__builtin_amdgcn_exp2f(z[0]), __builtin_amdgcn_exp2f(z[1])} + (f32x2){1.f, 1.f};
  return x * (f32x2){__builtin_amdgcn_rcpf(d[0]), __builtin_amdgcn_rcpf(d[1])};
}
DI int rotk(int n, int rot, int m) { int k = n + rot; return k >= m ? k - m : k; }
struct W2C { u32 l16; int rot2; __amdgpu_buffer_rsrc_t srd; };
template <int I> DI void w2_load1(frag (&W2)[60], const W2C& c) {
  const u32 vo = c.l16 + rotk(I / 5, c.rot2, 14) * 5120;
  if constexpr (I % 5 < 4) bload_a_sc1<(I % 5) * 1024>(W2[I], vo, c.srd, 0); else bload_a_sc1<0>(W2[I], vo + 4096, c.srd, 0);
}
template <int I, int N> DI void w2_loads(frag (&W2)[60], const W2C& c) {
  w2_load1<I>(W2, c);
  if constexpr (N > 1) w2_loads<I + 1, N - 1>(W2, c);
}
#define W2_M0V 8
#define W2_M1V 20
#define W2_G1V 20
constexpr int W2_M0 = W2_M0V, W2_M1 = W2_M1V, W2_G1 = W2_G1V;

constexpr int G1R = 6;
struct G1S {
  frag Y[G1R][2];
  frag A0[7], A1[7];
  f32x4 acc[7][2];
};
template <int FI> DI frag g1_lda(u32 ab) { return catf(trread(ab + 32 * FI), trread(ab + 32 * FI + 1792)); }
struct G1C { u32 b0, l16, ybase; int rot1; __amdgpu_buffer_rsrc_t srdY; };
template <int KS, int FI>
DI void g1_f(G1S& s, frag (&Ac)[7], frag (&An)[7], frag (&W2)[60], const G1C& c, const W2C& c2, u32 ab, u32 yo) {
  mfma_vv(s.acc[FI][0], Ac[FI], s.Y[KS % G1R][0]);
  SB0;
  if constexpr (KS < 19) An[FI] = g1_lda<FI>(ab);
  SB0;
  mfma_vv(s.acc[FI][1], Ac[FI], s.Y[KS % G1R][1]);
  if constexpr (KS >= 1 && KS - 1 + G1R < 20 && (FI == 1 || FI == 4)) bload_sc1<(FI == 4) * 1024>(s.Y[(KS - 1) % G1R][FI == 4], c.l16, c.srdY, yo);
  if constexpr (KS < W2_G1 && FI == 6) w2_load1<W2_M0 + W2_M1 + KS>(W2, c2);
  SB0;
  if constexpr (FI < 6) g1_f<KS, FI + 1>(s, Ac, An, W2, c, c2, ab, yo);
}
template <int KS> constexpr int g1_younger() {
  int cnt = 0; bool seen = false;
  for (int k = 0; k < G1R; k++) { if (seen) cnt += 2; if (k == KS) seen = true; }
  for (int n = 0; n < KS; n++) {
    if (n >= 1 && n - 1 + G1R < 20) { if (seen) cnt += 2; if (n - 1 + G1R == KS) seen = true; }
    if (n < W2_G1 && seen) cnt += 1;
  }
  return cnt;
}
template <int KS>
DI void g1_step(G1S& s, frag (&W2)[60], const G1C& c, const W2C& c2) {
  waitv2<g1_younger<KS>()>(s.Y[KS % G1R][0], s.Y[KS % G1R][1]);
  const u32 ab = c.b0 + rotk(KS + 1, c.rot1, 20) * 7168;
  const u32 yo = c.ybase + rotk(KS - 1 + G1R, c.rot1, 20) * 2048;
  if constexpr (KS & 1) g1_f<KS, 0>(s, s.A1, s.A0, W2, c, c2, ab, yo);
  else                  g1_f<KS, 0>(s, s.A0, s.A1, W2, c, c2, ab, yo);
  if constexpr (KS < 19) g1_step<KS + 1>(s, W2, c, c2);
}
struct G2S {
  frag H[4][4];
  frag WX[10];
  f32x4 acc[5][4];
  u32x2 xx[5], gg[5], b2v[5];
};
struct G2C { u32 l16, vx, vb2, vg; int rot2; __amdgpu_buffer_rsrc_t srdH, srdX, srdB2, srdMD; };
template <int N, int T> DI void g2_hload1(G2S& s, const G2C& c, u32 vo) { bload_sc1<T * 1024>(s.H[N & 3][T], vo, c.srdH, 0); }
template <int N> DI void g2_hload(G2S& s, const G2C& c) {
  const u32 vo = c.l16 + rotk(N, c.rot2, 14) * 4096;
  g2_hload1<N, 0>(s, c, vo); g2_hload1<N, 1>(s, c, vo); g2_hload1<N, 2>(s, c, vo); g2_hload1<N, 3>(s, c, vo);
}
template <int I> DI void g2_eload(G2S& s, const G2C& c) {
  if constexpr (I < 5) bload2_sc1<32 * I>(s.xx[I], c.vx, c.srdX, 0);
  else if constexpr (I < 10) bload2<32 * (I - 5)>(s.b2v[I - 5], c.vb2, c.srdB2, 0);
  else bload2_sc1<192 * (I - 10)>(s.gg[I - 10], c.vg, c.srdMD, 0);
}
template <int N, int M>
DI void g2_m(G2S& s, frag (&W2)[60], const G2C& c, u32 vo) {
  constexpr int d = M >> 2, t = M & 3;
  if constexpr (N == 0) mfma_av0(s.acc[d][t], W2[d], s.H[0][t]);
  else if constexpr (N < 12) mfma_av(s.acc[d][t], W2[N * 5 + d], s.H[N & 3][t]);
  else mfma_vv(s.acc[d][t], s.WX[(N - 12) * 5 + d], s.H[N & 3][t]);
  if constexpr (N + 3 < 14 && (M == 1 || M == 6 || M == 11 || M == 16)) g2_hload1<N + 3, (M - 1) / 5>(s, c, vo);
  if constexpr (N >= 11 && (M == 1 || M == 5 || M == 9 || M == 13 || M == 17)) g2_eload<5 * (N - 11) + (M - 1) / 4>(s, c);
  if constexpr (M < 19) g2_m<N, M + 1>(s, W2, c, vo);
}
template <int N> constexpr int g2_younger() {
  int cnt = 0; bool seen = false;
  for (int h = 0; h < 3; h++) { if (seen) cnt += 4; if (h == N) seen = true; }
  for (int n = 0; n < N; n++) {
    if (n + 3 < 14) { if (seen) cnt += 4; if (n + 3 == N) seen = true; }
    if (n >= 11 && seen) cnt += 5;
  }
  return cnt;
}
static_assert(g2_younger<0>() == 8 && g2_younger<3>() == 8 && g2_younger<11>() == 8 && g2_younger<12>() == 9 && g2_younger<13>() == 10, "");
template <int N>
DI void g2_step(G2S& s, frag (&W2)[60], const G2C& c) {
  waitv4<g2_younger<N>()>(s.H[N & 3][0], s.H[N & 3][1], s.H[N & 3][2], s.H[N & 3][3]);
  const u32 vo = c.l16 + rotk(N + 3, c.rot2, 14) * 4096;
  g2_m<N, 0>(s, W2, c, vo);
  if constexpr (N < 13) g2_step<N + 1>(s, W2, c);
}

__global__ __launch_bounds__(256, 1) void k_main(const u16* __restrict__ qin, const u16* __restrict__ kin, const u16* __restrict__ vin,
                                                 const u16* __restrict__ shk, const u16* __restrict__ ssk, const u16* __restrict__ ssD,
                                                 const u16* __restrict__ cin, const u16* __restrict__ wmod, const u16* __restrict__ w1,
                                                 const u16* __restrict__ b1, const u16* __restrict__ w2, const u16* __restrict__ b2,
                                                 char* __restrict__ ws, u16* __restrict__ out) {
  STAMP_BEGIN(ws)
  MARK_DECL
  extern __shared__ __attribute__((aligned(16))) char smem[];
  const int tid = threadIdx.x, lane = tid & 63, w = rfl(tid >> 6);
  const int b = blockIdx.x, xcd = b & 7, j = b >> 3;
  const int tile = 4 * (xcd >> 1) + (j >> 3), g = 8 * (xcd & 1) + (j & 7);
  const int TT = 4 * xcd + (j >> 3), cg = j & 7;
  const u32 l16 = lane * 16, lds0 = ldsaddr(smem);
  W1C wc;
#pragma unroll
  for (int r = 0; r < 7; r++) { const u32 idx = 64 * r + lane, row = (idx * 4682u) >> 16; wc.vo[r] = row * (F * 2 - 224) + idx * 16; }
  wc.m0b = lds0 + L_W1 + w * 7168; wc.sob = w * (32 * F * 2) + 224 * g; wc.srd = mksrd(w1, D * F * 2);
  const int tg = 16 * tile + g, t0 = 8 * tg;
  const int tk = lane & 7, slot = lane >> 3, q = lane >> 4;
  u32x2 xr[5];
  {
    const __amdgpu_buffer_rsrc_t srdQ = mksrd(qin + (size_t)t0 * D, 10240);
    const u32 vq = tk * 1280 + (40 * w + slot) * 8;
    asm volatile("s_nop 4");
    bload2<0>(xr[0], vq, srdQ, 0); bload2<64>(xr[1], vq, srdQ, 0); bload2<128>(xr[2], vq, srdQ, 0); bload2<192>(xr[3], vq, srdQ, 0); bload2<256>(xr[4], vq, srdQ, 0);
  }
  const u64 tag = call_tag(__hip_atomic_load((const GAS u32*)(ws + WS_EPOCH), __ATOMIC_RELAXED, __HIP_MEMORY_SCOPE_AGENT));
  u32x4 rk[4];
  u32 Draw = 0;
  if (j < 20) {
    const int ch = 80 * xcd + 4 * j + w;
    const __amdgpu_buffer_rsrc_t sF = mksrd(ssk + (size_t)ch * S, S * 2);
#pragma unroll
    for (int i = 0; i < 4; i++) rk[i] = __builtin_amdgcn_raw_buffer_load_b128(sF, l16 + 1024 * i, 0, 0);
    Draw = __builtin_amdgcn_raw_buffer_load_b32(mksrd(ssD, D * 2), (ch >> 1) * 4, 0, 0);
  }
  phase_prep(kin, vin, shk, ws, smem, xcd, j, tid, lane, w);
  waitv<0>();
  __builtin_amdgcn_s_barrier();
  if (tid == 0) signal(ws, WS_FU + (xcd * 32 + j) * 64, tag);
  MARK(23);
  if (j < 20) {
    phase_conv(rk, Draw, ws, smem, xcd, j, w, lane, tag, wc
    );
  } else {
    phase_spare(cin, wmod, w2, ws, smem, xcd, j, w, tid, lane);
  }
  asm volatile("s_waitcnt vmcnt(0)" : "+v"(xr[0]), "+v"(xr[1]), "+v"(xr[2]), "+v"(xr[3]), "+v"(xr[4]) :: "memory");
  __builtin_amdgcn_s_barrier();
  MARK(4);
  if (tid < 8) {
    signal(ws, WS_FG + tid * 2048 + b * 8, tag);
    const int r0 = (j - 20) * 8 + xcd;
    if (r0 >= 60) signal(ws, WS_FW + tid * 512 + (r0 - 60) * 8, tag);
  }
  const int ks0 = w < 2 ? 14 * w : 28 + 13 * (w - 2), nks = w < 2 ? 14 : 13;
  W2C c2;
  c2.l16 = l16; c2.srd = mksrd(ws + WS_W2F + (size_t)cg * 276480 + ks0 * 5120, nks * 5120);
  { int r2 = 2 * cg + 4 * (j >> 3); r2 = r2 >= 14 ? r2 - 14 : r2; c2.rot2 = r2 >= 14 ? r2 - 14 : r2; }
  frag W2[60];
  f32x2 x[10], t[10];
  f32x2* red = (f32x2*)(smem + L_RED);
  {
    if (j >= 20) { w1_step<0>(wc); w1_step<1>(wc); }
#pragma unroll
    for (int i = 0; i < 5; i++) { x[2 * i] = up(xr[i][0]); x[2 * i + 1] = up(xr[i][1]); }
    if (w == 0) poll_tags(ws + WS_FW + xcd * 512, 8, 36, tag, lane);
    float mean, rinv;
    ln_stats(x, red + 64, w, lane, mean, rinv);
    const f32x2 mm = {mean, mean}, rr = {rinv, rinv};
#pragma unroll
    for (int h = 0; h < 10; h++) t[h] = rb2(x[h] - mm);
#pragma unroll
    for (int h = 0; h < 10; h++) t[h] = rb2(t[h] * rr);
#pragma unroll
    for (int h = 0; h < 10; h++) asm volatile("" : "+v"(x[h]), "+v"(t[h]));
  }
  if (j >= 20) { w1_step<2>(wc); w1_step<3>(wc); }
  w2_loads<0, W2_M0>(W2, c2);
  if (w == 0) poll_tags256(ws + WS_FG + xcd * 2048, tag, lane);
  __builtin_amdgcn_s_barrier();
  if (b == 0 && tid == 0) __hip_atomic_store((GAS u32*)(ws + WS_EPOCH), (u32)tag, __ATOMIC_RELAXED, __HIP_MEMORY_SCOPE_AGENT);
  MARK(5);
  const __amdgpu_buffer_rsrc_t srdMD = mksrd(ws + WS_MODP, 7680);
  u32x2 bb[7];
  {
    const __amdgpu_buffer_rsrc_t srdYB = mksrd(ws + WS_Y2B + (size_t)(tg >> 1) * 20480, 20480);
    const __amdgpu_buffer_rsrc_t srdB1 = mksrd(b1, F * 2);
    u32x4 yr[3], mr[2];
    const u32 v16 = tid * 16, vb = (16 * 7 * g + 4 * q) * 2;
    asm volatile("s_nop 4");
    MARK(20);
    const u32 vy = tid * 32 + 16 * (tg & 1);
    bload_sc1<0>(yr[0], vy, srdYB, 0); bload_sc1<0>(yr[1], vy + 8192, srdYB, 0); bload_sc1<0>(yr[2], vy + 16384, srdYB, 0);
    bload_sc1<0>(mr[0], v16, srdMD, 0); bload_sc1<0>(mr[1], v16 + 4096, srdMD, 0);
    w1_step<4>(wc);
    asm volatile("s_waitcnt vmcnt(7)" : "+v"(yr[0]), "+v"(yr[1]), "+v"(yr[2]), "+v"(mr[0]), "+v"(mr[1]));
    MARK(19);
    *(u32x4*)(smem + L_Y + v16) = yr[0]; *(u32x4*)(smem + L_Y + 4096 + v16) = yr[1];
    if (tid < 128) *(u32x4*)(smem + L_Y + 8192 + v16) = yr[2];
    *(u32x4*)(smem + L_MOD + v16) = mr[0]; *(u32x4*)(smem + L_MOD + 4096 + v16) = mr[1];
    lds_barrier();
    MARK(6);
    const int i16 = lane & 15, jj = i16 >> 2, p = i16 & 3;
    const u32 ya = lds0 + L_Y + (4 * (40 * w + 2 * q + (p >> 1)) + jj) * 16 + 8 * (p & 1);
    const char* mdp = smem + L_MOD + (40 * w + slot) * 48;
    u32 yy[10], w1v[10], sav[10], gav[10];
#pragma unroll
    for (int i = 0; i < 5; i++) {
      const u32x2 yu = __builtin_bit_cast(u32x2, trread(ya + i * 8 * 64));
      const u32x4 m0 = *(const u32x4*)(mdp + i * 384);
      const u32x2 m1 = *(const u32x2*)(mdp + i * 384 + 16);
      yy[2 * i] = yu[0]; yy[2 * i + 1] = yu[1]; w1v[2 * i] = m0[0]; w1v[2 * i + 1] = m0[1]; sav[2 * i] = m0[2]; sav[2 * i + 1] = m0[3]; gav[2 * i] = m1[0]; gav[2 * i + 1] = m1[1];
    }
    u32 o[10];
#define STAGE(EXPR) _Pragma("unroll") for (int h = 0; h < 10; h++) { EXPR; }
    STAGE(t[h] = rb2(t[h] * up(w1v[h])))
    STAGE(t[h] = rb2(t[h] + up(sav[h])))
    STAGE(t[h] = rb2(t[h] * up(yy[h])))
    STAGE(t[h] = rb2(up(gav[h]) * t[h]))
    STAGE(o[h] = pk(x[h] + t[h]))
    const __amdgpu_buffer_rsrc_t srdX1 = mksrd(ws + WS_X1, 2621440);
    const u32 xo = (t0 + tk) * 1280 + (40 * w + slot) * 8;
#pragma unroll
    for (int i = 0; i < 5; i++) __builtin_amdgcn_raw_buffer_store_b64((u32x2){o[2 * i], o[2 * i + 1]}, srdX1, xo + i * 64, 0, 16);
    STAGE(x[h] = up(o[h]))
    MARK(7);
    float mean, rinv;
    SB0;
    ln_stats(x, red, w, lane, mean, rinv);
    {
      const f32x2 m2 = {mean, mean}, r2 = {rinv, rinv};
#pragma unroll
      for (int i = 0; i < 5; i++) {
        const u32x4 m0 = *(const u32x4*)(mdp + i * 384 + 16);
        const u32x2 m1 = *(const u32x2*)(mdp + i * 384 + 32);
        x[2 * i] = (x[2 * i] - m2) * r2 * up(m0[2]) + up(m1[0]);
        x[2 * i + 1] = (x[2 * i + 1] - m2) * r2 * up(m0[3]) + up(m1[1]);
      }
    }
    MARK(8);
    SB0;
    {
      bload2<0>(bb[0], vb, srdB1, 0); bload2<32>(bb[1], vb, srdB1, 0); bload2<64>(bb[2], vb, srdB1, 0); bload2<96>(bb[3], vb, srdB1, 0);
      bload2<128>(bb[4], vb, srdB1, 0); bload2<160>(bb[5], vb, srdB1, 0); bload2<192>(bb[6], vb, srdB1, 0);
    }
    SB0;
    ln_stats(x, red + 32, w, lane, mean, rinv);
    {
      const f32x2 m3 = {mean, mean}, r3 = {rinv, rinv};
      STAGE(o[h] = pk((x[h] - m3) * r3))
      const int rt = tg >> 1, r = 8 * (tg & 1) + tk;
      const __amdgpu_buffer_rsrc_t srdY3 = mksrd(ws + WS_Y3F, 2621440);
      const u32 yo = (rt >> 1) * 40960 + (rt & 1) * 1024 + ((2 * (slot >> 2) + (slot & 1)) * 16 + r) * 16 + 8 * ((slot >> 1) & 1) + 5 * w * 2048;
#pragma unroll
      for (int i = 0; i < 5; i++) __builtin_amdgcn_raw_buffer_store_b64((u32x2){o[2 * i], o[2 * i + 1]}, srdY3, yo + i * 2048, 0, 16);
    }
#undef STAGE
  }
  MARK(9);
  asm volatile("s_waitcnt vmcnt(0)" : "+v"(bb[0]), "+v"(bb[1]), "+v"(bb[2]), "+v"(bb[3]), "+v"(bb[4]), "+v"(bb[5]), "+v"(bb[6]) :: "memory");
  __builtin_amdgcn_s_barrier();
  if (tid == 0) signal(ws, WS_FA + (tile * 16 + g) * 64, tag);
  MARK(10);
  w2_loads<W2_M0, W2_M1>(W2, c2);
  if (w == 0) poll_tags(ws + WS_FA + tile * 1024, 64, 16, tag, lane);
  __builtin_amdgcn_s_barrier();
  MARK(11);
  f32x4 hacc[7][2];
  {
    G1C c1;
    const int i16 = lane & 15, jj = i16 >> 2, p = i16 & 3;
    c1.srdY = mksrd(ws + WS_Y3F, 2621440); c1.l16 = l16;
    c1.rot1 = (5 * (j & 7)) >> 1;
    c1.ybase = (tile * 4 + w) * 40960;
    c1.b0 = lds0 + L_W1 + (16 * (q >> 1) + 4 * (q & 1) + jj) * 224 + 8 * p;
    G1S s;
    asm volatile("s_nop 4");
#pragma unroll
    for (int k = 0; k < G1R; k++) { const u32 yo = c1.ybase + rotk(k, c1.rot1, 20) * 2048; bload_sc1<0>(s.Y[k][0], l16, c1.srdY, yo); bload_sc1<1024>(s.Y[k][1], l16, c1.srdY, yo); }
#pragma unroll
    for (int f = 0; f < 7; f++) {
      const f32x4 bv = {lo16(bb[f][0]), hi16(bb[f][0]), lo16(bb[f][1]), hi16(bb[f][1])};
      s.acc[f][0] = bv; s.acc[f][1] = bv;
      asm volatile("" : "+v"(s.acc[f][0]), "+v"(s.acc[f][1]));
    }
    const u32 ab0 = c1.b0 + c1.rot1 * 7168;
    s.A0[0] = g1_lda<0>(ab0); s.A0[1] = g1_lda<1>(ab0); s.A0[2] = g1_lda<2>(ab0); s.A0[3] = g1_lda<3>(ab0);
    s.A0[4] = g1_lda<4>(ab0); s.A0[5] = g1_lda<5>(ab0); s.A0[6] = g1_lda<6>(ab0);
    SB0;
    g1_step<0>(s, W2, c1, c2);
    asm volatile("s_nop 7\n\ts_nop 1");
#pragma unroll
    for (int f = 0; f < 7; f++) { asm volatile("" : "+v"(s.acc[f][0]), "+v"(s.acc[f][1])); hacc[f][0] = s.acc[f][0]; hacc[f][1] = s.acc[f][1]; }
  }
  MARK(12);
  G2S s2_;
  {
    const __amdgpu_buffer_rsrc_t srdH = mksrd(ws + WS_HF, 7077888);
    const int rt0 = 8 * tile + 2 * w;
    const u32 ho = ((rt0 >> 2) * 54 * 4 + (rt0 & 3)) * 1024 + l16 + ((7 * g) >> 1) * 4096;
    auto gel = [&](int f, int tt) -> u32x2 {
      const f32x4 a = hacc[f][tt];
      return (u32x2){pk(gelu2((f32x2){a[0], a[1]})), pk(gelu2((f32x2){a[2], a[3]}))};
    };
    if (g & 1) {
      { const u32x2 a = gel(0, 0), c = gel(0, 1);
        __builtin_amdgcn_raw_buffer_store_b64(a, srdH, ho + 8, 0, 16); __builtin_amdgcn_raw_buffer_store_b64(c, srdH, ho + 1024 + 8, 0, 16); }
#pragma unroll
      for (int i = 0; i < 3; i++) {
        const u32x2 a0 = gel(2 * i + 1, 0), a1 = gel(2 * i + 2, 0), c0 = gel(2 * i + 1, 1), c1 = gel(2 * i + 2, 1);
        if (7 * g + 2 * i + 2 < 108) {
          __builtin_amdgcn_raw_buffer_store_b128((u32x4){a0[0], a0[1], a1[0], a1[1]}, srdH, ho + (1 + i) * 4096, 0, 16);
          __builtin_amdgcn_raw_buffer_store_b128((u32x4){c0[0], c0[1], c1[0], c1[1]}, srdH, ho + (1 + i) * 4096 + 1024, 0, 16);
        }
      }
    } else {
#pragma unroll
      for (int i = 0; i < 3; i++) {
        const u32x2 a0 = gel(2 * i, 0), a1 = gel(2 * i + 1, 0), c0 = gel(2 * i, 1), c1 = gel(2 * i + 1, 1);
        __builtin_amdgcn_raw_buffer_store_b128((u32x4){a0[0], a0[1], a1[0], a1[1]}, srdH, ho + i * 4096, 0, 16);
        __builtin_amdgcn_raw_buffer_store_b128((u32x4){c0[0], c0[1], c1[0], c1[1]}, srdH, ho + i * 4096 + 1024, 0, 16);
      }
      { const u32x2 a = gel(6, 0), c = gel(6, 1);
        __builtin_amdgcn_raw_buffer_store_b64(a, srdH, ho + 3 * 4096, 0, 16); __builtin_amdgcn_raw_buffer_store_b64(c, srdH, ho + 3 * 4096 + 1024, 0, 16); }
    }
  }
  MARK(13);
  waitv<0>();
  __builtin_amdgcn_s_barrier();
  if (tid == 0) signal(ws, WS_FB + (tile * 16 + g) * 64, tag);
  {
    const u32 vo = l16 + rotk(12, c2.rot2, 14) * 5120, v1 = l16 + rotk(13, c2.rot2, 14) * 5120;
    if constexpr (W2_M0 + W2_M1 + W2_G1 < 60) w2_loads<W2_M0 + W2_M1 + W2_G1, 60 - W2_M0 - W2_M1 - W2_G1>(W2, c2);
    bload_sc1<0>(s2_.WX[0], vo, c2.srd, 0); bload_sc1<1024>(s2_.WX[1], vo, c2.srd, 0); bload_sc1<2048>(s2_.WX[2], vo, c2.srd, 0); bload_sc1<3072>(s2_.WX[3], vo, c2.srd, 0);
    bload_sc1<0>(s2_.WX[4], vo + 4096, c2.srd, 0);
    bload_sc1<0>(s2_.WX[5], v1, c2.srd, 0); bload_sc1<1024>(s2_.WX[6], v1, c2.srd, 0); bload_sc1<2048>(s2_.WX[7], v1, c2.srd, 0); bload_sc1<3072>(s2_.WX[8], v1, c2.srd, 0);
    bload_sc1<0>(s2_.WX[9], v1 + 4096, c2.srd, 0);
  }
  MARK(14);
  const int cc = lane & 15, tok = 64 * TT + 16 * w + cc;
  G2C cg2;
  cg2.l16 = l16; cg2.rot2 = c2.rot2;
  cg2.srdH = mksrd(ws + WS_HF + (size_t)TT * 221184 + ks0 * 4096, nks * 4096);
  cg2.srdX = mksrd(ws + WS_X1, 2621440); cg2.srdB2 = mksrd(b2, D * 2); cg2.srdMD = srdMD;
  cg2.vx = tok * 1280 + (80 * cg + 4 * q) * 2; cg2.vb2 = (80 * cg + 4 * q) * 2; cg2.vg = (20 * cg + q) * 48 + 40;
  if (w == 0) poll_tags(ws + WS_FB + (TT >> 1) * 1024, 64, 16, tag, lane);
  __builtin_amdgcn_s_barrier();
  MARK(15);
  asm volatile("s_nop 4");
  g2_hload<0>(s2_, cg2); g2_hload<1>(s2_, cg2); g2_hload<2>(s2_, cg2);
  asm volatile("s_waitcnt vmcnt(12)" : "+v"(s2_.WX[0]), "+v"(s2_.WX[1]), "+v"(s2_.WX[2]), "+v"(s2_.WX[3]), "+v"(s2_.WX[4]), "+v"(s2_.WX[5]), "+v"(s2_.WX[6]), "+v"(s2_.WX[7]), "+v"(s2_.WX[8]), "+v"(s2_.WX[9]));
  MARK(21);
  g2_step<0>(s2_, W2, cg2);
  asm volatile("s_nop 7\n\ts_nop 1");
#pragma unroll
  for (int d = 0; d < 5; d++) asm volatile("" : "+v"(s2_.acc[d][0]), "+v"(s2_.acc[d][1]), "+v"(s2_.acc[d][2]), "+v"(s2_.acc[d][3]));
  MARK(16);
  f32x4* redq = (f32x4*)smem;
  f32x4 own[5];
#define G2_OWN(T) { _Pragma("unroll") for (int d = 0; d < 5; d++) { own[d] = s2_.acc[d][T]; \
    _Pragma("unroll") for (int t = 0; t < 4; t++) if (t != T) redq[((T * 4 + t) * 5 + d) * 64 + lane] = s2_.acc[d][t]; } }
  if (w == 0) G2_OWN(0) else if (w == 1) G2_OWN(1) else if (w == 2) G2_OWN(2) else G2_OWN(3)
#undef G2_OWN
  lds_barrier();
  asm volatile("s_waitcnt vmcnt(0)" : "+v"(s2_.xx[0]), "+v"(s2_.xx[1]), "+v"(s2_.xx[2]), "+v"(s2_.xx[3]), "+v"(s2_.xx[4]), "+v"(s2_.b2v[0]), "+v"(s2_.b2v[1]), "+v"(s2_.b2v[2]), "+v"(s2_.b2v[3]), "+v"(s2_.b2v[4]),
               "+v"(s2_.gg[0]), "+v"(s2_.gg[1]), "+v"(s2_.gg[2]), "+v"(s2_.gg[3]), "+v"(s2_.gg[4]));
  MARK(17);
  {
    const f32x4* r1 = redq + ((((w + 1) & 3) * 4 + w) * 5) * 64 + lane;
    const f32x4* r2 = redq + ((((w + 2) & 3) * 4 + w) * 5) * 64 + lane;
    const f32x4* r3 = redq + ((((w + 3) & 3) * 4 + w) * 5) * 64 + lane;
#pragma unroll
    for (int d = 0; d < 5; d++) {
      const f32x4 sacc = (own[d] + r1[d * 64]) + (r2[d * 64] + r3[d * 64]);
      u32x2 o;
#pragma unroll
      for (int h = 0; h < 2; h++) {
        const f32x2 y = rb2(rb2((f32x2){sacc[2 * h], sacc[2 * h + 1]}) + up(s2_.b2v[d][h]));
        o[h] = pk(up(s2_.xx[d][h]) + rb2(up(s2_.gg[d][h]) * y));
      }
      *(u32x2*)(out + (size_t)tok * D + 80 * cg + 16 * d + 4 * q) = o;
    }
  }
  MARK(18);
  MARK_FLUSH(ws, 0, 24);
  STAMP_END(ws, 0);
}

extern "C" void kernel_launch(void* const* d_in, const int* in_sizes, int n_in, void* d_out, int out_size, void* d_ws, size_t ws_size,
                              hipStream_t stream) {
  const u16* q = (const u16*)d_in[0]; const u16* k = (const u16*)d_in[1]; const u16* v = (const u16*)d_in[2];
  const u16* shk = (const u16*)d_in[3]; const u16* ssk = (const u16*)d_in[4]; const u16* ssD = (const u16*)d_in[5];
  const u16* w1 = (const u16*)d_in[7]; const u16* b1 = (const u16*)d_in[8]; const u16* w2 = (const u16*)d_in[9];
  const u16* b2 = (const u16*)d_in[10]; const u16* c = (const u16*)d_in[11]; const u16* wmod = (const u16*)d_in[12];
  char* ws = (char*)d_ws;
  (void)hipFuncSetAttribute((const void*)k_main, hipFuncAttributeMaxDynamicSharedMemorySize, L_TOTAL);
  k_main<<<256, 256, L_TOTAL, stream>>>(q, k, v, shk, ssk, ssD, c, wmod, w1, b1, w2, b2, ws, (u16*)d_out);
}
```

```cpp
#include <hip/hip_runtime.h>
#include <stdint.h>

typedef unsigned short u16;
typedef unsigned int u32;
typedef short s16x4 __attribute__((ext_vector_type(4)));
typedef short s16x8 __attribute__((ext_vector_type(8)));
typedef __bf16 bf16x8 __attribute__((ext_vector_type(8)));
typedef __bf16 bf16x2 __attribute__((ext_vector_type(2)));
typedef float f32x4 __attribute__((ext_vector_type(4)));
typedef float f32x2 __attribute__((ext_vector_type(2)));
typedef u32 u32x4 __attribute__((ext_vector_type(4)));
typedef u32 u32x2 __attribute__((ext_vector_type(2)));
#define LAS __attribute__((address_space(3)))
#define GAS __attribute__((address_space(1)))
#define DI __device__ __forceinline__

constexpr int S = 2048, D = 640, F = 1728;
constexpr size_t WS_MODP = 0;
constexpr size_t WS_EPOCH = 8192;
constexpr size_t WS_FU = 16384;
constexpr size_t WS_FA = WS_FU + 16384;
constexpr size_t WS_FB = WS_FA + 16384;
constexpr size_t WS_FG = WS_FB + 16384;
constexpr size_t WS_FW = WS_FG + 16384;
constexpr size_t WS_U    = 131072;
constexpr size_t WS_Y2B  = WS_U + 2621440;
constexpr size_t WS_X1   = WS_Y2B + 2621440;
constexpr size_t WS_Y3F  = WS_X1 + 2621440;
constexpr size_t WS_HF   = WS_Y3F + 2621440;
constexpr size_t WS_W2F  = WS_HF + 7077888;

#define STAMP_BEGIN(ws)
#define STAMP_END(ws, kid)
#define MARK_DECL
#define MARK(i)
#define MARK_FLUSH(ws, kid, n)

DI float lo16(u32 x) { return __uint_as_float(x << 16); }
DI float hi16(u32 x) { return __uint_as_float(x & 0xffff0000u); }
DI u32 pk(f32x2 v) { return __builtin_bit_cast(u32, __builtin_convertvector(v, bf16x2)); }
DI u32 pk2(float a, float b) { return pk((f32x2){a, b}); }
DI f32x2 up(u32 x) { return (f32x2){lo16(x), hi16(x)}; }
DI f32x2 rb2(f32x2 v) { return up(pk(v)); }
DI float rbf(float a) { return lo16(pk2(a, a)); }
DI int rfl(int x) { return __builtin_amdgcn_readfirstlane(x); }
DI s16x4 trread(u32 ldsaddr) { return __builtin_amdgcn_ds_read_tr16_b64_v4i16((LAS s16x4*)(size_t)ldsaddr); }
DI bf16x8 cat8(s16x4 a, s16x4 b) { return __builtin_bit_cast(bf16x8, __builtin_shufflevector(a, b, 0, 1, 2, 3, 4, 5, 6, 7)); }
DI f32x4 mfma16(bf16x8 a, bf16x8 b, f32x4 c) { return __builtin_amdgcn_mfma_f32_16x16x32_bf16(a, b, c, 0, 0, 0); }
DI u32 ldsaddr(const void* p) { return (u32)(size_t)(LAS const char*)p; }
DI __amdgpu_buffer_rsrc_t mksrd(const void* p, u32 bytes) { return __builtin_amdgcn_make_buffer_rsrc((void*)p, 0, bytes, 0x00020000); }

[[maybe_unused]] DI float wsum64(float x) {
  x += __int_as_float(__builtin_amdgcn_update_dpp(0, __float_as_int(x), 0xB1, 0xf, 0xf, false));
  x += __int_as_float(__builtin_amdgcn_update_dpp(0, __float_as_int(x), 0x4E, 0xf, 0xf, false));
  x += __int_as_float(__builtin_amdgcn_update_dpp(0, __float_as_int(x), 0x141, 0xf, 0xf, false));
  x += __int_as_float(__builtin_amdgcn_update_dpp(0, __float_as_int(x), 0x128, 0xf, 0xf, false));
  { auto r = __builtin_amdgcn_permlane16_swap(__float_as_uint(x), __float_as_uint(x), false, false); x = __uint_as_float(r[0]) + __uint_as_float(r[1]); }
  { auto r = __builtin_amdgcn_permlane32_swap(__float_as_uint(x), __float_as_uint(x), false, false); x = __uint_as_float(r[0]) + __uint_as_float(r[1]); }
  return x;
}
DI void ln_finish(float sum, float sq, float& mean, float& rinv) {
  const float m = sum * (1.f / D);
  const float var = fmaxf(sq * (1.f / D) - m * m, 0.f);
  mean = rbf(m);
  rinv = __builtin_amdgcn_rcpf(rbf(__builtin_amdgcn_sqrtf(rbf(rbf(var) + 1e-5f))));
}

template <int IMM> DI void bload(u32x4& dst, u32 voff, __amdgpu_buffer_rsrc_t srd, u32 soff) {
  asm volatile("buffer_load_dwordx4 %0, %1, %2, %3 offen offset:%c4" : "=v"(dst) : "v"(voff), "s"(srd), "s"(soff), "i"(IMM));
}
template <int IMM> DI void bload_sc1(u32x4& dst, u32 voff, __amdgpu_buffer_rsrc_t srd, u32 soff) {
  asm volatile("buffer_load_dwordx4 %0, %1, %2, %3 offen offset:%c4 sc1" : "=v"(dst) : "v"(voff), "s"(srd), "s"(soff), "i"(IMM));
}
template <int IMM> DI void bload_a(u32x4& dst, u32 voff, __amdgpu_buffer_rsrc_t srd, u32 soff) {
  asm volatile("buffer_load_dwordx4 %0, %1, %2, %3 offen offset:%c4" : "=a"(dst) : "v"(voff), "s"(srd), "s"(soff), "i"(IMM));
}
template <int IMM> DI void bload_a_sc1(u32x4& dst, u32 voff, __amdgpu_buffer_rsrc_t srd, u32 soff) {
  asm volatile("buffer_load_dwordx4 %0, %1, %2, %3 offen offset:%c4 sc1" : "=a"(dst) : "v"(voff), "s"(srd), "s"(soff), "i"(IMM));
}
DI void touch(u32& dst, u32 voff, __amdgpu_buffer_rsrc_t srd) {
  asm volatile("buffer_load_dword %0, %1, %2, 0 offen sc1" : "=v"(dst) : "v"(voff), "s"(srd));
}
template <int IMM> DI void bload2_sc1(u32x2& dst, u32 voff, __amdgpu_buffer_rsrc_t srd, u32 soff) {
  asm volatile("buffer_load_dwordx2 %0, %1, %2, %3 offen offset:%c4 sc1" : "=v"(dst) : "v"(voff), "s"(srd), "s"(soff), "i"(IMM));
}
template <int IMM> DI void bload2(u32x2& dst, u32 voff, __amdgpu_buffer_rsrc_t srd, u32 soff) {
  asm volatile("buffer_load_dwordx2 %0, %1, %2, %3 offen offset:%c4" : "=v"(dst) : "v"(voff), "s"(srd), "s"(soff), "i"(IMM));
}
DI void dma16(u32 m0v, u32 voff, __amdgpu_buffer_rsrc_t srd, u32 soff) {
  asm volatile("s_mov_b32 m0, %0\n\ts_nop 0\n\tbuffer_load_dwordx4 %1, %2, %3 offen lds" ::"s"(m0v), "v"(voff), "s"(srd), "s"(soff) : "m0");
}
template <int IMM> DI void dsread64(u32x2& dst, u32 addr) { asm volatile("ds_read_b64 %0, %1 offset:%c2" : "=v"(dst) : "v"(addr), "i"(IMM)); }
typedef u32x4 frag;
DI void mfma_vv(f32x4& acc, const frag& a, const frag& b) { asm volatile("v_mfma_f32_16x16x32_bf16 %0, %1, %2, %0" : "+v"(acc) : "v"(a), "v"(b)); }
DI void mfma_vv0(f32x4& acc, const frag& a, const frag& b) { asm volatile("v_mfma_f32_16x16x32_bf16 %0, %1, %2, 0" : "=&v"(acc) : "v"(a), "v"(b)); }
DI void mfma_av(f32x4& acc, const frag& a, const frag& b) { asm volatile("v_mfma_f32_16x16x32_bf16 %0, %1, %2, %0" : "+v"(acc) : "a"(a), "v"(b)); }
DI void mfma_av0(f32x4& acc, const frag& a, const frag& b) { asm volatile("v_mfma_f32_16x16x32_bf16 %0, %1, %2, 0" : "=&v"(acc) : "a"(a), "v"(b)); }
template <int N> DI void waitv() { asm volatile("s_waitcnt vmcnt(%c0)" ::"i"(N) : "memory"); }
template <int N> DI void waitv2(u32x4& a, u32x4& b) { asm volatile("s_waitcnt vmcnt(%c2)" : "+v"(a), "+v"(b) : "i"(N)); }
template <int N> DI void waitv4(u32x4& a, u32x4& b, u32x4& c, u32x4& d) { asm volatile("s_waitcnt vmcnt(%c4)" : "+v"(a), "+v"(b), "+v"(c), "+v"(d) : "i"(N)); }
DI frag catf(s16x4 a, s16x4 b) { return __builtin_bit_cast(frag, __builtin_shufflevector(a, b, 0, 1, 2, 3, 4, 5, 6, 7)); }
#define SB0 __builtin_amdgcn_sched_barrier(0)

constexpr int CV_RS = 1184;
constexpr int CV_KB = 4128;
constexpr int CV_WB = CV_KB + 4 * CV_RS;
template <int OFF> DI frag cv_ldB(u32 vb) { return catf(*(LAS const s16x4*)(size_t)(vb + OFF), *(volatile LAS const s16x4*)(size_t)(vb + OFF + 8)); }
template <int G> constexpr int cv_off0() { return 2 * (G >> 4) * CV_RS + 64 * (G & 15); }
template <int G> constexpr int cv_off1() { return (2 * (G >> 4) + 1) * CV_RS + 64 * (15 - (G & 15)); }
template <int G, int I> DI void cv_one(f32x4 (&acc)[4][8], const frag (&A)[16], const frag& B0, const frag& B1) {
  constexpr int R = 2 * (G >> 4), N = G & 15;
  constexpr int c0 = N <= 1 ? 0 : N / 2;
  constexpr int n0 = 8 - c0;
  if constexpr (I < n0) {
    constexpr int cb = c0 + I, s = 1 - N + 2 * cb;
    if constexpr (N == 0) mfma_vv0(acc[R][cb], A[s], B0); else mfma_vv(acc[R][cb], A[s], B0);
  } else {
    constexpr int c1 = (15 - N) / 2, cb = c1 + (I - n0), s = N - 14 + 2 * cb;
    static_assert(cb <= 7 && s >= 0 && s <= 15, "");
    if constexpr (s == 0) mfma_vv0(acc[R + 1][cb], A[s], B1); else mfma_vv(acc[R + 1][cb], A[s], B1);
  }
}
struct W1C { u32 vo[7]; u32 m0b, sob; __amdgpu_buffer_rsrc_t srd; };
template <int N, int R> DI void w1_m0(const W1C& c) { asm volatile("s_add_u32 m0, %0, %1" ::"s"(c.m0b), "i"(N * 4 * 7168 + R * 1024) : "scc", "m0"); }
template <int N, int R> DI void w1_ld(const W1C& c) {
  asm volatile("buffer_load_dwordx4 %0, %1, %2 offen lds" ::"v"(c.vo[R]), "s"(c.srd), "s"(c.sob + N * 4 * (32 * F * 2)));
}
template <int N, int R> DI void w1_piece(const W1C& c) { w1_m0<N, R>(c); asm volatile("s_nop 0"); w1_ld<N, R>(c); }
template <int N> DI void w1_step(const W1C& c) {
  w1_piece<N, 0>(c); w1_piece<N, 1>(c); w1_piece<N, 2>(c); w1_piece<N, 3>(c); w1_piece<N, 4>(c); w1_piece<N, 5>(c); w1_piece<N, 6>(c);
}
template <int G> DI void cv_steps(f32x4 (&acc)[4][8], const frag (&A)[16], u32 vb, frag P0, frag P1, frag Q0, frag Q1, const W1C& wc) {
  frag N0, N1;
  cv_one<G, 0>(acc, A, P0, P1); SB0;
  if constexpr (G + 2 < 32) N0 = cv_ldB<cv_off0<G + 2>()>(vb);
  SB0; cv_one<G, 1>(acc, A, P0, P1); cv_one<G, 2>(acc, A, P0, P1); SB0;
  if constexpr (G + 2 < 32) N1 = cv_ldB<cv_off1<G + 2>()>(vb);
  SB0; cv_one<G, 3>(acc, A, P0, P1); cv_one<G, 4>(acc, A, P0, P1);
  if constexpr (G < 28) w1_m0<G / 7, G % 7>(wc);
  cv_one<G, 5>(acc, A, P0, P1);
  if constexpr (G < 28) w1_ld<G / 7, G % 7>(wc);
  cv_one<G, 6>(acc, A, P0, P1); cv_one<G, 7>(acc, A, P0, P1); cv_one<G, 8>(acc, A, P0, P1);
  SB0;
  if constexpr (G + 1 < 32) cv_steps<G + 1>(acc, A, vb, Q0, Q1, N0, N1, wc);
}
constexpr int L_W1 = 0;
constexpr int L_Y = 143360;
constexpr int L_MOD = L_Y + 10240;
constexpr int L_RED = L_MOD + 8192;
constexpr int L_TOTAL = 163840;
constexpr int L_CONV = L_TOTAL - 4 * CV_WB;
static_assert(L_CONV >= 16 * 7168 && (L_CONV & 15) == 0 && L_RED + 1024 <= L_TOTAL, "");

typedef unsigned long long u64;
DI u64 call_tag(u32 epoch) { const u32 t = epoch + 1u; return (u64)t | ((u64)(~t) << 32); }
DI void signal(char* ws, size_t off, u64 tag) { __hip_atomic_store((GAS u64*)(ws + off), tag, __ATOMIC_RELAXED, __HIP_MEMORY_SCOPE_AGENT); }
DI void poll_tags(const char* base, int stride, int n, u64 tag, int lane) {
  const GAS u64* p = (const GAS u64*)(base + (lane < n ? lane : n - 1) * stride);
  for (u32 spins = 0; spins < (1u << 18); spins++) {
    const u64 v = __hip_atomic_load(p, __ATOMIC_RELAXED, __HIP_MEMORY_SCOPE_AGENT);
    if (__all(v == tag)) break;
    __builtin_amdgcn_s_sleep(1);
  }
}
DI void poll_tags256(const char* base, u64 tag, int lane) {
  const __amdgpu_buffer_rsrc_t srd = mksrd(base, 2048);
  const u32 lo = (u32)tag, hi = (u32)(tag >> 32);
  for (u32 spins = 0; spins < (1u << 18); spins++) {
    asm volatile("" ::: "memory");
    const u32x4 a = __builtin_amdgcn_raw_buffer_load_b128(srd, lane * 32, 0, 16), b = __builtin_amdgcn_raw_buffer_load_b128(srd, lane * 32 + 16, 0, 16);
    const bool ok = a[0] == lo && a[1] == hi && a[2] == lo && a[3] == hi && b[0] == lo && b[1] == hi && b[2] == lo && b[3] == hi;
    if (__all(ok)) break;
    __builtin_amdgcn_s_sleep(1);
  }
}
DI void lds_barrier() { asm volatile("s_waitcnt lgkmcnt(0)" ::: "memory"); __builtin_amdgcn_s_barrier(); asm volatile("" ::: "memory"); }

struct KVI { u32x2 k[7], v[4]; u32x4 s0, s1; };
template <int NT> DI void kv_item(const u32x2* kr, const u32x2* vr, const u32x4& s0, const u32x4& s1, char* dst) {
  const f32x2 sk[2][4] = {{{lo16(s0[0]), lo16(s0[2])}, {hi16(s0[0]), hi16(s0[2])}, {lo16(s0[1]), lo16(s0[3])}, {hi16(s0[1]), hi16(s0[3])}},
                          {{lo16(s1[0]), lo16(s1[2])}, {hi16(s1[0]), hi16(s1[2])}, {lo16(s1[1]), lo16(s1[3])}, {hi16(s1[1]), hi16(s1[3])}}};
  f32x2 kf[NT + 3][2], a[NT][2];
#pragma unroll
  for (int i = 0; i < NT + 3; i++) { kf[i][0] = up(kr[i][0]); kf[i][1] = up(kr[i][1]); }
#define KVS(EXPR) _Pragma("unroll") for (int r = 0; r < NT; r++) { _Pragma("unroll") for (int h = 0; h < 2; h++) { EXPR; } }
  KVS(a[r][h] = rb2(kf[r][h] * sk[h][3]))
  f32x2 pqt[NT][2];
  KVS(pqt[r][h] = rb2(kf[r + 1][h] * sk[h][2]))
  KVS(a[r][h] = rb2(a[r][h] + pqt[r][h]))
  KVS(pqt[r][h] = rb2(kf[r + 2][h] * sk[h][1]))
  KVS(a[r][h] = rb2(a[r][h] + pqt[r][h]))
  KVS(pqt[r][h] = rb2(kf[r + 3][h] * sk[h][0]))
  KVS(a[r][h] = rb2(a[r][h] + pqt[r][h]))
#undef KVS
#pragma unroll
  for (int r = 0; r < NT; r++) *(u32x2*)(dst + r * 176) = (u32x2){pk(a[r][0] * up(vr[r][0])), pk(a[r][1] * up(vr[r][1]))};
}
DI void phase_prep(const u16* __restrict__ k, const u16* __restrict__ v, const u16* __restrict__ shk, char* __restrict__ ws, char* smem,
                   int xcd, int tt, int tid, int lane, int w) {
  const __amdgpu_buffer_rsrc_t sK = mksrd(k, S * D * 2), sV = mksrd(v, S * D * 2), sS = mksrd(shk, D * 8);
  const u32 mgA = ((u32)tid * 3277u) >> 16, cqA = tid - 20 * mgA;
  const u32 i2 = 256 + (tid >> 2), mgB = (i2 * 3277u) >> 16, cqB = i2 - 20 * mgB, tB = 4 * mgB + (tid & 3);
  const u32 oA = ((64 * tt + 4 * mgA) * D + 80 * xcd + 4 * cqA) * 2, oB = ((64 * tt + tB - 3) * D + 80 * xcd + 4 * cqB) * 2;
  u32x2 kA[7], vA[4], kB[4], vB[1];
#pragma unroll
  for (int i = 0; i < 3; i++) {
    u32 o = oA - (3 - i) * (D * 2);
    asm volatile("" : "+v"(o));
    kA[i] = __builtin_amdgcn_raw_buffer_load_b64(sK, o, 0, 0);
  }
#pragma unroll
  for (int i = 0; i < 4; i++) kA[3 + i] = __builtin_amdgcn_raw_buffer_load_b64(sK, oA + i * (D * 2), 0, 0);
#pragma unroll
  for (int i = 0; i < 4; i++) vA[i] = __builtin_amdgcn_raw_buffer_load_b64(sV, oA + i * (D * 2), 0, 0);
  const u32x4 sA0 = __builtin_amdgcn_raw_buffer_load_b128(sS, (80 * xcd + 4 * cqA) * 8, 0, 0), sA1 = __builtin_amdgcn_raw_buffer_load_b128(sS, (80 * xcd + 4 * cqA) * 8 + 16, 0, 0);
#pragma unroll
  for (int i = 0; i < 4; i++) kB[i] = __builtin_amdgcn_raw_buffer_load_b64(sK, oB + i * (D * 2), 0, 0);
  vB[0] = __builtin_amdgcn_raw_buffer_load_b64(sV, oB + 3 * (D * 2), 0, 0);
  const u32x4 sB0 = __builtin_amdgcn_raw_buffer_load_b128(sS, (80 * xcd + 4 * cqB) * 8, 0, 0), sB1 = __builtin_amdgcn_raw_buffer_load_b128(sS, (80 * xcd + 4 * cqB) * 8 + 16, 0, 0);
  SB0;
  kv_item<4>(kA, vA, sA0, sA1, smem + 4 * mgA * 176 + 8 * cqA);
  kv_item<1>(kB, vB, sB0, sB1, smem + tB * 176 + 8 * cqB);
  lds_barrier();
  char* us = smem + 12288;
  {
    const int grp = lane >> 4, i16 = lane & 15, jj = i16 >> 2, p = i16 & 3;
#pragma unroll
    for (int n = 0; n < 3; n++) {
      const u32 task = 4 * (w + 4 * n) + grp;
      if (n < 2 || w < 2) {
        const u32 hr = (task * 52429u) >> 18, cgrp = task - 5 * hr, rho = hr >> 1, half = hr & 1;
        const u32 base = ldsaddr(smem) + (4 * (8 * half + jj) + rho) * 176 + (16 * cgrp + 4 * p) * 2;
        *(frag*)(us + (16 * cgrp + i16) * 144 + rho * 32 + half * 16) = catf(trread(base), trread(base + 16 * 176));
      }
    }
  }
  lds_barrier();
  {
    const __amdgpu_buffer_rsrc_t sU = mksrd(ws + WS_U, 2621440);
    const u32 uo = (tt * D + 80 * xcd + (lane >> 3)) * 128 + (lane & 7) * 16;
#pragma unroll
    for (int n = 0; n < 3; n++) {
      const int c8 = w + 4 * n;
      if (n < 2 || w < 2) __builtin_amdgcn_raw_buffer_store_b128(*(const u32x4*)(us + (8 * c8 + (lane >> 3)) * 144 + (lane & 7) * 16), sU, uo + c8 * 1024, 0, 16);
    }
  }
}

DI void phase_conv(const u32x4 (&rk)[4], u32 Draw, char* __restrict__ ws, char* smem, int xcd, int j, int w, int lane, u64 tag, const W1C& wc
) {
  const int ch = 80 * xcd + 4 * j + w;
  char* kim = smem + L_CONV + w * CV_WB;
  char* uim = kim + CV_KB;
  if (lane < 32) *(u32x4*)(uim + (lane >> 3) * CV_RS + 16 * (lane & 7)) = (u32x4){0u, 0u, 0u, 0u};
  if (lane < 2) *(u32x4*)(kim + 16 * lane) = (u32x4){0u, 0u, 0u, 0u};
#pragma unroll
  for (int i = 0; i < 4; i++) *(u32x4*)(kim + 32 + 16 * (64 * i + lane)) = rk[i];
  frag A[16];
  {
    const int q = lane >> 4, cc = lane & 15;
    const u32 va = ldsaddr(kim) + 224 - 64 * q - 8 * (cc >> 2) + 8 * (cc & 3);
#pragma unroll
    for (int s = 0; s < 16; s++) A[s] = catf(trread(va + 256 * s + 32), trread(va + 256 * s));
  }
  if (w == 0) poll_tags(ws + WS_FU + xcd * 2048, 64, 32, tag, lane);
  __builtin_amdgcn_s_barrier();
  MARK(22);
  {
    const __amdgpu_buffer_rsrc_t sU = mksrd(ws + WS_U, 2621440);
    const u32 uo = ((lane >> 3) * D + ch) * 128 + (lane & 7) * 16;
    u32x4 ru[4];
#pragma unroll
    for (int i = 0; i < 4; i++) ru[i] = __builtin_amdgcn_raw_buffer_load_b128(sU, uo + i * (8 * D * 128), 0, 16);
    char* ud = uim + ((lane & 7) >> 1) * CV_RS + 128 + 32 * (lane >> 3) + 16 * (lane & 1);
#pragma unroll
    for (int i = 0; i < 4; i++) *(u32x4*)(ud + 256 * i) = ru[i];
  }
  MARK(0);
  const int q = lane >> 4, cc = lane & 15;
  const u32 vb = ldsaddr(uim) + 8 + 8 * cc + 16 * q;
  f32x4 acc[4][8];
  frag P0, P1, Q0, Q1;
  P0 = cv_ldB<cv_off0<0>()>(vb); P1 = cv_ldB<cv_off1<0>()>(vb); Q0 = cv_ldB<cv_off0<1>()>(vb); Q1 = cv_ldB<cv_off1<1>()>(vb);
  MARK(1);
  cv_steps<0>(acc, A, vb, P0, P1, Q0, Q1, wc);
  asm volatile("s_nop 7\n\ts_nop 1");
#pragma unroll
  for (int r = 0; r < 4; r++)
#pragma unroll
    for (int cb = 0; cb < 8; cb += 4) asm volatile("" : "+v"(acc[r][cb]), "+v"(acc[r][cb + 1]), "+v"(acc[r][cb + 2]), "+v"(acc[r][cb + 3]));
  MARK(2);
  const int idx = 4 * (lane == 0 ? 63 : (lane >= 16 ? lane - 16 : lane + 47));
  const u32 ub = ldsaddr(uim) + 2 * (64 + 4 * cc + q);
  char* yl = kim + 32 * cc + 8 * q;
  const float Dch = (w & 1) ? hi16(Draw) : lo16(Draw);
  const f32x2 DD = {Dch, Dch};
  float o[8][4], cy[8][3];
  u32 u01[8], u23[8];
#pragma unroll
  for (int cb = 0; cb < 8; cb++) {
    const LAS u16* up_ = (const LAS u16*)(size_t)(ub + 128 * cb);
    u01[cb] = (u32)up_[0] | ((u32)up_[CV_RS / 2] << 16);
    u23[cb] = (u32)up_[CV_RS] | ((u32)up_[3 * CV_RS / 2] << 16);
  }
#pragma unroll
  for (int cb = 0; cb < 8; cb++) {
    const f32x4 z0 = acc[0][cb], z1 = acc[1][cb], z2 = acc[2][cb], z3 = acc[3][cb];
    o[cb][0] = z0[0]; o[cb][1] = z0[1] + z1[0]; o[cb][2] = z0[2] + z1[1] + z2[0]; o[cb][3] = (z0[3] + z1[2]) + (z2[1] + z3[0]);
    cy[cb][0] = z1[3] + z2[2] + z3[1]; cy[cb][1] = z2[3] + z3[2]; cy[cb][2] = z3[3];
  }
#pragma unroll
  for (int cb = 0; cb < 8; cb++)
#pragma unroll
    for (int e = 0; e < 3; e++) {
      const float t = lane == 63 ? (cb ? cy[cb - 1][e] : 0.f) : cy[cb][e];
      o[cb][e] += __int_as_float(__builtin_amdgcn_ds_bpermute(idx, __float_as_int(t)));
    }
#pragma unroll
  for (int cb = 0; cb < 8; cb++) {
    const f32x2 y01 = rb2((f32x2){o[cb][0], o[cb][1]}) + rb2(up(u01[cb]) * DD), y23 = rb2((f32x2){o[cb][2], o[cb][3]}) + rb2(up(u23[cb]) * DD);
    *(u32x2*)(yl + 512 * cb) = (u32x2){pk(y01), pk(y23)};
  }
  lds_barrier();
  {
    const __amdgpu_buffer_rsrc_t sY = mksrd(ws + WS_Y2B, 2621440);
    const int c4 = (lane >> 1) & 3, tgl = 32 * w + (lane >> 3), hf = lane & 1;
    const char* src = smem + L_CONV + c4 * CV_WB + tgl * 32 + 16 * hf;
    const u32 yo = (tgl * D + 80 * xcd + 4 * j + c4) * 32 + 16 * hf;
    u32x4 v[4];
#pragma unroll
    for (int i = 0; i < 4; i++) v[i] = *(const u32x4*)(src + 256 * i);
#pragma unroll
    for (int i = 0; i < 4; i++) __builtin_amdgcn_raw_buffer_store_b128(v[i], sY, yo + i * (8 * D * 32), 0, 16);
  }
  MARK(3);
}

DI void phase_spare(const u16* __restrict__ c, const u16* __restrict__ wmod, const u16* __restrict__ w2,
                    char* __restrict__ ws, char* smem, int xcd, int j, int w, int tid, int lane) {
  const int r0 = (j - 20) * 8 + xcd;
  const __amdgpu_buffer_rsrc_t sWS = mksrd(ws, 65536);
  if (r0 < 60) {
    float* sc = (float*)(smem + L_Y);
    float* red = sc + 640;
    const int col0 = 64 * r0, cl = tid & 7, rg = tid >> 3;
    u32x4 r[20];
#pragma unroll
    for (int i = 0; i < 20; i++) r[i] = *(const u32x4*)(wmod + (size_t)(rg + 32 * i) * (6 * D) + col0 + 8 * cl);
    SB0;
    for (int i = tid; i < D; i += 256) { float x = lo16(c[i]); sc[i] = rbf(x / (1.f + __expf(-x))); }
    __syncthreads();
    f32x2 a[4] = {{0.f, 0.f}, {0.f, 0.f}, {0.f, 0.f}, {0.f, 0.f}};
#pragma unroll
    for (int i = 0; i < 20; i++) {
      const float s = sc[rg + 32 * i];
#pragma unroll
      for (int e = 0; e < 4; e++) a[e] += up(r[i][e]) * (f32x2){s, s};
    }
    float af[8] = {a[0][0], a[0][1], a[1][0], a[1][1], a[2][0], a[2][1], a[3][0], a[3][1]};
#pragma unroll
    for (int e = 0; e < 8; e++) af[e] += __int_as_float(__builtin_amdgcn_update_dpp(0, __float_as_int(af[e]), 0x128, 0xf, 0xf, false));
#pragma unroll
    for (int e = 0; e < 8; e++) { auto rr = __builtin_amdgcn_permlane16_swap(__float_as_uint(af[e]), __float_as_uint(af[e]), false, false); af[e] = __uint_as_float(rr[0]) + __uint_as_float(rr[1]); }
#pragma unroll
    for (int e = 0; e < 8; e++) { auto rr = __builtin_amdgcn_permlane32_swap(__float_as_uint(af[e]), __float_as_uint(af[e]), false, false); af[e] = __uint_as_float(rr[0]) + __uint_as_float(rr[1]); }
    if (lane < 8) { *(f32x4*)(red + w * 64 + 8 * lane) = (f32x4){af[0], af[1], af[2], af[3]}; *(f32x4*)(red + w * 64 + 8 * lane + 4) = (f32x4){af[4], af[5], af[6], af[7]}; }
    __syncthreads();
    if (tid < 32) {
      const int c0 = 2 * tid;
      float s0 = rbf((red[c0] + red[64 + c0]) + (red[128 + c0] + red[192 + c0]));
      float s1 = rbf((red[c0 + 1] + red[65 + c0]) + (red[129 + c0] + red[193 + c0]));
      const int jc = col0 + c0, vv = jc / D, ch = jc - vv * D;
      if (vv == 1 || vv == 4) { s0 += 1.f; s1 += 1.f; }
      const int slot = vv == 0 ? 1 : vv == 1 ? 0 : vv == 2 ? 2 : vv == 3 ? 4 : vv == 4 ? 3 : 5;
      __builtin_amdgcn_raw_buffer_store_b32(pk2(s0, s1), sWS, WS_MODP + ((ch >> 2) * 24 + slot * 4 + (ch & 3)) * 2, 0, 16);
    }
  } else {
    const int wv = (r0 - 60) * 4 + w;
    char* mb = smem + L_Y + w * (32 * 144);
    u32x4 r[4][4];
#pragma unroll
    for (int n = 0; n < 4; n++) {
      const int u = wv + 144 * n, uc = u < 540 ? u : 539, ks = uc / 10, cc = uc - 10 * ks;
#pragma unroll
      for (int i = 0; i < 4; i++) r[n][i] = *(const u32x4*)(w2 + (size_t)(32 * ks + 8 * i + (lane >> 3)) * D + 64 * cc + 8 * (lane & 7));
    }
    SB0;
    const int q = lane >> 4, i16 = lane & 15, jj = i16 >> 2, p = i16 & 3;
    const __amdgpu_buffer_rsrc_t sO = mksrd(ws + WS_W2F, 2211840);
    const u32 a0 = ldsaddr(mb) + (4 * q + jj) * 144 + 8 * p;
#pragma unroll
    for (int n = 0; n < 4; n++) {
      const int u = wv + 144 * n, uc = u < 540 ? u : 539, ks = uc / 10, cc = uc - 10 * ks;
#pragma unroll
      for (int i = 0; i < 4; i++) *(u32x4*)(mb + (8 * i + (lane >> 3)) * 144 + 16 * (lane & 7)) = r[n][i];
      frag v[4];
#pragma unroll
      for (int f = 0; f < 4; f++) v[f] = catf(trread(a0 + 32 * f), trread(a0 + 32 * f + 16 * 144));
#pragma unroll
      for (int f = 0; f < 4; f++) {
        const int ft = 4 * cc + f;
        if (u < 540) __builtin_amdgcn_raw_buffer_store_b128(v[f], sO, (((ft / 5) * 54 + ks) * 5 + (ft % 5)) * 1024 + lane * 16, 0, 16);
      }
    }
  }
}

DI float xsum8_16_32(float x) {
  x += __int_as_float(__builtin_amdgcn_update_dpp(0, __float_as_int(x), 0x128, 0xf, 0xf, false));
  { auto r = __builtin_amdgcn_permlane16_swap(__float_as_uint(x), __float_as_uint(x), false, false); x = __uint_as_float(r[0]) + __uint_as_float(r[1]); }
  { auto r = __builtin_amdgcn_permlane32_swap(__float_as_uint(x), __float_as_uint(x), false, false); x = __uint_as_float(r[0]) + __uint_as_float(r[1]); }
  return x;
}
DI void ln_stats(const f32x2 (&x)[10], f32x2* red, int w, int lane, float& mean, float& rinv) {
  f32x2 a = ((x[0] + x[1]) + (x[2] + x[3])) + ((x[4] + x[5]) + (x[6] + x[7])) + (x[8] + x[9]);
  f32x2 b0 = x[0] * x[0], b1 = x[1] * x[1];
#pragma unroll
  for (int h = 2; h < 10; h += 2) { b0 += x[h] * x[h]; b1 += x[h + 1] * x[h + 1]; }
  b0 += b1;
  const float s1 = xsum8_16_32(a[0] + a[1]), s2 = xsum8_16_32(b0[0] + b0[1]);
  if (lane < 8) red[w * 8 + lane] = (f32x2){s1, s2};
  lds_barrier();
  const int tk = lane & 7;
  const f32x2 t = (red[tk] + red[8 + tk]) + (red[16 + tk] + red[24 + tk]);
  ln_finish(t[0], t[1], mean, rinv);
}
DI f32x2 gelu2(f32x2 x) {
  const float c0 = -2.f * 0.7978845608028654f * 1.4426950408889634f, c1 = c0 * 0.044715f;
  const f32x2 z = x * (x * x * (f32x2){c1, c1} + (f32x2){c0, c0});
  const f32x2 d = (f32x2){__builtin_amdgcn_exp2f(z[0]), __builtin_amdgcn_exp2f(z[1])} + (f32x2){1.f, 1.f};
  return x * (f32x2){__builtin_amdgcn_rcpf(d[0]), __builtin_amdgcn_rcpf(d[1])};
}
DI int rotk(int n, int rot, int m) { int k = n + rot; return k >= m ? k - m : k; }
struct W2C { u32 l16; int rot2; __amdgpu_buffer_rsrc_t srd; };
template <int I> DI void w2_load1(frag (&W2)[60], const W2C& c) {
  const u32 vo = c.l16 + rotk(I / 5, c.rot2, 14) * 5120;
  if constexpr (I % 5 < 4) bload_a_sc1<(I % 5) * 1024>(W2[I], vo, c.srd, 0); else bload_a_sc1<0>(W2[I], vo + 4096, c.srd, 0);
}
template <int I, int N> DI void w2_loads(frag (&W2)[60], const W2C& c) {
  w2_load1<I>(W2, c);
  if constexpr (N > 1) w2_loads<I + 1, N - 1>(W2, c);
}
#define W2_M0V 8
#define W2_M1V 20
#define W2_G1V 20
constexpr int W2_M0 = W2_M0V, W2_M1 = W2_M1V, W2_G1 = W2_G1V;

constexpr int G1R = 6;
struct G1S {
  frag Y[G1R][2];
  frag A0[7], A1[7];
  f32x4 acc[7][2];
};
template <int FI> DI frag g1_lda(u32 ab) { return catf(trread(ab + 32 * FI), trread(ab + 32 * FI + 1792)); }
struct G1C { u32 b0, l16, ybase; int rot1; __amdgpu_buffer_rsrc_t srdY; };
template <int KS, int FI>
DI void g1_f(G1S& s, frag (&Ac)[7], frag (&An)[7], frag (&W2)[60], const G1C& c, const W2C& c2, u32 ab, u32 yo) {
  mfma_vv(s.acc[FI][0], Ac[FI], s.Y[KS % G1R][0]);
  SB0;
  if constexpr (KS < 19) An[FI] = g1_lda<FI>(ab);
  SB0;
  mfma_vv(s.acc[FI][1], Ac[FI], s.Y[KS % G1R][1]);
  if constexpr (KS >= 1 && KS - 1 + G1R < 20 && (FI == 1 || FI == 4)) bload_sc1<(FI == 4) * 1024>(s.Y[(KS - 1) % G1R][FI == 4], c.l16, c.srdY, yo);
  if constexpr (KS < W2_G1 && FI == 6) w2_load1<W2_M0 + W2_M1 + KS>(W2, c2);
  SB0;
  if constexpr (FI < 6) g1_f<KS, FI + 1>(s, Ac, An, W2, c, c2, ab, yo);
}
template <int KS> constexpr int g1_younger() {
  int cnt = 0; bool seen = false;
  for (int k = 0; k < G1R; k++) { if (seen) cnt += 2; if (k == KS) seen = true; }
  for (int n = 0; n < KS; n++) {
    if (n >= 1 && n - 1 + G1R < 20) { if (seen) cnt += 2; if (n - 1 + G1R == KS) seen = true; }
    if (n < W2_G1 && seen) cnt += 1;
  }
  return cnt;
}
template <int KS>
DI void g1_step(G1S& s, frag (&W2)[60], const G1C& c, const W2C& c2) {
  waitv2<g1_younger<KS>()>(s.Y[KS % G1R][0], s.Y[KS % G1R][1]);
  const u32 ab = c.b0 + rotk(KS + 1, c.rot1, 20) * 7168;
  const u32 yo = c.ybase + rotk(KS - 1 + G1R, c.rot1, 20) * 2048;
  if constexpr (KS & 1) g1_f<KS, 0>(s, s.A1, s.A0, W2, c, c2, ab, yo);
  else                  g1_f<KS, 0>(s, s.A0, s.A1, W2, c, c2, ab, yo);
  if constexpr (KS < 19) g1_step<KS + 1>(s, W2, c, c2);
}
struct G2S {
  frag H[4][4];
  frag WX[10];
  f32x4 acc[5][4];
  u32x2 xx[5], gg[5], b2v[5];
};
struct G2C { u32 l16, vx, vb2, vg; int rot2; __amdgpu_buffer_rsrc_t srdH, srdX, srdB2, srdMD; };
template <int N, int T> DI void g2_hload1(G2S& s, const G2C& c, u32 vo) { bload_sc1<T * 1024>(s.H[N & 3][T], vo, c.srdH, 0); }
template <int N> DI void g2_hload(G2S& s, const G2C& c) {
  const u32 vo = c.l16 + rotk(N, c.rot2, 14) * 4096;
  g2_hload1<N, 0>(s, c, vo); g2_hload1<N, 1>(s, c, vo); g2_hload1<N, 2>(s, c, vo); g2_hload1<N, 3>(s, c, vo);
}
template <int I> DI void g2_eload(G2S& s, const G2C& c) {
  if constexpr (I < 5) bload2_sc1<32 * I>(s.xx[I], c.vx, c.srdX, 0);
  else if constexpr (I < 10) bload2<32 * (I - 5)>(s.b2v[I - 5], c.vb2, c.srdB2, 0);
  else bload2_sc1<192 * (I - 10)>(s.gg[I - 10], c.vg, c.srdMD, 0);
}
template <int N, int M>
DI void g2_m(G2S& s, frag (&W2)[60], const G2C& c, u32 vo) {
  constexpr int d = M >> 2, t = M & 3;
  if constexpr (N == 0) mfma_av0(s.acc[d][t], W2[d], s.H[0][t]);
  else if constexpr (N < 12) mfma_av(s.acc[d][t], W2[N * 5 + d], s.H[N & 3][t]);
  else mfma_vv(s.acc[d][t], s.WX[(N - 12) * 5 + d], s.H[N & 3][t]);
  if constexpr (N + 3 < 14 && (M == 1 || M == 6 || M == 11 || M == 16)) g2_hload1<N + 3, (M - 1) / 5>(s, c, vo);
  if constexpr (N >= 11 && (M == 1 || M == 5 || M == 9 || M == 13 || M == 17)) g2_eload<5 * (N - 11) + (M - 1) / 4>(s, c);
  if constexpr (M < 19) g2_m<N, M + 1>(s, W2, c, vo);
}
template <int N> constexpr int g2_younger() {
  int cnt = 0; bool seen = false;
  for (int h = 0; h < 3; h++) { if (seen) cnt += 4; if (h == N) seen = true; }
  for (int n = 0; n < N; n++) {
    if (n + 3 < 14) { if (seen) cnt += 4; if (n + 3 == N) seen = true; }
    if (n >= 11 && seen) cnt += 5;
  }
  return cnt;
}
static_assert(g2_younger<0>() == 8 && g2_younger<3>() == 8 && g2_younger<11>() == 8 && g2_younger<12>() == 9 && g2_younger<13>() == 10, "");
template <int N>
DI void g2_step(G2S& s, frag (&W2)[60], const G2C& c) {
  waitv4<g2_younger<N>()>(s.H[N & 3][0], s.H[N & 3][1], s.H[N & 3][2], s.H[N & 3][3]);
  const u32 vo = c.l16 + rotk(N + 3, c.rot2, 14) * 4096;
  g2_m<N, 0>(s, W2, c, vo);
  if constexpr (N < 13) g2_step<N + 1>(s, W2, c);
}

__global__ __launch_bounds__(256, 1) void k_main(const u16* __restrict__ qin, const u16* __restrict__ kin, const u16* __restrict__ vin,
                                                 const u16* __restrict__ shk, const u16* __restrict__ ssk, const u16* __restrict__ ssD,
                                                 const u16* __restrict__ cin, const u16* __restrict__ wmod, const u16* __restrict__ w1,
                                                 const u16* __restrict__ b1, const u16* __restrict__ w2, const u16* __restrict__ b2,
                                                 char* __restrict__ ws, u16* __restrict__ out) {
  STAMP_BEGIN(ws)
  MARK_DECL
  extern __shared__ __attribute__((aligned(16))) char smem[];
  const int tid = threadIdx.x, lane = tid & 63, w = rfl(tid >> 6);
  const int b = blockIdx.x, xcd = b & 7, j = b >> 3;
  const int tile = 4 * (xcd >> 1) + (j >> 3), g = 8 * (xcd & 1) + (j & 7);
  const int TT = 4 * xcd + (j >> 3), cg = j & 7;
  const u32 l16 = lane * 16, lds0 = ldsaddr(smem);
  u32 wu[3];
  wu[0] = wu[1] = wu[2] = 0;
  {
    unsigned long long pc; u32 len;
    asm volatile("s_getpc_b64 %0\n.Lkmain_here:\n\ts_mov_b32 %1, .Lkmain_end-.Lkmain_here" : "=s"(pc), "=s"(len));
    const u32 off = (j + 32 * lane) * 128;
    if (w == 3 && off < len) asm volatile("global_load_dword %0, %1, %2" : "=v"(wu[0]) : "v"(off), "s"(pc));
  }
  if (w == 2 && j < 2) {
    const char* p = ws + (size_t)(lane < 13 ? lane : 0) * 2097152;
    if (lane == 13 || lane == 14) p = (const char*)out + (lane - 13) * 2097152;
    if (lane == 15 || lane == 16) p = (const char*)w1 + (lane - 15) * 2097152;
    if (lane == 17 || lane == 18) p = (const char*)w2 + (lane - 17) * 2097152;
    if (lane >= 19 && lane <= 21) p = (const char*)wmod + (lane - 19) * 2097152;
    if (lane == 22) p = (const char*)b1;
    if (lane == 23) p = (const char*)b2;
    if (lane == 24) p = (const char*)cin;
    if (lane < 25) asm volatile("global_load_dword %0, %1, off" : "=v"(wu[1]) : "v"(p));
  }
  W1C wc;
#pragma unroll
  for (int r = 0; r < 7; r++) { const u32 idx = 64 * r + lane, row = (idx * 4682u) >> 16; wc.vo[r] = row * (F * 2 - 224) + idx * 16; }
  wc.m0b = lds0 + L_W1 + w * 7168; wc.sob = w * (32 * F * 2) + 224 * g; wc.srd = mksrd(w1, D * F * 2);
  const int tg = 16 * tile + g, t0 = 8 * tg;
  const int tk = lane & 7, slot = lane >> 3, q = lane >> 4;
  u32x2 xr[5];
  {
    const __amdgpu_buffer_rsrc_t srdQ = mksrd(qin + (size_t)t0 * D, 10240);
    const u32 vq = tk * 1280 + (40 * w + slot) * 8;
    asm volatile("s_nop 4");
    bload2<0>(xr[0], vq, srdQ, 0); bload2<64>(xr[1], vq, srdQ, 0); bload2<128>(xr[2], vq, srdQ, 0); bload2<192>(xr[3], vq, srdQ, 0); bload2<256>(xr[4], vq, srdQ, 0);
  }
  const u64 tag = call_tag(__hip_atomic_load((const GAS u32*)(ws + WS_EPOCH), __ATOMIC_RELAXED, __HIP_MEMORY_SCOPE_AGENT));
  u32x4 rk[4];
  u32 Draw = 0;
  if (j < 20) {
    const int ch = 80 * xcd + 4 * j + w;
    const __amdgpu_buffer_rsrc_t sF = mksrd(ssk + (size_t)ch * S, S * 2);
#pragma unroll
    for (int i = 0; i < 4; i++) rk[i] = __builtin_amdgcn_raw_buffer_load_b128(sF, l16 + 1024 * i, 0, 0);
    Draw = __builtin_amdgcn_raw_buffer_load_b32(mksrd(ssD, D * 2), (ch >> 1) * 4, 0, 0);
  }
  phase_prep(kin, vin, shk, ws, smem, xcd, j, tid, lane, w);
  asm volatile("s_waitcnt vmcnt(0)" : "+v"(wu[0]), "+v"(wu[1]) :: "memory");
  __builtin_amdgcn_s_barrier();
  if (tid == 0) signal(ws, WS_FU + (xcd * 32 + j) * 64, tag);
  MARK(23);
  if (j < 20) {
    phase_conv(rk, Draw, ws, smem, xcd, j, w, lane, tag, wc
    );
  } else {
    phase_spare(cin, wmod, w2, ws, smem, xcd, j, w, tid, lane);
  }
  asm volatile("s_waitcnt vmcnt(0)" : "+v"(xr[0]), "+v"(xr[1]), "+v"(xr[2]), "+v"(xr[3]), "+v"(xr[4]) :: "memory");
  __builtin_amdgcn_s_barrier();
  MARK(4);
  if (tid < 8) {
    signal(ws, WS_FG + tid * 2048 + b * 8, tag);
    const int r0 = (j - 20) * 8 + xcd;
    if (r0 >= 60) signal(ws, WS_FW + tid * 512 + (r0 - 60) * 8, tag);
  }
  const int ks0 = w < 2 ? 14 * w : 28 + 13 * (w - 2), nks = w < 2 ? 14 : 13;
  W2C c2;
  c2.l16 = l16; c2.srd = mksrd(ws + WS_W2F + (size_t)cg * 276480 + ks0 * 5120, nks * 5120);
  { int r2 = 2 * cg + 4 * (j >> 3); r2 = r2 >= 14 ? r2 - 14 : r2; c2.rot2 = r2 >= 14 ? r2 - 14 : r2; }
  frag W2[60];
  f32x2 x[10], t[10];
  f32x2* red = (f32x2*)(smem + L_RED);
  {
    if (j >= 20) { w1_step<0>(wc); w1_step<1>(wc); }
#pragma unroll
    for (int i = 0; i < 5; i++) { x[2 * i] = up(xr[i][0]); x[2 * i + 1] = up(xr[i][1]); }
    if (w == 0) poll_tags(ws + WS_FW + xcd * 512, 8, 36, tag, lane);
    float mean, rinv;
    ln_stats(x, red + 64, w, lane, mean, rinv);
    const f32x2 mm = {mean, mean}, rr = {rinv, rinv};
#pragma unroll
    for (int h = 0; h < 10; h++) t[h] = rb2(x[h] - mm);
#pragma unroll
    for (int h = 0; h < 10; h++) t[h] = rb2(t[h] * rr);
#pragma unroll
    for (int h = 0; h < 10; h++) asm volatile("" : "+v"(x[h]), "+v"(t[h]));
  }
  if (j >= 20) { w1_step<2>(wc); w1_step<3>(wc); }
  w2_loads<0, W2_M0>(W2, c2);
  if (w == 0) poll_tags256(ws + WS_FG + xcd * 2048, tag, lane);
  __builtin_amdgcn_s_barrier();
  if (b == 0 && tid == 0) __hip_atomic_store((GAS u32*)(ws + WS_EPOCH), (u32)tag, __ATOMIC_RELAXED, __HIP_MEMORY_SCOPE_AGENT);
  MARK(5);
  const __amdgpu_buffer_rsrc_t srdMD = mksrd(ws + WS_MODP, 7680);
  u32x2 bb[7];
  {
    const __amdgpu_buffer_rsrc_t srdYB = mksrd(ws + WS_Y2B + (size_t)(tg >> 1) * 20480, 20480);
    const __amdgpu_buffer_rsrc_t srdB1 = mksrd(b1, F * 2);
    u32x4 yr[3], mr[2];
    const u32 v16 = tid * 16, vb = (16 * 7 * g + 4 * q) * 2;
    asm volatile("s_nop 4");
    MARK(20);
    const u32 vy = tid * 32 + 16 * (tg & 1);
    bload_sc1<0>(yr[0], vy, srdYB, 0); bload_sc1<0>(yr[1], vy + 8192, srdYB, 0); bload_sc1<0>(yr[2], vy + 16384, srdYB, 0);
    bload_sc1<0>(mr[0], v16, srdMD, 0); bload_sc1<0>(mr[1], v16 + 4096, srdMD, 0);
    w1_step<4>(wc);
    asm volatile("s_waitcnt vmcnt(7)" : "+v"(yr[0]), "+v"(yr[1]), "+v"(yr[2]), "+v"(mr[0]), "+v"(mr[1]));
    MARK(19);
    *(u32x4*)(smem + L_Y + v16) = yr[0]; *(u32x4*)(smem + L_Y + 4096 + v16) = yr[1];
    if (tid < 128) *(u32x4*)(smem + L_Y + 8192 + v16) = yr[2];
    *(u32x4*)(smem + L_MOD + v16) = mr[0]; *(u32x4*)(smem + L_MOD + 4096 + v16) = mr[1];
    lds_barrier();
    MARK(6);
    const int i16 = lane & 15, jj = i16 >> 2, p = i16 & 3;
    const u32 ya = lds0 + L_Y + (4 * (40 * w + 2 * q + (p >> 1)) + jj) * 16 + 8 * (p & 1);
    const char* mdp = smem + L_MOD + (40 * w + slot) * 48;
    u32 yy[10], w1v[10], sav[10], gav[10];
#pragma unroll
    for (int i = 0; i < 5; i++) {
      const u32x2 yu = __builtin_bit_cast(u32x2, trread(ya + i * 8 * 64));
      const u32x4 m0 = *(const u32x4*)(mdp + i * 384);
      const u32x2 m1 = *(const u32x2*)(mdp + i * 384 + 16);
      yy[2 * i] = yu[0]; yy[2 * i + 1] = yu[1]; w1v[2 * i] = m0[0]; w1v[2 * i + 1] = m0[1]; sav[2 * i] = m0[2]; sav[2 * i + 1] = m0[3]; gav[2 * i] = m1[0]; gav[2 * i + 1] = m1[1];
    }
    u32 o[10];
#define STAGE(EXPR) _Pragma("unroll") for (int h = 0; h < 10; h++) { EXPR; }
    STAGE(t[h] = rb2(t[h] * up(w1v[h])))
    STAGE(t[h] = rb2(t[h] + up(sav[h])))
    STAGE(t[h] = rb2(t[h] * up(yy[h])))
    STAGE(t[h] = rb2(up(gav[h]) * t[h]))
    STAGE(o[h] = pk(x[h] + t[h]))
    const __amdgpu_buffer_rsrc_t srdX1 = mksrd(ws + WS_X1, 2621440);
    const u32 xo = (t0 + tk) * 1280 + (40 * w + slot) * 8;
#pragma unroll
    for (int i = 0; i < 5; i++) __builtin_amdgcn_raw_buffer_store_b64((u32x2){o[2 * i], o[2 * i + 1]}, srdX1, xo + i * 64, 0, 16);
    STAGE(x[h] = up(o[h]))
    MARK(7);
    float mean, rinv;
    SB0;
    ln_stats(x, red, w, lane, mean, rinv);
    {
      const f32x2 m2 = {mean, mean}, r2 = {rinv, rinv};
#pragma unroll
      for (int i = 0; i < 5; i++) {
        const u32x4 m0 = *(const u32x4*)(mdp + i * 384 + 16);
        const u32x2 m1 = *(const u32x2*)(mdp + i * 384 + 32);
        x[2 * i] = (x[2 * i] - m2) * r2 * up(m0[2]) + up(m1[0]);
        x[2 * i + 1] = (x[2 * i + 1] - m2) * r2 * up(m0[3]) + up(m1[1]);
      }
    }
    MARK(8);
    SB0;
    {
      bload2<0>(bb[0], vb, srdB1, 0); bload2<32>(bb[1], vb, srdB1, 0); bload2<64>(bb[2], vb, srdB1, 0); bload2<96>(bb[3], vb, srdB1, 0);
      bload2<128>(bb[4], vb, srdB1, 0); bload2<160>(bb[5], vb, srdB1, 0); bload2<192>(bb[6], vb, srdB1, 0);
    }
    SB0;
    ln_stats(x, red + 32, w, lane, mean, rinv);
    {
      const f32x2 m3 = {mean, mean}, r3 = {rinv, rinv};
      STAGE(o[h] = pk((x[h] - m3) * r3))
      const int rt = tg >> 1, r = 8 * (tg & 1) + tk;
      const __amdgpu_buffer_rsrc_t srdY3 = mksrd(ws + WS_Y3F, 2621440);
      const u32 yo = (rt >> 1) * 40960 + (rt & 1) * 1024 + ((2 * (slot >> 2) + (slot & 1)) * 16 + r) * 16 + 8 * ((slot >> 1) & 1) + 5 * w * 2048;
#pragma unroll
      for (int i = 0; i < 5; i++) __builtin_amdgcn_raw_buffer_store_b64((u32x2){o[2 * i], o[2 * i + 1]}, srdY3, yo + i * 2048, 0, 16);
    }
#undef STAGE
  }
  MARK(9);
  asm volatile("s_waitcnt vmcnt(0)" : "+v"(bb[0]), "+v"(bb[1]), "+v"(bb[2]), "+v"(bb[3]), "+v"(bb[4]), "+v"(bb[5]), "+v"(bb[6]) :: "memory");
  __builtin_amdgcn_s_barrier();
  if (tid == 0) signal(ws, WS_FA + (tile * 16 + g) * 64, tag);
  MARK(10);
  w2_loads<W2_M0, W2_M1>(W2, c2);
  if (w == 0) poll_tags(ws + WS_FA + tile * 1024, 64, 16, tag, lane);
  __builtin_amdgcn_s_barrier();
  MARK(11);
  f32x4 hacc[7][2];
  {
    G1C c1;
    const int i16 = lane & 15, jj = i16 >> 2, p = i16 & 3;
    c1.srdY = mksrd(ws + WS_Y3F, 2621440); c1.l16 = l16;
    c1.rot1 = (5 * (j & 7)) >> 1;
    c1.ybase = (tile * 4 + w) * 40960;
    c1.b0 = lds0 + L_W1 + (16 * (q >> 1) + 4 * (q & 1) + jj) * 224 + 8 * p;
    G1S s;
    asm volatile("s_nop 4");
#pragma unroll
    for (int k = 0; k < G1R; k++) { const u32 yo = c1.ybase + rotk(k, c1.rot1, 20) * 2048; bload_sc1<0>(s.Y[k][0], l16, c1.srdY, yo); bload_sc1<1024>(s.Y[k][1], l16, c1.srdY, yo); }
#pragma unroll
    for (int f = 0; f < 7; f++) {
      const f32x4 bv = {lo16(bb[f][0]), hi16(bb[f][0]), lo16(bb[f][1]), hi16(bb[f][1])};
      s.acc[f][0] = bv; s.acc[f][1] = bv;
      asm volatile("" : "+v"(s.acc[f][0]), "+v"(s.acc[f][1]));
    }
    const u32 ab0 = c1.b0 + c1.rot1 * 7168;
    s.A0[0] = g1_lda<0>(ab0); s.A0[1] = g1_lda<1>(ab0); s.A0[2] = g1_lda<2>(ab0); s.A0[3] = g1_lda<3>(ab0);
    s.A0[4] = g1_lda<4>(ab0); s.A0[5] = g1_lda<5>(ab0); s.A0[6] = g1_lda<6>(ab0);
    SB0;
    g1_step<0>(s, W2, c1, c2);
    asm volatile("s_nop 7\n\ts_nop 1");
#pragma unroll
    for (int f = 0; f < 7; f++) { asm volatile("" : "+v"(s.acc[f][0]), "+v"(s.acc[f][1])); hacc[f][0] = s.acc[f][0]; hacc[f][1] = s.acc[f][1]; }
  }
  MARK(12);
  G2S s2_;
  {
    const __amdgpu_buffer_rsrc_t srdH = mksrd(ws + WS_HF, 7077888);
    const int rt0 = 8 * tile + 2 * w;
    const u32 ho = ((rt0 >> 2) * 54 * 4 + (rt0 & 3)) * 1024 + l16 + ((7 * g) >> 1) * 4096;
    auto gel = [&](int f, int tt) -> u32x2 {
      const f32x4 a = hacc[f][tt];
      return (u32x2){pk(gelu2((f32x2){a[0], a[1]})), pk(gelu2((f32x2){a[2], a[3]}))};
    };
    if (g & 1) {
      { const u32x2 a = gel(0, 0), c = gel(0, 1);
        __builtin_amdgcn_raw_buffer_store_b64(a, srdH, ho + 8, 0, 16); __builtin_amdgcn_raw_buffer_store_b64(c, srdH, ho + 1024 + 8, 0, 16); }
#pragma unroll
      for (int i = 0; i < 3; i++) {
        const u32x2 a0 = gel(2 * i + 1, 0), a1 = gel(2 * i + 2, 0), c0 = gel(2 * i + 1, 1), c1 = gel(2 * i + 2, 1);
        if (7 * g + 2 * i + 2 < 108) {
          __builtin_amdgcn_raw_buffer_store_b128((u32x4){a0[0], a0[1], a1[0], a1[1]}, srdH, ho + (1 + i) * 4096, 0, 16);
          __builtin_amdgcn_raw_buffer_store_b128((u32x4){c0[0], c0[1], c1[0], c1[1]}, srdH, ho + (1 + i) * 4096 + 1024, 0, 16);
        }
      }
    } else {
#pragma unroll
      for (int i = 0; i < 3; i++) {
        const u32x2 a0 = gel(2 * i, 0), a1 = gel(2 * i + 1, 0), c0 = gel(2 * i, 1), c1 = gel(2 * i + 1, 1);
        __builtin_amdgcn_raw_buffer_store_b128((u32x4){a0[0], a0[1], a1[0], a1[1]}, srdH, ho + i * 4096, 0, 16);
        __builtin_amdgcn_raw_buffer_store_b128((u32x4){c0[0], c0[1], c1[0], c1[1]}, srdH, ho + i * 4096 + 1024, 0, 16);
      }
      { const u32x2 a = gel(6, 0), c = gel(6, 1);
        __builtin_amdgcn_raw_buffer_store_b64(a, srdH, ho + 3 * 4096, 0, 16); __builtin_amdgcn_raw_buffer_store_b64(c, srdH, ho + 3 * 4096 + 1024, 0, 16); }
    }
  }
  MARK(13);
  waitv<0>();
  __builtin_amdgcn_s_barrier();
  if (tid == 0) signal(ws, WS_FB + (tile * 16 + g) * 64, tag);
  {
    const u32 vo = l16 + rotk(12, c2.rot2, 14) * 5120, v1 = l16 + rotk(13, c2.rot2, 14) * 5120;
    if constexpr (W2_M0 + W2_M1 + W2_G1 < 60) w2_loads<W2_M0 + W2_M1 + W2_G1, 60 - W2_M0 - W2_M1 - W2_G1>(W2, c2);
    bload_sc1<0>(s2_.WX[0], vo, c2.srd, 0); bload_sc1<1024>(s2_.WX[1], vo, c2.srd, 0); bload_sc1<2048>(s2_.WX[2], vo, c2.srd, 0); bload_sc1<3072>(s2_.WX[3], vo, c2.srd, 0);
    bload_sc1<0>(s2_.WX[4], vo + 4096, c2.srd, 0);
    bload_sc1<0>(s2_.WX[5], v1, c2.srd, 0); bload_sc1<1024>(s2_.WX[6], v1, c2.srd, 0); bload_sc1<2048>(s2_.WX[7], v1, c2.srd, 0); bload_sc1<3072>(s2_.WX[8], v1, c2.srd, 0);
    bload_sc1<0>(s2_.WX[9], v1 + 4096, c2.srd, 0);
  }
  MARK(14);
  const int cc = lane & 15, tok = 64 * TT + 16 * w + cc;
  G2C cg2;
  cg2.l16 = l16; cg2.rot2 = c2.rot2;
  cg2.srdH = mksrd(ws + WS_HF + (size_t)TT * 221184 + ks0 * 4096, nks * 4096);
  cg2.srdX = mksrd(ws + WS_X1, 2621440); cg2.srdB2 = mksrd(b2, D * 2); cg2.srdMD = srdMD;
  cg2.vx = tok * 1280 + (80 * cg + 4 * q) * 2; cg2.vb2 = (80 * cg + 4 * q) * 2; cg2.vg = (20 * cg + q) * 48 + 40;
  if (w == 0) poll_tags(ws + WS_FB + (TT >> 1) * 1024, 64, 16, tag, lane);
  __builtin_amdgcn_s_barrier();
  MARK(15);
  asm volatile("s_nop 4");
  g2_hload<0>(s2_, cg2); g2_hload<1>(s2_, cg2); g2_hload<2>(s2_, cg2);
  asm volatile("s_waitcnt vmcnt(12)" : "+v"(s2_.WX[0]), "+v"(s2_.WX[1]), "+v"(s2_.WX[2]), "+v"(s2_.WX[3]), "+v"(s2_.WX[4]), "+v"(s2_.WX[5]), "+v"(s2_.WX[6]), "+v"(s2_.WX[7]), "+v"(s2_.WX[8]), "+v"(s2_.WX[9]));
  MARK(21);
  g2_step<0>(s2_, W2, cg2);
  asm volatile("s_nop 7\n\ts_nop 1");
#pragma unroll
  for (int d = 0; d < 5; d++) asm volatile("" : "+v"(s2_.acc[d][0]), "+v"(s2_.acc[d][1]), "+v"(s2_.acc[d][2]), "+v"(s2_.acc[d][3]));
  MARK(16);
  f32x4* redq = (f32x4*)smem;
  f32x4 own[5];
#define G2_OWN(T) { _Pragma("unroll") for (int d = 0; d < 5; d++) { own[d] = s2_.acc[d][T]; \
    _Pragma("unroll") for (int t = 0; t < 4; t++) if (t != T) redq[((T * 4 + t) * 5 + d) * 64 + lane] = s2_.acc[d][t]; } }
  if (w == 0) G2_OWN(0) else if (w == 1) G2_OWN(1) else if (w == 2) G2_OWN(2) else G2_OWN(3)
#undef G2_OWN
  lds_barrier();
  asm volatile("s_waitcnt vmcnt(0)" : "+v"(s2_.xx[0]), "+v"(s2_.xx[1]), "+v"(s2_.xx[2]), "+v"(s2_.xx[3]), "+v"(s2_.xx[4]), "+v"(s2_.b2v[0]), "+v"(s2_.b2v[1]), "+v"(s2_.b2v[2]), "+v"(s2_.b2v[3]), "+v"(s2_.b2v[4]),
               "+v"(s2_.gg[0]), "+v"(s2_.gg[1]), "+v"(s2_.gg[2]), "+v"(s2_.gg[3]), "+v"(s2_.gg[4]));
  MARK(17);
  {
    const f32x4* r1 = redq + ((((w + 1) & 3) * 4 + w) * 5) * 64 + lane;
    const f32x4* r2 = redq + ((((w + 2) & 3) * 4 + w) * 5) * 64 + lane;
    const f32x4* r3 = redq + ((((w + 3) & 3) * 4 + w) * 5) * 64 + lane;
#pragma unroll
    for (int d = 0; d < 5; d++) {
      const f32x4 sacc = (own[d] + r1[d * 64]) + (r2[d * 64] + r3[d * 64]);
      u32x2 o;
#pragma unroll
      for (int h = 0; h < 2; h++) {
        const f32x2 y = rb2(rb2((f32x2){sacc[2 * h], sacc[2 * h + 1]}) + up(s2_.b2v[d][h]));
        o[h] = pk(up(s2_.xx[d][h]) + rb2(up(s2_.gg[d][h]) * y));
      }
      *(u32x2*)(out + (size_t)tok * D + 80 * cg + 16 * d + 4 * q) = o;
    }
  }
  asm volatile(".Lkmain_end:");
  MARK(18);
  MARK_FLUSH(ws, 0, 24);
  STAMP_END(ws, 0);
}

extern "C" void kernel_launch(void* const* d_in, const int* in_sizes, int n_in, void* d_out, int out_size, void* d_ws, size_t ws_size,
                              hipStream_t stream) {
  const u16* q = (const u16*)d_in[0]; const u16* k = (const u16*)d_in[1]; const u16* v = (const u16*)d_in[2];
  const u16* shk = (const u16*)d_in[3]; const u16* ssk = (const u16*)d_in[4]; const u16* ssD = (const u16*)d_in[5];
  const u16* w1 = (const u16*)d_in[7]; const u16* b1 = (const u16*)d_in[8]; const u16* w2 = (const u16*)d_in[9];
  const u16* b2 = (const u16*)d_in[10]; const u16* c = (const u16*)d_in[11]; const u16* wmod = (const u16*)d_in[12];
  char* ws = (char*)d_ws;
  (void)hipFuncSetAttribute((const void*)k_main, hipFuncAttributeMaxDynamicSharedMemorySize, L_TOTAL);
  k_main<<<256, 256, L_TOTAL, stream>>>(q, k, v, shk, ssk, ssD, c, wmod, w1, b1, w2, b2, ws, (u16*)d_out);
}
```

```cpp
#include <hip/hip_runtime.h>
#include <stdint.h>

typedef unsigned short u16;
typedef unsigned int u32;
typedef short s16x4 __attribute__((ext_vector_type(4)));
typedef short s16x8 __attribute__((ext_vector_type(8)));
typedef __bf16 bf16x8 __attribute__((ext_vector_type(8)));
typedef __bf16 bf16x2 __attribute__((ext_vector_type(2)));
typedef float f32x4 __attribute__((ext_vector_type(4)));
typedef float f32x2 __attribute__((ext_vector_type(2)));
typedef u32 u32x4 __attribute__((ext_vector_type(4)));
typedef u32 u32x2 __attribute__((ext_vector_type(2)));
#define LAS __attribute__((address_space(3)))
#define GAS __attribute__((address_space(1)))
#define DI __device__ __forceinline__

constexpr int S = 2048, D = 640, F = 1728;
constexpr size_t WS_MODP = 0;
constexpr size_t WS_EPOCH = 8192;
constexpr size_t WS_FU = 16384;
constexpr size_t WS_FA = WS_FU + 16384;
constexpr size_t WS_FB = WS_FA + 16384;
constexpr size_t WS_FG = WS_FB + 16384;
constexpr size_t WS_FW = WS_FG + 16384;
constexpr size_t WS_U    = 131072;
constexpr size_t WS_Y2B  = WS_U + 2621440;
constexpr size_t WS_X1   = WS_Y2B + 2621440;
constexpr size_t WS_Y3F  = WS_X1 + 2621440;
constexpr size_t WS_HF   = WS_Y3F + 2621440;
constexpr size_t WS_W2F  = WS_HF + 7077888;

#define STAMP_BEGIN(ws)
#define STAMP_END(ws, kid)
#define MARK_DECL
#define MARK(i)
#define MARK_FLUSH(ws, kid, n)

DI float lo16(u32 x) { return __uint_as_float(x << 16); }
DI float hi16(u32 x) { return __uint_as_float(x & 0xffff0000u); }
DI u32 pk(f32x2 v) { return __builtin_bit_cast(u32, __builtin_convertvector(v, bf16x2)); }
DI u32 pk2(float a, float b) { return pk((f32x2){a, b}); }
DI f32x2 up(u32 x) { return (f32x2){lo16(x), hi16(x)}; }
DI f32x2 rb2(f32x2 v) { return up(pk(v)); }
DI float rbf(float a) { return lo16(pk2(a, a)); }
DI int rfl(int x) { return __builtin_amdgcn_readfirstlane(x); }
DI s16x4 trread(u32 ldsaddr) { return __builtin_amdgcn_ds_read_tr16_b64_v4i16((LAS s16x4*)(size_t)ldsaddr); }
DI bf16x8 cat8(s16x4 a, s16x4 b) { return __builtin_bit_cast(bf16x8, __builtin_shufflevector(a, b, 0, 1, 2, 3, 4, 5, 6, 7)); }
DI f32x4 mfma16(bf16x8 a, bf16x8 b, f32x4 c) { return __builtin_amdgcn_mfma_f32_16x16x32_bf16(a, b, c, 0, 0, 0); }
DI u32 ldsaddr(const void* p) { return (u32)(size_t)(LAS const char*)p; }
DI __amdgpu_buffer_rsrc_t mksrd(const void* p, u32 bytes) { return __builtin_amdgcn_make_buffer_rsrc((void*)p, 0, bytes, 0x00020000); }

[[maybe_unused]] DI float wsum64(float x) {
  x += __int_as_float(__builtin_amdgcn_update_dpp(0, __float_as_int(x), 0xB1, 0xf, 0xf, false));
  x += __int_as_float(__builtin_amdgcn_update_dpp(0, __float_as_int(x), 0x4E, 0xf, 0xf, false));
  x += __int_as_float(__builtin_amdgcn_update_dpp(0, __float_as_int(x), 0x141, 0xf, 0xf, false));
  x += __int_as_float(__builtin_amdgcn_update_dpp(0, __float_as_int(x), 0x128, 0xf, 0xf, false));
  { auto r = __builtin_amdgcn_permlane16_swap(__float_as_uint(x), __float_as_uint(x), false, false); x = __uint_as_float(r[0]) + __uint_as_float(r[1]); }
  { auto r = __builtin_amdgcn_permlane32_swap(__float_as_uint(x), __float_as_uint(x), false, false); x = __uint_as_float(r[0]) + __uint_as_float(r[1]); }
  return x;
}
DI void ln_finish(float sum, float sq, float& mean, float& rinv) {
  const float m = sum * (1.f / D);
  const float var = fmaxf(sq * (1.f / D) - m * m, 0.f);
  mean = rbf(m);
  rinv = __builtin_amdgcn_rcpf(rbf(__builtin_amdgcn_sqrtf(rbf(rbf(var) + 1e-5f))));
}

template <int IMM> DI void bload(u32x4& dst, u32 voff, __amdgpu_buffer_rsrc_t srd, u32 soff) {
  asm volatile("buffer_load_dwordx4 %0, %1, %2, %3 offen offset:%c4" : "=v"(dst) : "v"(voff), "s"(srd), "s"(soff), "i"(IMM));
}
template <int IMM> DI void bload_sc1(u32x4& dst, u32 voff, __amdgpu_buffer_rsrc_t srd, u32 soff) {
  asm volatile("buffer_load_dwordx4 %0, %1, %2, %3 offen offset:%c4 sc1" : "=v"(dst) : "v"(voff), "s"(srd), "s"(soff), "i"(IMM));
}
template <int IMM> DI void bload_a(u32x4& dst, u32 voff, __amdgpu_buffer_rsrc_t srd, u32 soff) {
  asm volatile("buffer_load_dwordx4 %0, %1, %2, %3 offen offset:%c4" : "=a"(dst) : "v"(voff), "s"(srd), "s"(soff), "i"(IMM));
}
template <int IMM> DI void bload_a_sc1(u32x4& dst, u32 voff, __amdgpu_buffer_rsrc_t srd, u32 soff) {
  asm volatile("buffer_load_dwordx4 %0, %1, %2, %3 offen offset:%c4 sc1" : "=a"(dst) : "v"(voff), "s"(srd), "s"(soff), "i"(IMM));
}
DI void touch(u32& dst, u32 voff, __amdgpu_buffer_rsrc_t srd) {
  asm volatile("buffer_load_dword %0, %1, %2, 0 offen sc1" : "=v"(dst) : "v"(voff), "s"(srd));
}
template <int IMM> DI void bload2_sc1(u32x2& dst, u32 voff, __amdgpu_buffer_rsrc_t srd, u32 soff) {
  asm volatile("buffer_load_dwordx2 %0, %1, %2, %3 offen offset:%c4 sc1" : "=v"(dst) : "v"(voff), "s"(srd), "s"(soff), "i"(IMM));
}
template <int IMM> DI void bload2(u32x2& dst, u32 voff, __amdgpu_buffer_rsrc_t srd, u32 soff) {
  asm volatile("buffer_load_dwordx2 %0, %1, %2, %3 offen offset:%c4" : "=v"(dst) : "v"(voff), "s"(srd), "s"(soff), "i"(IMM));
}
DI void dma16(u32 m0v, u32 voff, __amdgpu_buffer_rsrc_t srd, u32 soff) {
  asm volatile("s_mov_b32 m0, %0\n\ts_nop 0\n\tbuffer_load_dwordx4 %1, %2, %3 offen lds" ::"s"(m0v), "v"(voff), "s"(srd), "s"(soff) : "m0");
}
DI void dma4(u32 m0v, u32 voff, __amdgpu_buffer_rsrc_t srd, u32 soff) {
  asm volatile("s_mov_b32 m0, %0\n\ts_nop 0\n\tbuffer_load_dword %1, %2, %3 offen lds" ::"s"(m0v), "v"(voff), "s"(srd), "s"(soff) : "m0");
}
template <int IMM> DI void dsread64(u32x2& dst, u32 addr) { asm volatile("ds_read_b64 %0, %1 offset:%c2" : "=v"(dst) : "v"(addr), "i"(IMM)); }
typedef u32x4 frag;
DI void mfma_vv(f32x4& acc, const frag& a, const frag& b) { asm volatile("v_mfma_f32_16x16x32_bf16 %0, %1, %2, %0" : "+v"(acc) : "v"(a), "v"(b)); }
DI void mfma_vv0(f32x4& acc, const frag& a, const frag& b) { asm volatile("v_mfma_f32_16x16x32_bf16 %0, %1, %2, 0" : "=&v"(acc) : "v"(a), "v"(b)); }
DI void mfma_av(f32x4& acc, const frag& a, const frag& b) { asm volatile("v_mfma_f32_16x16x32_bf16 %0, %1, %2, %0" : "+v"(acc) : "a"(a), "v"(b)); }
DI void mfma_av0(f32x4& acc, const frag& a, const frag& b) { asm volatile("v_mfma_f32_16x16x32_bf16 %0, %1, %2, 0" : "=&v"(acc) : "a"(a), "v"(b)); }
template <int N> DI void waitv() { asm volatile("s_waitcnt vmcnt(%c0)" ::"i"(N) : "memory"); }
template <int N> DI void waitv2(u32x4& a, u32x4& b) { asm volatile("s_waitcnt vmcnt(%c2)" : "+v"(a), "+v"(b) : "i"(N)); }
template <int N> DI void waitv4(u32x4& a, u32x4& b, u32x4& c, u32x4& d) { asm volatile("s_waitcnt vmcnt(%c4)" : "+v"(a), "+v"(b), "+v"(c), "+v"(d) : "i"(N)); }
DI frag catf(s16x4 a, s16x4 b) { return __builtin_bit_cast(frag, __builtin_shufflevector(a, b, 0, 1, 2, 3, 4, 5, 6, 7)); }
#define SB0 __builtin_amdgcn_sched_barrier(0)

constexpr int CV_RS = 1184;
constexpr int CV_KB = 4128;
constexpr int CV_WB = CV_KB + 4 * CV_RS;
template <int OFF> DI frag cv_ldB(u32 vb) { return catf(*(LAS const s16x4*)(size_t)(vb + OFF), *(volatile LAS const s16x4*)(size_t)(vb + OFF + 8)); }
template <int G> constexpr int cv_off0() { return 2 * (G >> 4) * CV_RS + 64 * (G & 15); }
template <int G> constexpr int cv_off1() { return (2 * (G >> 4) + 1) * CV_RS + 64 * (15 - (G & 15)); }
template <int G, int I> DI void cv_one(f32x4 (&acc)[4][8], const frag (&A)[16], const frag& B0, const frag& B1) {
  constexpr int R = 2 * (G >> 4), N = G & 15;
  constexpr int c0 = N <= 1 ? 0 : N / 2;
  constexpr int n0 = 8 - c0;
  if constexpr (I < n0) {
    constexpr int cb = c0 + I, s = 1 - N + 2 * cb;
    if constexpr (N == 0) mfma_vv0(acc[R][cb], A[s], B0); else mfma_vv(acc[R][cb], A[s], B0);
  } else {
    constexpr int c1 = (15 - N) / 2, cb = c1 + (I - n0), s = N - 14 + 2 * cb;
    static_assert(cb <= 7 && s >= 0 && s <= 15, "");
    if constexpr (s == 0) mfma_vv0(acc[R + 1][cb], A[s], B1); else mfma_vv(acc[R + 1][cb], A[s], B1);
  }
}
struct W1C { u32 vo[7]; u32 m0b, sob; __amdgpu_buffer_rsrc_t srd; };
template <int N, int R> DI void w1_m0(const W1C& c) { asm volatile("s_add_u32 m0, %0, %1" ::"s"(c.m0b), "i"(N * 4 * 7168 + R * 1024) : "scc", "m0"); }
template <int N, int R> DI void w1_ld(const W1C& c) {
  asm volatile("buffer_load_dwordx4 %0, %1, %2 offen lds" ::"v"(c.vo[R]), "s"(c.srd), "s"(c.sob + N * 4 * (32 * F * 2)));
}
template <int N, int R> DI void w1_piece(const W1C& c) { w1_m0<N, R>(c); asm volatile("s_nop 0"); w1_ld<N, R>(c); }
template <int N> DI void w1_step(const W1C& c) {
  w1_piece<N, 0>(c); w1_piece<N, 1>(c); w1_piece<N, 2>(c); w1_piece<N, 3>(c); w1_piece<N, 4>(c); w1_piece<N, 5>(c); w1_piece<N, 6>(c);
}
template <int G> DI void cv_steps(f32x4 (&acc)[4][8], const frag (&A)[16], u32 vb, frag P0, frag P1, frag Q0, frag Q1, const W1C& wc) {
  frag N0, N1;
  cv_one<G, 0>(acc, A, P0, P1); SB0;
  if constexpr (G + 2 < 32) N0 = cv_ldB<cv_off0<G + 2>()>(vb);
  SB0; cv_one<G, 1>(acc, A, P0, P1); cv_one<G, 2>(acc, A, P0, P1); SB0;
  if constexpr (G + 2 < 32) N1 = cv_ldB<cv_off1<G + 2>()>(vb);
  SB0; cv_one<G, 3>(acc, A, P0, P1); cv_one<G, 4>(acc, A, P0, P1);
  if constexpr (G < 28) w1_m0<G / 7, G % 7>(wc);
  cv_one<G, 5>(acc, A, P0, P1);
  if constexpr (G < 28) w1_ld<G / 7, G % 7>(wc);
  cv_one<G, 6>(acc, A, P0, P1); cv_one<G, 7>(acc, A, P0, P1); cv_one<G, 8>(acc, A, P0, P1);
  SB0;
  if constexpr (G + 1 < 32) cv_steps<G + 1>(acc, A, vb, Q0, Q1, N0, N1, wc);
}
constexpr int L_W1 = 0;
constexpr int L_Y = 143360;
constexpr int L_MOD = L_Y + 10240;
constexpr int L_RED = L_MOD + 8192;
constexpr int L_TOTAL = 163840;
constexpr int L_CONV = L_TOTAL - 4 * CV_WB;
static_assert(L_CONV >= 16 * 7168 && (L_CONV & 15) == 0 && L_RED + 1024 <= L_TOTAL, "");

typedef unsigned long long u64;
DI u64 call_tag(u32 epoch) { const u32 t = epoch + 1u; return (u64)t | ((u64)(~t) << 32); }
DI void signal(char* ws, size_t off, u64 tag) { __hip_atomic_store((GAS u64*)(ws + off), tag, __ATOMIC_RELAXED, __HIP_MEMORY_SCOPE_AGENT); }
DI void poll_tags(const char* base, int stride, int n, u64 tag, int lane) {
  const GAS u64* p = (const GAS u64*)(base + (lane < n ? lane : n - 1) * stride);
  for (u32 spins = 0; spins < (1u << 18); spins++) {
    const u64 v = __hip_atomic_load(p, __ATOMIC_RELAXED, __HIP_MEMORY_SCOPE_AGENT);
    if (__all(v == tag)) break;
    __builtin_amdgcn_s_sleep(1);
  }
}
DI void poll_tags256(const char* base, u64 tag, int lane) {
  const __amdgpu_buffer_rsrc_t srd = mksrd(base, 2048);
  const u32 lo = (u32)tag, hi = (u32)(tag >> 32);
  for (u32 spins = 0; spins < (1u << 18); spins++) {
    asm volatile("" ::: "memory");
    const u32x4 a = __builtin_amdgcn_raw_buffer_load_b128(srd, lane * 32, 0, 16), b = __builtin_amdgcn_raw_buffer_load_b128(srd, lane * 32 + 16, 0, 16);
    const bool ok = a[0] == lo && a[1] == hi && a[2] == lo && a[3] == hi && b[0] == lo && b[1] == hi && b[2] == lo && b[3] == hi;
    if (__all(ok)) break;
    __builtin_amdgcn_s_sleep(1);
  }
}
DI void lds_barrier() { asm volatile("s_waitcnt lgkmcnt(0)" ::: "memory"); __builtin_amdgcn_s_barrier(); asm volatile("" ::: "memory"); }

struct KVI { u32x2 k[7], v[4]; u32x4 s0, s1; };
template <int NT> DI void kv_item(const u32x2* kr, const u32x2* vr, const u32x4& s0, const u32x4& s1, char* dst) {
  const f32x2 sk[2][4] = {{{lo16(s0[0]), lo16(s0[2])}, {hi16(s0[0]), hi16(s0[2])}, {lo16(s0[1]), lo16(s0[3])}, {hi16(s0[1]), hi16(s0[3])}},
                          {{lo16(s1[0]), lo16(s1[2])}, {hi16(s1[0]), hi16(s1[2])}, {lo16(s1[1]), lo16(s1[3])}, {hi16(s1[1]), hi16(s1[3])}}};
  f32x2 kf[NT + 3][2], a[NT][2];
#pragma unroll
  for (int i = 0; i < NT + 3; i++) { kf[i][0] = up(kr[i][0]); kf[i][1] = up(kr[i][1]); }
#define KVS(EXPR) _Pragma("unroll") for (int r = 0; r < NT; r++) { _Pragma("unroll") for (int h = 0; h < 2; h++) { EXPR; } }
  KVS(a[r][h] = rb2(kf[r][h] * sk[h][3]))
  f32x2 pqt[NT][2];
  KVS(pqt[r][h] = rb2(kf[r + 1][h] * sk[h][2]))
  KVS(a[r][h] = rb2(a[r][h] + pqt[r][h]))
  KVS(pqt[r][h] = rb2(kf[r + 2][h] * sk[h][1]))
  KVS(a[r][h] = rb2(a[r][h] + pqt[r][h]))
  KVS(pqt[r][h] = rb2(kf[r + 3][h] * sk[h][0]))
  KVS(a[r][h] = rb2(a[r][h] + pqt[r][h]))
#undef KVS
#pragma unroll
  for (int r = 0; r < NT; r++) *(u32x2*)(dst + r * 176) = (u32x2){pk(a[r][0] * up(vr[r][0])), pk(a[r][1] * up(vr[r][1]))};
}
struct KVR { u32x2 kA[7], vA[4], kB[4], vB; u32x4 sA0, sA1, sB0, sB1; u32 dA, dB; };
constexpr int P_K = 24576, P_V = P_K + 11264, P_S = P_V + 10240;
DI void prep_request(const u16* __restrict__ k, const u16* __restrict__ v, const u16* __restrict__ shk, u32 lds0, int xcd, int tt, int w, int lane) {
  const __amdgpu_buffer_rsrc_t sK = mksrd(k, S * D * 2), sV = mksrd(v, S * D * 2), sS = mksrd(shk + 80 * xcd * 4, 640);
  const u32 col = 160 * xcd;
#pragma unroll
  for (int n = 0; n < 3; n++) {
    const u32 c = 64 * (w + 4 * n) + lane, row = (c * 6554u) >> 16, o = row * (D * 2 - 160) + c * 16 + col;
    if (n < 2 || w < 3) dma16(lds0 + P_K + (w + 4 * n) * 1024, c < 670 ? o + (64 * tt - 3) * (D * 2) : 0x80000000u, sK, 0);
    if (n < 2 || w < 2) dma16(lds0 + P_V + (w + 4 * n) * 1024, o + 64 * tt * (D * 2), sV, 0);
  }
  if (w == 3) dma16(lds0 + P_S, lane * 16, sS, 0);
  waitv<0>();
}
DI void prep_read(KVR& r, const char* smem, int tid) {
  const u32 mgA = ((u32)tid * 3277u) >> 16, cqA = tid - 20 * mgA;
  const u32 i2 = 256 + (tid >> 2), mgB = (i2 * 3277u) >> 16, cqB = i2 - 20 * mgB, tB = 4 * mgB + (tid & 3);
  const char* pA = smem + 4 * mgA * 160 + 8 * cqA;
  const char* pB = smem + tB * 160 + 8 * cqB;
#pragma unroll
  for (int i = 0; i < 7; i++) r.kA[i] = *(const u32x2*)(pA + P_K + i * 160);
#pragma unroll
  for (int i = 0; i < 4; i++) r.vA[i] = *(const u32x2*)(pA + P_V + i * 160);
#pragma unroll
  for (int i = 0; i < 4; i++) r.kB[i] = *(const u32x2*)(pB + P_K + i * 160);
  r.vB = *(const u32x2*)(pB + P_V);
  r.sA0 = *(const u32x4*)(smem + P_S + 32 * cqA); r.sA1 = *(const u32x4*)(smem + P_S + 32 * cqA + 16);
  r.sB0 = *(const u32x4*)(smem + P_S + 32 * cqB); r.sB1 = *(const u32x4*)(smem + P_S + 32 * cqB + 16);
  r.dA = 4 * mgA * 176 + 8 * cqA; r.dB = tB * 176 + 8 * cqB;
}
DI void prep_finish(u32x2 (&xr)[5], char* __restrict__ ws, char* smem, int xcd, int tt, int tid, int lane, int w, u64 tag) {
  KVR r;
  prep_read(r, smem, tid);
  kv_item<4>(r.kA, r.vA, r.sA0, r.sA1, smem + r.dA);
  kv_item<1>(r.kB, &r.vB, r.sB0, r.sB1, smem + r.dB);
  lds_barrier();
  char* us = smem + 12288;
  {
    const int grp = lane >> 4, i16 = lane & 15, jj = i16 >> 2, p = i16 & 3;
#pragma unroll
    for (int n = 0; n < 3; n++) {
      const u32 task = 4 * (w + 4 * n) + grp;
      if (n < 2 || w < 2) {
        const u32 hr = (task * 52429u) >> 18, cgrp = task - 5 * hr, rho = hr >> 1, half = hr & 1;
        const u32 base = ldsaddr(smem) + (4 * (8 * half + jj) + rho) * 176 + (16 * cgrp + 4 * p) * 2;
        *(frag*)(us + (16 * cgrp + i16) * 144 + rho * 32 + half * 16) = catf(trread(base), trread(base + 16 * 176));
      }
    }
  }
  lds_barrier();
  {
    const __amdgpu_buffer_rsrc_t sU = mksrd(ws + WS_U, 2621440);
    const u32 uo = (tt * D + 80 * xcd + (lane >> 3)) * 128 + (lane & 7) * 16;
#pragma unroll
    for (int n = 0; n < 3; n++) {
      const int c8 = w + 4 * n;
      if (n < 2 || w < 2) __builtin_amdgcn_raw_buffer_store_b128(*(const u32x4*)(us + (8 * c8 + (lane >> 3)) * 144 + (lane & 7) * 16), sU, uo + c8 * 1024, 0, 16);
    }
  }
  asm volatile("s_waitcnt vmcnt(0)" : "+v"(xr[0]), "+v"(xr[1]), "+v"(xr[2]), "+v"(xr[3]), "+v"(xr[4]) :: "memory");
  __builtin_amdgcn_s_barrier();
  if (tid == 0) signal(ws, WS_FU + (xcd * 32 + tt) * 64, tag);
}
DI void q_request(u32x2 (&xr)[5], const u16* __restrict__ qin, int t0, int w, int lane) {
  const __amdgpu_buffer_rsrc_t srdQ = mksrd(qin + (size_t)t0 * D, 10240);
  const u32 vq = (lane & 7) * 1280 + (40 * w + (lane >> 3)) * 8;
  asm volatile("s_nop 4");
  bload2<0>(xr[0], vq, srdQ, 0); bload2<64>(xr[1], vq, srdQ, 0); bload2<128>(xr[2], vq, srdQ, 0); bload2<192>(xr[3], vq, srdQ, 0); bload2<256>(xr[4], vq, srdQ, 0);
}

DI void phase_conv(const u32x4 (&rk)[4], u32 Draw, char* __restrict__ ws, char* smem, int xcd, int j, int w, int lane, u64 tag, const W1C& wc
) {
  const int ch = 80 * xcd + 4 * j + w;
  char* kim = smem + L_CONV + w * CV_WB;
  char* uim = kim + CV_KB;
  if (lane < 32) *(u32x4*)(uim + (lane >> 3) * CV_RS + 16 * (lane & 7)) = (u32x4){0u, 0u, 0u, 0u};
  if (lane < 2) *(u32x4*)(kim + 16 * lane) = (u32x4){0u, 0u, 0u, 0u};
#pragma unroll
  for (int i = 0; i < 4; i++) *(u32x4*)(kim + 32 + 16 * (64 * i + lane)) = rk[i];
  frag A[16];
  {
    const int q = lane >> 4, cc = lane & 15;
    const u32 va = ldsaddr(kim) + 224 - 64 * q - 8 * (cc >> 2) + 8 * (cc & 3);
#pragma unroll
    for (int s = 0; s < 16; s++) A[s] = catf(trread(va + 256 * s + 32), trread(va + 256 * s));
  }
  if (w == 0) poll_tags(ws + WS_FU + xcd * 2048, 64, 32, tag, lane);
  __builtin_amdgcn_s_barrier();
  MARK(22);
  {
    const __amdgpu_buffer_rsrc_t sU = mksrd(ws + WS_U, 2621440);
    const u32 uo = ((lane >> 3) * D + ch) * 128 + (lane & 7) * 16;
    u32x4 ru[4];
#pragma unroll
    for (int i = 0; i < 4; i++) ru[i] = __builtin_amdgcn_raw_buffer_load_b128(sU, uo + i * (8 * D * 128), 0, 16);
    char* ud = uim + ((lane & 7) >> 1) * CV_RS + 128 + 32 * (lane >> 3) + 16 * (lane & 1);
#pragma unroll
    for (int i = 0; i < 4; i++) *(u32x4*)(ud + 256 * i) = ru[i];
  }
  MARK(0);
  const int q = lane >> 4, cc = lane & 15;
  const u32 vb = ldsaddr(uim) + 8 + 8 * cc + 16 * q;
  f32x4 acc[4][8];
  frag P0, P1, Q0, Q1;
  P0 = cv_ldB<cv_off0<0>()>(vb); P1 = cv_ldB<cv_off1<0>()>(vb); Q0 = cv_ldB<cv_off0<1>()>(vb); Q1 = cv_ldB<cv_off1<1>()>(vb);
  MARK(1);
  cv_steps<0>(acc, A, vb, P0, P1, Q0, Q1, wc);
  asm volatile("s_nop 7\n\ts_nop 1");
#pragma unroll
  for (int r = 0; r < 4; r++)
#pragma unroll
    for (int cb = 0; cb < 8; cb += 4) asm volatile("" : "+v"(acc[r][cb]), "+v"(acc[r][cb + 1]), "+v"(acc[r][cb + 2]), "+v"(acc[r][cb + 3]));
  MARK(2);
  const int idx = 4 * (lane == 0 ? 63 : (lane >= 16 ? lane - 16 : lane + 47));
  const u32 ub = ldsaddr(uim) + 2 * (64 + 4 * cc + q);
  char* yl = kim + 32 * cc + 8 * q;
  const float Dch = (w & 1) ? hi16(Draw) : lo16(Draw);
  const f32x2 DD = {Dch, Dch};
  float o[8][4], cy[8][3];
  u32 u01[8], u23[8];
#pragma unroll
  for (int cb = 0; cb < 8; cb++) {
    const LAS u16* up_ = (const LAS u16*)(size_t)(ub + 128 * cb);
    u01[cb] = (u32)up_[0] | ((u32)up_[CV_RS / 2] << 16);
    u23[cb] = (u32)up_[CV_RS] | ((u32)up_[3 * CV_RS / 2] << 16);
  }
#pragma unroll
  for (int cb = 0; cb < 8; cb++) {
    const f32x4 z0 = acc[0][cb], z1 = acc[1][cb], z2 = acc[2][cb], z3 = acc[3][cb];
    o[cb][0] = z0[0]; o[cb][1] = z0[1] + z1[0]; o[cb][2] = z0[2] + z1[1] + z2[0]; o[cb][3] = (z0[3] + z1[2]) + (z2[1] + z3[0]);
    cy[cb][0] = z1[3] + z2[2] + z3[1]; cy[cb][1] = z2[3] + z3[2]; cy[cb][2] = z3[3];
  }
#pragma unroll
  for (int cb = 0; cb < 8; cb++)
#pragma unroll
    for (int e = 0; e < 3; e++) {
      const float t = lane == 63 ? (cb ? cy[cb - 1][e] : 0.f) : cy[cb][e];
      o[cb][e] += __int_as_float(__builtin_amdgcn_ds_bpermute(idx, __float_as_int(t)));
    }
#pragma unroll
  for (int cb = 0; cb < 8; cb++) {
    const f32x2 y01 = rb2((f32x2){o[cb][0], o[cb][1]}) + rb2(up(u01[cb]) * DD), y23 = rb2((f32x2){o[cb][2], o[cb][3]}) + rb2(up(u23[cb]) * DD);
    *(u32x2*)(yl + 512 * cb) = (u32x2){pk(y01), pk(y23)};
  }
  lds_barrier();
  {
    const __amdgpu_buffer_rsrc_t sY = mksrd(ws + WS_Y2B, 2621440);
    const int c4 = (lane >> 1) & 3, tgl = 32 * w + (lane >> 3), hf = lane & 1;
    const char* src = smem + L_CONV + c4 * CV_WB + tgl * 32 + 16 * hf;
    const u32 yo = (tgl * D + 80 * xcd + 4 * j + c4) * 32 + 16 * hf;
    u32x4 v[4];
#pragma unroll
    for (int i = 0; i < 4; i++) v[i] = *(const u32x4*)(src + 256 * i);
#pragma unroll
    for (int i = 0; i < 4; i++) __builtin_amdgcn_raw_buffer_store_b128(v[i], sY, yo + i * (8 * D * 32), 0, 16);
  }
  MARK(3);
}

#define PRE_G 0
#define PRE_R 0
template <int LO, int HI> DI void gemv_request(u32x4 (&r)[20], const u16* __restrict__ wmod, int r0, int tid) {
  const __amdgpu_buffer_rsrc_t sW = mksrd(wmod, D * 6 * D * 2);
  const u32 o = ((tid >> 3) * (6 * D) + 64 * r0 + 8 * (tid & 7)) * 2;
#pragma unroll
  for (int i = LO; i < HI; i++) r[i] = __builtin_amdgcn_raw_buffer_load_b128(sW, o, i * (32 * 6 * D * 2), 0);
}
DI void gemv_finish(const u32x4 (&r)[20], const u16* __restrict__ c, char* __restrict__ ws, char* smem, int r0, int w, int tid, int lane) {
    const __amdgpu_buffer_rsrc_t sWS = mksrd(ws, 65536);
    float* sc = (float*)(smem + L_Y);
    float* red = sc + 640;
    const int col0 = 64 * r0, rg = tid >> 3;
    for (int i = tid; i < D; i += 256) { float x = lo16(c[i]); sc[i] = rbf(x / (1.f + __expf(-x))); }
    __syncthreads();
    f32x2 a[4] = {{0.f, 0.f}, {0.f, 0.f}, {0.f, 0.f}, {0.f, 0.f}};
#pragma unroll
    for (int i = 0; i < 20; i++) {
      const float s = sc[rg + 32 * i];
#pragma unroll
      for (int e = 0; e < 4; e++) a[e] += up(r[i][e]) * (f32x2){s, s};
    }
    float af[8] = {a[0][0], a[0][1], a[1][0], a[1][1], a[2][0], a[2][1], a[3][0], a[3][1]};
#pragma unroll
    for (int e = 0; e < 8; e++) af[e] += __int_as_float(__builtin_amdgcn_update_dpp(0, __float_as_int(af[e]), 0x128, 0xf, 0xf, false));
#pragma unroll
    for (int e = 0; e < 8; e++) { auto rr = __builtin_amdgcn_permlane16_swap(__float_as_uint(af[e]), __float_as_uint(af[e]), false, false); af[e] = __uint_as_float(rr[0]) + __uint_as_float(rr[1]); }
#pragma unroll
    for (int e = 0; e < 8; e++) { auto rr = __builtin_amdgcn_permlane32_swap(__float_as_uint(af[e]), __float_as_uint(af[e]), false, false); af[e] = __uint_as_float(rr[0]) + __uint_as_float(rr[1]); }
    if (lane < 8) { *(f32x4*)(red + w * 64 + 8 * lane) = (f32x4){af[0], af[1], af[2], af[3]}; *(f32x4*)(red + w * 64 + 8 * lane + 4) = (f32x4){af[4], af[5], af[6], af[7]}; }
    __syncthreads();
    if (tid < 32) {
      const int c0 = 2 * tid;
      float s0 = rbf((red[c0] + red[64 + c0]) + (red[128 + c0] + red[192 + c0]));
      float s1 = rbf((red[c0 + 1] + red[65 + c0]) + (red[129 + c0] + red[193 + c0]));
      const int jc = col0 + c0, vv = jc / D, ch = jc - vv * D;
      if (vv == 1 || vv == 4) { s0 += 1.f; s1 += 1.f; }
      const int slot = vv == 0 ? 1 : vv == 1 ? 0 : vv == 2 ? 2 : vv == 3 ? 4 : vv == 4 ? 3 : 5;
      __builtin_amdgcn_raw_buffer_store_b32(pk2(s0, s1), sWS, WS_MODP + ((ch >> 2) * 24 + slot * 4 + (ch & 3)) * 2, 0, 16);
    }
}
template <int LO, int HI> DI void retile_request(u32x4 (&r)[4][4], const u16* __restrict__ w2, int r0, int w, int lane) {
  const __amdgpu_buffer_rsrc_t sW = mksrd(w2, F * D * 2);
  const int wv = (r0 - 60) * 4 + w;
#pragma unroll
  for (int n = LO; n < HI; n++) {
    const int u = wv + 144 * n, uc = u < 540 ? u : 539, ks = uc / 10, cc = uc - 10 * ks;
#pragma unroll
    for (int i = 0; i < 4; i++) r[n][i] = __builtin_amdgcn_raw_buffer_load_b128(sW, ((8 * i + (lane >> 3)) * D + 8 * (lane & 7)) * 2, (32 * ks * D + 64 * cc) * 2, 0);
  }
}
DI void retile_finish(const u32x4 (&r)[4][4], char* __restrict__ ws, char* smem, int r0, int w, int lane) {
    const int wv = (r0 - 60) * 4 + w;
    char* mb = smem + L_Y + w * (32 * 144);
    const int q = lane >> 4, i16 = lane & 15, jj = i16 >> 2, p = i16 & 3;
    const __amdgpu_buffer_rsrc_t sO = mksrd(ws + WS_W2F, 2211840);
    const u32 a0 = ldsaddr(mb) + (4 * q + jj) * 144 + 8 * p;
#pragma unroll
    for (int n = 0; n < 4; n++) {
      const int u = wv + 144 * n, uc = u < 540 ? u : 539, ks = uc / 10, cc = uc - 10 * ks;
#pragma unroll
      for (int i = 0; i < 4; i++) *(u32x4*)(mb + (8 * i + (lane >> 3)) * 144 + 16 * (lane & 7)) = r[n][i];
      frag v[4];
#pragma unroll
      for (int f = 0; f < 4; f++) v[f] = catf(trread(a0 + 32 * f), trread(a0 + 32 * f + 16 * 144));
#pragma unroll
      for (int f = 0; f < 4; f++) {
        const int ft = 4 * cc + f;
        if (u < 540) __builtin_amdgcn_raw_buffer_store_b128(v[f], sO, (((ft / 5) * 54 + ks) * 5 + (ft % 5)) * 1024 + lane * 16, 0, 16);
      }
    }
}

DI float xsum8_16_32(float x) {
  x += __int_as_float(__builtin_amdgcn_update_dpp(0, __float_as_int(x), 0x128, 0xf, 0xf, false));
  { auto r = __builtin_amdgcn_permlane16_swap(__float_as_uint(x), __float_as_uint(x), false, false); x = __uint_as_float(r[0]) + __uint_as_float(r[1]); }
  { auto r = __builtin_amdgcn_permlane32_swap(__float_as_uint(x), __float_as_uint(x), false, false); x = __uint_as_float(r[0]) + __uint_as_float(r[1]); }
  return x;
}
DI void ln_stats(const f32x2 (&x)[10], f32x2* red, int w, int lane, float& mean, float& rinv) {
  f32x2 a = ((x[0] + x[1]) + (x[2] + x[3])) + ((x[4] + x[5]) + (x[6] + x[7])) + (x[8] + x[9]);
  f32x2 b0 = x[0] * x[0], b1 = x[1] * x[1];
#pragma unroll
  for (int h = 2; h < 10; h += 2) { b0 += x[h] * x[h]; b1 += x[h + 1] * x[h + 1]; }
  b0 += b1;
  const float s1 = xsum8_16_32(a[0] + a[1]), s2 = xsum8_16_32(b0[0] + b0[1]);
  if (lane < 8) red[w * 8 + lane] = (f32x2){s1, s2};
  lds_barrier();
  const int tk = lane & 7;
  const f32x2 t = (red[tk] + red[8 + tk]) + (red[16 + tk] + red[24 + tk]);
  ln_finish(t[0], t[1], mean, rinv);
}
DI f32x2 gelu2(f32x2 x) {
  const float c0 = -2.f * 0.7978845608028654f * 1.4426950408889634f, c1 = c0 * 0.044715f;
  const f32x2 z = x * (x * x * (f32x2){c1, c1} + (f32x2){c0, c0});
  const f32x2 d = (f32x2){__builtin_amdgcn_exp2f(z[0]), __builtin_amdgcn_exp2f(z[1])} + (f32x2){1.f, 1.f};
  return x * (f32x2){__builtin_amdgcn_rcpf(d[0]), __builtin_amdgcn_rcpf(d[1])};
}
DI int rotk(int n, int rot, int m) { int k = n + rot; return k >= m ? k - m : k; }
struct W2C { u32 l16; int rot2; __amdgpu_buffer_rsrc_t srd; };
template <int I> DI void w2_load1(frag (&W2)[60], const W2C& c) {
  const u32 vo = c.l16 + rotk(I / 5, c.rot2, 14) * 5120;
  if constexpr (I % 5 < 4) bload_a_sc1<(I % 5) * 1024>(W2[I], vo, c.srd, 0); else bload_a_sc1<0>(W2[I], vo + 4096, c.srd, 0);
}
template <int I, int N> DI void w2_loads(frag (&W2)[60], const W2C& c) {
  w2_load1<I>(W2, c);
  if constexpr (N > 1) w2_loads<I + 1, N - 1>(W2, c);
}
#define W2_M0V 8
#define W2_M1V 20
#define W2_G1V 20
constexpr int W2_M0 = W2_M0V, W2_M1 = W2_M1V, W2_G1 = W2_G1V;

constexpr int G1R = 6;
struct G1S {
  frag Y[G1R][2];
  frag A0[7], A1[7];
  f32x4 acc[7][2];
};
template <int FI> DI frag g1_lda(u32 ab) { return catf(trread(ab + 32 * FI), trread(ab + 32 * FI + 1792)); }
struct G1C { u32 b0, l16, ybase; int rot1; __amdgpu_buffer_rsrc_t srdY; };
template <int KS, int FI>
DI void g1_f(G1S& s, frag (&Ac)[7], frag (&An)[7], frag (&W2)[60], const G1C& c, const W2C& c2, u32 ab, u32 yo) {
  mfma_vv(s.acc[FI][0], Ac[FI], s.Y[KS % G1R][0]);
  SB0;
  if constexpr (KS < 19) An[FI] = g1_lda<FI>(ab);
  SB0;
  mfma_vv(s.acc[FI][1], Ac[FI], s.Y[KS % G1R][1]);
  if constexpr (KS >= 1 && KS - 1 + G1R < 20 && (FI == 1 || FI == 4)) bload_sc1<(FI == 4) * 1024>(s.Y[(KS - 1) % G1R][FI == 4], c.l16, c.srdY, yo);
  if constexpr (KS < W2_G1 && FI == 6) w2_load1<W2_M0 + W2_M1 + KS>(W2, c2);
  SB0;
  if constexpr (FI < 6) g1_f<KS, FI + 1>(s, Ac, An, W2, c, c2, ab, yo);
}
template <int KS> constexpr int g1_younger() {
  int cnt = 0; bool seen = false;
  for (int k = 0; k < G1R; k++) { if (seen) cnt += 2; if (k == KS) seen = true; }
  for (int n = 0; n < KS; n++) {
    if (n >= 1 && n - 1 + G1R < 20) { if (seen) cnt += 2; if (n - 1 + G1R == KS) seen = true; }
    if (n < W2_G1 && seen) cnt += 1;
  }
  return cnt;
}
template <int KS>
DI void g1_step(G1S& s, frag (&W2)[60], const G1C& c, const W2C& c2) {
  waitv2<g1_younger<KS>()>(s.Y[KS % G1R][0], s.Y[KS % G1R][1]);
  const u32 ab = c.b0 + rotk(KS + 1, c.rot1, 20) * 7168;
  const u32 yo = c.ybase + rotk(KS - 1 + G1R, c.rot1, 20) * 2048;
  if constexpr (KS & 1) g1_f<KS, 0>(s, s.A1, s.A0, W2, c, c2, ab, yo);
  else                  g1_f<KS, 0>(s, s.A0, s.A1, W2, c, c2, ab, yo);
  if constexpr (KS < 19) g1_step<KS + 1>(s, W2, c, c2);
}
struct G2S {
  frag H[4][4];
  frag WX[10];
  f32x4 acc[5][4];
  u32x2 xx[5], gg[5], b2v[5];
};
struct G2C { u32 l16, vx, vb2, vg; int rot2; __amdgpu_buffer_rsrc_t srdH, srdX, srdB2, srdMD; };
template <int N, int T> DI void g2_hload1(G2S& s, const G2C& c, u32 vo) { bload_sc1<T * 1024>(s.H[N & 3][T], vo, c.srdH, 0); }
template <int N> DI void g2_hload(G2S& s, const G2C& c) {
  const u32 vo = c.l16 + rotk(N, c.rot2, 14) * 4096;
  g2_hload1<N, 0>(s, c, vo); g2_hload1<N, 1>(s, c, vo); g2_hload1<N, 2>(s, c, vo); g2_hload1<N, 3>(s, c, vo);
}
template <int I> DI void g2_eload(G2S& s, const G2C& c) {
  if constexpr (I < 5) bload2_sc1<32 * I>(s.xx[I], c.vx, c.srdX, 0);
  else if constexpr (I < 10) bload2<32 * (I - 5)>(s.b2v[I - 5], c.vb2, c.srdB2, 0);
  else bload2_sc1<192 * (I - 10)>(s.gg[I - 10], c.vg, c.srdMD, 0);
}
template <int N, int M>
DI void g2_m(G2S& s, frag (&W2)[60], const G2C& c, u32 vo) {
  constexpr int d = M >> 2, t = M & 3;
  if constexpr (N == 0) mfma_av0(s.acc[d][t], W2[d], s.H[0][t]);
  else if constexpr (N < 12) mfma_av(s.acc[d][t], W2[N * 5 + d], s.H[N & 3][t]);
  else mfma_vv(s.acc[d][t], s.WX[(N - 12) * 5 + d], s.H[N & 3][t]);
  if constexpr (N + 3 < 14 && (M == 1 || M == 6 || M == 11 || M == 16)) g2_hload1<N + 3, (M - 1) / 5>(s, c, vo);
  if constexpr (N >= 11 && (M == 1 || M == 5 || M == 9 || M == 13 || M == 17)) g2_eload<5 * (N - 11) + (M - 1) / 4>(s, c);
  if constexpr (M < 19) g2_m<N, M + 1>(s, W2, c, vo);
}
template <int N> constexpr int g2_younger() {
  int cnt = 0; bool seen = false;
  for (int h = 0; h < 3; h++) { if (seen) cnt += 4; if (h == N) seen = true; }
  for (int n = 0; n < N; n++) {
    if (n + 3 < 14) { if (seen) cnt += 4; if (n + 3 == N) seen = true; }
    if (n >= 11 && seen) cnt += 5;
  }
  return cnt;
}
static_assert(g2_younger<0>() == 8 && g2_younger<3>() == 8 && g2_younger<11>() == 8 && g2_younger<12>() == 9 && g2_younger<13>() == 10, "");
template <int N>
DI void g2_step(G2S& s, frag (&W2)[60], const G2C& c) {
  waitv4<g2_younger<N>()>(s.H[N & 3][0], s.H[N & 3][1], s.H[N & 3][2], s.H[N & 3][3]);
  const u32 vo = c.l16 + rotk(N + 3, c.rot2, 14) * 4096;
  g2_m<N, 0>(s, W2, c, vo);
  if constexpr (N < 13) g2_step<N + 1>(s, W2, c);
}

__global__ __launch_bounds__(256, 1) void k_main(const u16* __restrict__ qin, const u16* __restrict__ kin, const u16* __restrict__ vin,
                                                 const u16* __restrict__ shk, const u16* __restrict__ ssk, const u16* __restrict__ ssD,
                                                 const u16* __restrict__ cin, const u16* __restrict__ wmod, const u16* __restrict__ w1,
                                                 const u16* __restrict__ b1, const u16* __restrict__ w2, const u16* __restrict__ b2,
                                                 char* __restrict__ ws, u16* __restrict__ out) {
  STAMP_BEGIN(ws)
  MARK_DECL
  extern __shared__ __attribute__((aligned(16))) char smem[];
  const int tid = threadIdx.x, lane = tid & 63, w = rfl(tid >> 6);
  const int b = blockIdx.x, xcd = b & 7, j = b >> 3;
  const int tile = 4 * (xcd >> 1) + (j >> 3), g = 8 * (xcd & 1) + (j & 7);
  const int TT = 4 * xcd + (j >> 3), cg = j & 7;
  const u32 l16 = lane * 16, lds0 = ldsaddr(smem);
  W1C wc;
#pragma unroll
  for (int r = 0; r < 7; r++) { const u32 idx = 64 * r + lane, row = (idx * 4682u) >> 16; wc.vo[r] = row * (F * 2 - 224) + idx * 16; }
  wc.m0b = lds0 + L_W1 + w * 7168; wc.sob = w * (32 * F * 2) + 224 * g; wc.srd = mksrd(w1, D * F * 2);
  const int tg = 16 * tile + g, t0 = 8 * tg;
  const int tk = lane & 7, slot = lane >> 3, q = lane >> 4;
  const u64 tag = call_tag(__hip_atomic_load((const GAS u32*)(ws + WS_EPOCH), __ATOMIC_RELAXED, __HIP_MEMORY_SCOPE_AGENT));
  asm volatile("s_nop 4");
  prep_request(kin, vin, shk, lds0, xcd, j, w, lane);
  __builtin_amdgcn_s_barrier();
  if (w == 3) {
    unsigned long long pc; u32 len;
    asm volatile("s_getpc_b64 %0\n.Lkmain_here:\n\ts_mov_b32 %1, .Lkmain_end-.Lkmain_here" : "=s"(pc), "=s"(len));
    dma4(lds0 + L_RED + 768, (j + 32 * lane) * 128, mksrd((const void*)pc, len), 0);
  }
  if (w == 2 && j < 2) {
    const char* p = ws + (size_t)(lane < 13 ? lane : 0) * 2097152;
    if (lane == 13 || lane == 14) p = (const char*)out + (lane - 13) * 2097152;
    if (lane == 15 || lane == 16) p = (const char*)w1 + (lane - 15) * 2097152;
    if (lane == 17 || lane == 18) p = (const char*)w2 + (lane - 17) * 2097152;
    if (lane >= 19 && lane <= 21) p = (const char*)wmod + (lane - 19) * 2097152;
    if (lane == 22) p = (const char*)b1;
    if (lane == 23) p = (const char*)b2;
    if (lane >= 24) p = (const char*)cin;
    asm volatile("s_mov_b32 m0, %0\n\ts_nop 0\n\tglobal_load_lds_dword %1, off sc1" ::"s"(lds0 + L_RED + 768), "v"(p) : "m0");
  }
  MARK(21);
  u32x2 xr[5];
  const int r0 = (j - 20) * 8 + xcd;
  if (j < 20) {
    const int ch = 80 * xcd + 4 * j + w;
    const __amdgpu_buffer_rsrc_t sF = mksrd(ssk + (size_t)ch * S, S * 2);
    u32x4 rk[4];
#pragma unroll
    for (int i = 0; i < 4; i++) rk[i] = __builtin_amdgcn_raw_buffer_load_b128(sF, l16 + 1024 * i, 0, 0);
    const u32 Draw = __builtin_amdgcn_raw_buffer_load_b32(mksrd(ssD, D * 2), (ch >> 1) * 4, 0, 0);
    q_request(xr, qin, t0, w, lane);
    prep_finish(xr, ws, smem, xcd, j, tid, lane, w, tag);
    MARK(23);
    phase_conv(rk, Draw, ws, smem, xcd, j, w, lane, tag, wc
    );
  } else if (r0 < 60) {
    u32x4 r[20];
    q_request(xr, qin, t0, w, lane);
    gemv_request<0, PRE_G>(r, wmod, r0, tid);
    prep_finish(xr, ws, smem, xcd, j, tid, lane, w, tag);
    gemv_request<PRE_G, 20>(r, wmod, r0, tid);
    SB0;
    gemv_finish(r, cin, ws, smem, r0, w, tid, lane);
  } else {
    u32x4 r[4][4];
    q_request(xr, qin, t0, w, lane);
    retile_request<0, PRE_R>(r, w2, r0, w, lane);
    prep_finish(xr, ws, smem, xcd, j, tid, lane, w, tag);
    retile_request<PRE_R, 4>(r, w2, r0, w, lane);
    SB0;
    retile_finish(r, ws, smem, r0, w, lane);
  }
  waitv<0>();
  __builtin_amdgcn_s_barrier();
  MARK(4);
  if (tid < 8) {
    signal(ws, WS_FG + tid * 2048 + b * 8, tag);
    if (j >= 20 && r0 >= 60) signal(ws, WS_FW + tid * 512 + (r0 - 60) * 8, tag);
  }
  const int ks0 = w < 2 ? 14 * w : 28 + 13 * (w - 2), nks = w < 2 ? 14 : 13;
  W2C c2;
  c2.l16 = l16; c2.srd = mksrd(ws + WS_W2F + (size_t)cg * 276480 + ks0 * 5120, nks * 5120);
  { int r2 = 2 * cg + 4 * (j >> 3); r2 = r2 >= 14 ? r2 - 14 : r2; c2.rot2 = r2 >= 14 ? r2 - 14 : r2; }
  frag W2[60];
  f32x2 x[10], t[10];
  f32x2* red = (f32x2*)(smem + L_RED);
  {
    if (j >= 20) { w1_step<0>(wc); w1_step<1>(wc); }
#pragma unroll
    for (int i = 0; i < 5; i++) { x[2 * i] = up(xr[i][0]); x[2 * i + 1] = up(xr[i][1]); }
    if (w == 0) poll_tags(ws + WS_FW + xcd * 512, 8, 36, tag, lane);
    float mean, rinv;
    ln_stats(x, red + 64, w, lane, mean, rinv);
    const f32x2 mm = {mean, mean}, rr = {rinv, rinv};
#pragma unroll
    for (int h = 0; h < 10; h++) t[h] = rb2(x[h] - mm);
#pragma unroll
    for (int h = 0; h < 10; h++) t[h] = rb2(t[h] * rr);
#pragma unroll
    for (int h = 0; h < 10; h++) asm volatile("" : "+v"(x[h]), "+v"(t[h]));
  }
  if (j >= 20) { w1_step<2>(wc); w1_step<3>(wc); }
  w2_loads<0, W2_M0>(W2, c2);
  if (w == 0) poll_tags256(ws + WS_FG + xcd * 2048, tag, lane);
  __builtin_amdgcn_s_barrier();
  if (b == 0 && tid == 0) __hip_atomic_store((GAS u32*)(ws + WS_EPOCH), (u32)tag, __ATOMIC_RELAXED, __HIP_MEMORY_SCOPE_AGENT);
  MARK(5);
  const __amdgpu_buffer_rsrc_t srdMD = mksrd(ws + WS_MODP, 7680);
  u32x2 bb[7];
  {
    const __amdgpu_buffer_rsrc_t srdYB = mksrd(ws + WS_Y2B + (size_t)(tg >> 1) * 20480, 20480);
    const __amdgpu_buffer_rsrc_t srdB1 = mksrd(b1, F * 2);
    u32x4 yr[3], mr[2];
    const u32 v16 = tid * 16, vb = (16 * 7 * g + 4 * q) * 2;
    asm volatile("s_nop 4");
    MARK(20);
    const u32 vy = tid * 32 + 16 * (tg & 1);
    bload_sc1<0>(yr[0], vy, srdYB, 0); bload_sc1<0>(yr[1], vy + 8192, srdYB, 0); bload_sc1<0>(yr[2], vy + 16384, srdYB, 0);
    bload_sc1<0>(mr[0], v16, srdMD, 0); bload_sc1<0>(mr[1], v16 + 4096, srdMD, 0);
    w1_step<4>(wc);
    asm volatile("s_waitcnt vmcnt(7)" : "+v"(yr[0]), "+v"(yr[1]), "+v"(yr[2]), "+v"(mr[0]), "+v"(mr[1]));
    MARK(19);
    *(u32x4*)(smem + L_Y + v16) = yr[0]; *(u32x4*)(smem + L_Y + 4096 + v16) = yr[1];
    if (tid < 128) *(u32x4*)(smem + L_Y + 8192 + v16) = yr[2];
    *(u32x4*)(smem + L_MOD + v16) = mr[0]; *(u32x4*)(smem + L_MOD + 4096 + v16) = mr[1];
    lds_barrier();
    MARK(6);
    const int i16 = lane & 15, jj = i16 >> 2, p = i16 & 3;
    const u32 ya = lds0 + L_Y + (4 * (40 * w + 2 * q + (p >> 1)) + jj) * 16 + 8 * (p & 1);
    const char* mdp = smem + L_MOD + (40 * w + slot) * 48;
    u32 yy[10], w1v[10], sav[10], gav[10];
#pragma unroll
    for (int i = 0; i < 5; i++) {
      const u32x2 yu = __builtin_bit_cast(u32x2, trread(ya + i * 8 * 64));
      const u32x4 m0 = *(const u32x4*)(mdp + i * 384);
      const u32x2 m1 = *(const u32x2*)(mdp + i * 384 + 16);
      yy[2 * i] = yu[0]; yy[2 * i + 1] = yu[1]; w1v[2 * i] = m0[0]; w1v[2 * i + 1] = m0[1]; sav[2 * i] = m0[2]; sav[2 * i + 1] = m0[3]; gav[2 * i] = m1[0]; gav[2 * i + 1] = m1[1];
    }
    u32 o[10];
#define STAGE(EXPR) _Pragma("unroll") for (int h = 0; h < 10; h++) { EXPR; }
    STAGE(t[h] = rb2(t[h] * up(w1v[h])))
    STAGE(t[h] = rb2(t[h] + up(sav[h])))
    STAGE(t[h] = rb2(t[h] * up(yy[h])))
    STAGE(t[h] = rb2(up(gav[h]) * t[h]))
    STAGE(o[h] = pk(x[h] + t[h]))
    const __amdgpu_buffer_rsrc_t srdX1 = mksrd(ws + WS_X1, 2621440);
    const u32 xo = (t0 + tk) * 1280 + (40 * w + slot) * 8;
#pragma unroll
    for (int i = 0; i < 5; i++) __builtin_amdgcn_raw_buffer_store_b64((u32x2){o[2 * i], o[2 * i + 1]}, srdX1, xo + i * 64, 0, 16);
    STAGE(x[h] = up(o[h]))
    MARK(7);
    float mean, rinv;
    SB0;
    ln_stats(x, red, w, lane, mean, rinv);
    {
      const f32x2 m2 = {mean, mean}, r2 = {rinv, rinv};
#pragma unroll
      for (int i = 0; i < 5; i++) {
        const u32x4 m0 = *(const u32x4*)(mdp + i * 384 + 16);
        const u32x2 m1 = *(const u32x2*)(mdp + i * 384 + 32);
        x[2 * i] = (x[2 * i] - m2) * r2 * up(m0[2]) + up(m1[0]);
        x[2 * i + 1] = (x[2 * i + 1] - m2) * r2 * up(m0[3]) + up(m1[1]);
      }
    }
    MARK(8);
    SB0;
    {
      bload2<0>(bb[0], vb, srdB1, 0); bload2<32>(bb[1], vb, srdB1, 0); bload2<64>(bb[2], vb, srdB1, 0); bload2<96>(bb[3], vb, srdB1, 0);
      bload2<128>(bb[4], vb, srdB1, 0); bload2<160>(bb[5], vb, srdB1, 0); bload2<192>(bb[6], vb, srdB1, 0);
    }
    SB0;
    ln_stats(x, red + 32, w, lane, mean, rinv);
    {
      const f32x2 m3 = {mean, mean}, r3 = {rinv, rinv};
      STAGE(o[h] = pk((x[h] - m3) * r3))
      const int rt = tg >> 1, r = 8 * (tg & 1) + tk;
      const __amdgpu_buffer_rsrc_t srdY3 = mksrd(ws + WS_Y3F, 2621440);
      const u32 yo = (rt >> 1) * 40960 + (rt & 1) * 1024 + ((2 * (slot >> 2) + (slot & 1)) * 16 + r) * 16 + 8 * ((slot >> 1) & 1) + 5 * w * 2048;
#pragma unroll
      for (int i = 0; i < 5; i++) __builtin_amdgcn_raw_buffer_store_b64((u32x2){o[2 * i], o[2 * i + 1]}, srdY3, yo + i * 2048, 0, 16);
    }
#undef STAGE
  }
  MARK(9);
  asm volatile("s_waitcnt vmcnt(0)" : "+v"(bb[0]), "+v"(bb[1]), "+v"(bb[2]), "+v"(bb[3]), "+v"(bb[4]), "+v"(bb[5]), "+v"(bb[6]) :: "memory");
  __builtin_amdgcn_s_barrier();
  if (tid == 0) signal(ws, WS_FA + (tile * 16 + g) * 64, tag);
  MARK(10);
  w2_loads<W2_M0, W2_M1>(W2, c2);
  if (w == 0) poll_tags(ws + WS_FA + tile * 1024, 64, 16, tag, lane);
  __builtin_amdgcn_s_barrier();
  MARK(11);
  f32x4 hacc[7][2];
  {
    G1C c1;
    const int i16 = lane & 15, jj = i16 >> 2, p = i16 & 3;
    c1.srdY = mksrd(ws + WS_Y3F, 2621440); c1.l16 = l16;
    c1.rot1 = (5 * (j & 7)) >> 1;
    c1.ybase = (tile * 4 + w) * 40960;
    c1.b0 = lds0 + L_W1 + (16 * (q >> 1) + 4 * (q & 1) + jj) * 224 + 8 * p;
    G1S s;
    asm volatile("s_nop 4");
#pragma unroll
    for (int k = 0; k < G1R; k++) { const u32 yo = c1.ybase + rotk(k, c1.rot1, 20) * 2048; bload_sc1<0>(s.Y[k][0], l16, c1.srdY, yo); bload_sc1<1024>(s.Y[k][1], l16, c1.srdY, yo); }
#pragma unroll
    for (int f = 0; f < 7; f++) {
      const f32x4 bv = {lo16(bb[f][0]), hi16(bb[f][0]), lo16(bb[f][1]), hi16(bb[f][1])};
      s.acc[f][0] = bv; s.acc[f][1] = bv;
      asm volatile("" : "+v"(s.acc[f][0]), "+v"(s.acc[f][1]));
    }
    const u32 ab0 = c1.b0 + c1.rot1 * 7168;
    s.A0[0] = g1_lda<0>(ab0); s.A0[1] = g1_lda<1>(ab0); s.A0[2] = g1_lda<2>(ab0); s.A0[3] = g1_lda<3>(ab0);
    s.A0[4] = g1_lda<4>(ab0); s.A0[5] = g1_lda<5>(ab0); s.A0[6] = g1_lda<6>(ab0);
    SB0;
    g1_step<0>(s, W2, c1, c2);
    asm volatile("s_nop 7\n\ts_nop 1");
#pragma unroll
    for (int f = 0; f < 7; f++) { asm volatile("" : "+v"(s.acc[f][0]), "+v"(s.acc[f][1])); hacc[f][0] = s.acc[f][0]; hacc[f][1] = s.acc[f][1]; }
  }
  MARK(12);
  G2S s2_;
  {
    const __amdgpu_buffer_rsrc_t srdH = mksrd(ws + WS_HF, 7077888);
    const int rt0 = 8 * tile + 2 * w;
    const u32 ho = ((rt0 >> 2) * 54 * 4 + (rt0 & 3)) * 1024 + l16 + ((7 * g) >> 1) * 4096;
    auto gel = [&](int f, int tt) -> u32x2 {
      const f32x4 a = hacc[f][tt];
      return (u32x2){pk(gelu2((f32x2){a[0], a[1]})), pk(gelu2((f32x2){a[2], a[3]}))};
    };
    if (g & 1) {
      { const u32x2 a = gel(0, 0), c = gel(0, 1);
        __builtin_amdgcn_raw_buffer_store_b64(a, srdH, ho + 8, 0, 16); __builtin_amdgcn_raw_buffer_store_b64(c, srdH, ho + 1024 + 8, 0, 16); }
#pragma unroll
      for (int i = 0; i < 3; i++) {
        const u32x2 a0 = gel(2 * i + 1, 0), a1 = gel(2 * i + 2, 0), c0 = gel(2 * i + 1, 1), c1 = gel(2 * i + 2, 1);
        if (7 * g + 2 * i + 2 < 108) {
          __builtin_amdgcn_raw_buffer_store_b128((u32x4){a0[0], a0[1], a1[0], a1[1]}, srdH, ho + (1 + i) * 4096, 0, 16);
          __builtin_amdgcn_raw_buffer_store_b128((u32x4){c0[0], c0[1], c1[0], c1[1]}, srdH, ho + (1 + i) * 4096 + 1024, 0, 16);
        }
      }
    } else {
#pragma unroll
      for (int i = 0; i < 3; i++) {
        const u32x2 a0 = gel(2 * i, 0), a1 = gel(2 * i + 1, 0), c0 = gel(2 * i, 1), c1 = gel(2 * i + 1, 1);
        __builtin_amdgcn_raw_buffer_store_b128((u32x4){a0[0], a0[1], a1[0], a1[1]}, srdH, ho + i * 4096, 0, 16);
        __builtin_amdgcn_raw_buffer_store_b128((u32x4){c0[0], c0[1], c1[0], c1[1]}, srdH, ho + i * 4096 + 1024, 0, 16);
      }
      { const u32x2 a = gel(6, 0), c = gel(6, 1);
        __builtin_amdgcn_raw_buffer_store_b64(a, srdH, ho + 3 * 4096, 0, 16); __builtin_amdgcn_raw_buffer_store_b64(c, srdH, ho + 3 * 4096 + 1024, 0, 16); }
    }
  }
  MARK(13);
  waitv<0>();
  __builtin_amdgcn_s_barrier();
  if (tid == 0) signal(ws, WS_FB + (tile * 16 + g) * 64, tag);
  {
    const u32 vo = l16 + rotk(12, c2.rot2, 14) * 5120, v1 = l16 + rotk(13, c2.rot2, 14) * 5120;
    if constexpr (W2_M0 + W2_M1 + W2_G1 < 60) w2_loads<W2_M0 + W2_M1 + W2_G1, 60 - W2_M0 - W2_M1 - W2_G1>(W2, c2);
    bload_sc1<0>(s2_.WX[0], vo, c2.srd, 0); bload_sc1<1024>(s2_.WX[1], vo, c2.srd, 0); bload_sc1<2048>(s2_.WX[2], vo, c2.srd, 0); bload_sc1<3072>(s2_.WX[3], vo, c2.srd, 0);
    bload_sc1<0>(s2_.WX[4], vo + 4096, c2.srd, 0);
    bload_sc1<0>(s2_.WX[5], v1, c2.srd, 0); bload_sc1<1024>(s2_.WX[6], v1, c2.srd, 0); bload_sc1<2048>(s2_.WX[7], v1, c2.srd, 0); bload_sc1<3072>(s2_.WX[8], v1, c2.srd, 0);
    bload_sc1<0>(s2_.WX[9], v1 + 4096, c2.srd, 0);
  }
  MARK(14);
  const int cc = lane & 15, tok = 64 * TT + 16 * w + cc;
  G2C cg2;
  cg2.l16 = l16; cg2.rot2 = c2.rot2;
  cg2.srdH = mksrd(ws + WS_HF + (size_t)TT * 221184 + ks0 * 4096, nks * 4096);
  cg2.srdX = mksrd(ws + WS_X1, 2621440); cg2.srdB2 = mksrd(b2, D * 2); cg2.srdMD = srdMD;
  cg2.vx = tok * 1280 + (80 * cg + 4 * q) * 2; cg2.vb2 = (80 * cg + 4 * q) * 2; cg2.vg = (20 * cg + q) * 48 + 40;
  if (w == 0) poll_tags(ws + WS_FB + (TT >> 1) * 1024, 64, 16, tag, lane);
  __builtin_amdgcn_s_barrier();
  MARK(15);
  asm volatile("s_nop 4");
  g2_hload<0>(s2_, cg2); g2_hload<1>(s2_, cg2); g2_hload<2>(s2_, cg2);
  asm volatile("s_waitcnt vmcnt(12)" : "+v"(s2_.WX[0]), "+v"(s2_.WX[1]), "+v"(s2_.WX[2]), "+v"(s2_.WX[3]), "+v"(s2_.WX[4]), "+v"(s2_.WX[5]), "+v"(s2_.WX[6]), "+v"(s2_.WX[7]), "+v"(s2_.WX[8]), "+v"(s2_.WX[9]));
  g2_step<0>(s2_, W2, cg2);
  asm volatile("s_nop 7\n\ts_nop 1");
#pragma unroll
  for (int d = 0; d < 5; d++) asm volatile("" : "+v"(s2_.acc[d][0]), "+v"(s2_.acc[d][1]), "+v"(s2_.acc[d][2]), "+v"(s2_.acc[d][3]));
  MARK(16);
  f32x4* redq = (f32x4*)smem;
  f32x4 own[5];
#define G2_OWN(T) { _Pragma("unroll") for (int d = 0; d < 5; d++) { own[d] = s2_.acc[d][T]; \
    _Pragma("unroll") for (int t = 0; t < 4; t++) if (t != T) redq[((T * 4 + t) * 5 + d) * 64 + lane] = s2_.acc[d][t]; } }
  if (w == 0) G2_OWN(0) else if (w == 1) G2_OWN(1) else if (w == 2) G2_OWN(2) else G2_OWN(3)
#undef G2_OWN
  lds_barrier();
  asm volatile("s_waitcnt vmcnt(0)" : "+v"(s2_.xx[0]), "+v"(s2_.xx[1]), "+v"(s2_.xx[2]), "+v"(s2_.xx[3]), "+v"(s2_.xx[4]), "+v"(s2_.b2v[0]), "+v"(s2_.b2v[1]), "+v"(s2_.b2v[2]), "+v"(s2_.b2v[3]), "+v"(s2_.b2v[4]),
               "+v"(s2_.gg[0]), "+v"(s2_.gg[1]), "+v"(s2_.gg[2]), "+v"(s2_.gg[3]), "+v"(s2_.gg[4]));
  MARK(17);
  {
    const f32x4* r1 = redq + ((((w + 1) & 3) * 4 + w) * 5) * 64 + lane;
    const f32x4* r2 = redq + ((((w + 2) & 3) * 4 + w) * 5) * 64 + lane;
    const f32x4* r3 = redq + ((((w + 3) & 3) * 4 + w) * 5) * 64 + lane;
#pragma unroll
    for (int d = 0; d < 5; d++) {
      const f32x4 sacc = (own[d] + r1[d * 64]) + (r2[d * 64] + r3[d * 64]);
      u32x2 o;
#pragma unroll
      for (int h = 0; h < 2; h++) {
        const f32x2 y = rb2(rb2((f32x2){sacc[2 * h], sacc[2 * h + 1]}) + up(s2_.b2v[d][h]));
        o[h] = pk(up(s2_.xx[d][h]) + rb2(up(s2_.gg[d][h]) * y));
      }
      *(u32x2*)(out + (size_t)tok * D + 80 * cg + 16 * d + 4 * q) = o;
    }
  }
  asm volatile(".Lkmain_end:");
  MARK(18);
  MARK_FLUSH(ws, 0, 24);
  STAMP_END(ws, 0);
}

extern "C" void kernel_launch(void* const* d_in, const int* in_sizes, int n_in, void* d_out, int out_size, void* d_ws, size_t ws_size,
                              hipStream_t stream) {
  const u16* q = (const u16*)d_in[0]; const u16* k = (const u16*)d_in[1]; const u16* v = (const u16*)d_in[2];
  const u16* shk = (const u16*)d_in[3]; const u16* ssk = (const u16*)d_in[4]; const u16* ssD = (const u16*)d_in[5];
  const u16* w1 = (const u16*)d_in[7]; const u16* b1 = (const u16*)d_in[8]; const u16* w2 = (const u16*)d_in[9];
  const u16* b2 = (const u16*)d_in[10]; const u16* c = (const u16*)d_in[11]; const u16* wmod = (const u16*)d_in[12];
  char* ws = (char*)d_ws;
  (void)hipFuncSetAttribute((const void*)k_main, hipFuncAttributeMaxDynamicSharedMemorySize, L_TOTAL);
  k_main<<<256, 256, L_TOTAL, stream>>>(q, k, v, shk, ssk, ssD, c, wmod, w1, b1, w2, b2, ws, (u16*)d_out);
}
```

```cpp
#include <hip/hip_runtime.h>
#include <stdint.h>

typedef unsigned short u16;
typedef unsigned int u32;
typedef short s16x4 __attribute__((ext_vector_type(4)));
typedef short s16x8 __attribute__((ext_vector_type(8)));
typedef __bf16 bf16x8 __attribute__((ext_vector_type(8)));
typedef __bf16 bf16x2 __attribute__((ext_vector_type(2)));
typedef float f32x4 __attribute__((ext_vector_type(4)));
typedef float f32x2 __attribute__((ext_vector_type(2)));
typedef u32 u32x4 __attribute__((ext_vector_type(4)));
typedef u32 u32x2 __attribute__((ext_vector_type(2)));
#define LAS __attribute__((address_space(3)))
#define GAS __attribute__((address_space(1)))
#define DI __device__ __forceinline__

constexpr int S = 2048, D = 640, F = 1728;
constexpr size_t WS_MODP = 0;
constexpr size_t WS_EPOCH = 8192;
constexpr size_t WS_FU = 16384;
constexpr size_t WS_FA = WS_FU + 16384;
constexpr size_t WS_FB = WS_FA + 16384;
constexpr size_t WS_FG = WS_FB + 16384;
constexpr size_t WS_U    = 131072;
constexpr size_t WS_Y2B  = WS_U + 2621440;
constexpr size_t WS_X1   = WS_Y2B + 2621440;
constexpr size_t WS_Y3F  = WS_X1 + 2621440;
constexpr size_t WS_HF   = WS_Y3F + 2621440;
constexpr size_t WS_W2F  = WS_HF + 7077888;

#define STAMP_BEGIN(ws)
#define STAMP_END(ws, kid)
#define MARK_DECL
#define MARK(i)
#define MARK_FLUSH(ws, kid, n)

DI float lo16(u32 x) { return __uint_as_float(x << 16); }
DI float hi16(u32 x) { return __uint_as_float(x & 0xffff0000u); }
DI u32 pk(f32x2 v) { return __builtin_bit_cast(u32, __builtin_convertvector(v, bf16x2)); }
DI u32 pk2(float a, float b) { return pk((f32x2){a, b}); }
DI f32x2 up(u32 x) { return (f32x2){lo16(x), hi16(x)}; }
DI f32x2 rb2(f32x2 v) { return up(pk(v)); }
DI float rbf(float a) { return lo16(pk2(a, a)); }
DI int rfl(int x) { return __builtin_amdgcn_readfirstlane(x); }
DI s16x4 trread(u32 ldsaddr) { return __builtin_amdgcn_ds_read_tr16_b64_v4i16((LAS s16x4*)(size_t)ldsaddr); }
DI bf16x8 cat8(s16x4 a, s16x4 b) { return __builtin_bit_cast(bf16x8, __builtin_shufflevector(a, b, 0, 1, 2, 3, 4, 5, 6, 7)); }
DI f32x4 mfma16(bf16x8 a, bf16x8 b, f32x4 c) { return __builtin_amdgcn_mfma_f32_16x16x32_bf16(a, b, c, 0, 0, 0); }
DI u32 ldsaddr(const void* p) { return (u32)(size_t)(LAS const char*)p; }
DI __amdgpu_buffer_rsrc_t mksrd(const void* p, u32 bytes) { return __builtin_amdgcn_make_buffer_rsrc((void*)p, 0, bytes, 0x00020000); }

[[maybe_unused]] DI float wsum64(float x) {
  x += __int_as_float(__builtin_amdgcn_update_dpp(0, __float_as_int(x), 0xB1, 0xf, 0xf, false));
  x += __int_as_float(__builtin_amdgcn_update_dpp(0, __float_as_int(x), 0x4E, 0xf, 0xf, false));
  x += __int_as_float(__builtin_amdgcn_update_dpp(0, __float_as_int(x), 0x141, 0xf, 0xf, false));
  x += __int_as_float(__builtin_amdgcn_update_dpp(0, __float_as_int(x), 0x128, 0xf, 0xf, false));
  { auto r = __builtin_amdgcn_permlane16_swap(__float_as_uint(x), __float_as_uint(x), false, false); x = __uint_as_float(r[0]) + __uint_as_float(r[1]); }
  { auto r = __builtin_amdgcn_permlane32_swap(__float_as_uint(x), __float_as_uint(x), false, false); x = __uint_as_float(r[0]) + __uint_as_float(r[1]); }
  return x;
}
DI void ln_finish(float sum, float sq, float& mean, float& rinv) {
  const float m = sum * (1.f / D);
  const float var = fmaxf(sq * (1.f / D) - m * m, 0.f);
  mean = rbf(m);
  rinv = __builtin_amdgcn_rcpf(rbf(__builtin_amdgcn_sqrtf(rbf(rbf(var) + 1e-5f))));
}

template <int IMM> DI void bload(u32x4& dst, u32 voff, __amdgpu_buffer_rsrc_t srd, u32 soff) {
  asm volatile("buffer_load_dwordx4 %0, %1, %2, %3 offen offset:%c4" : "=v"(dst) : "v"(voff), "s"(srd), "s"(soff), "i"(IMM));
}
template <int IMM> DI void bload_sc1(u32x4& dst, u32 voff, __amdgpu_buffer_rsrc_t srd, u32 soff) {
  asm volatile("buffer_load_dwordx4 %0, %1, %2, %3 offen offset:%c4 sc1" : "=v"(dst) : "v"(voff), "s"(srd), "s"(soff), "i"(IMM));
}
template <int IMM> DI void bload_a(u32x4& dst, u32 voff, __amdgpu_buffer_rsrc_t srd, u32 soff) {
  asm volatile("buffer_load_dwordx4 %0, %1, %2, %3 offen offset:%c4" : "=a"(dst) : "v"(voff), "s"(srd), "s"(soff), "i"(IMM));
}
template <int IMM> DI void bload_a_sc1(u32x4& dst, u32 voff, __amdgpu_buffer_rsrc_t srd, u32 soff) {
  asm volatile("buffer_load_dwordx4 %0, %1, %2, %3 offen offset:%c4 sc1" : "=a"(dst) : "v"(voff), "s"(srd), "s"(soff), "i"(IMM));
}
DI void touch(u32& dst, u32 voff, __amdgpu_buffer_rsrc_t srd) {
  asm volatile("buffer_load_dword %0, %1, %2, 0 offen sc1" : "=v"(dst) : "v"(voff), "s"(srd));
}
template <int IMM> DI void bload2_sc1(u32x2& dst, u32 voff, __amdgpu_buffer_rsrc_t srd, u32 soff) {
  asm volatile("buffer_load_dwordx2 %0, %1, %2, %3 offen offset:%c4 sc1" : "=v"(dst) : "v"(voff), "s"(srd), "s"(soff), "i"(IMM));
}
template <int IMM> DI void bload2(u32x2& dst, u32 voff, __amdgpu_buffer_rsrc_t srd, u32 soff) {
  asm volatile("buffer_load_dwordx2 %0, %1, %2, %3 offen offset:%c4" : "=v"(dst) : "v"(voff), "s"(srd), "s"(soff), "i"(IMM));
}
DI void dma16(u32 m0v, u32 voff, __amdgpu_buffer_rsrc_t srd, u32 soff) {
  asm volatile("s_mov_b32 m0, %0\n\ts_nop 0\n\tbuffer_load_dwordx4 %1, %2, %3 offen lds" ::"s"(m0v), "v"(voff), "s"(srd), "s"(soff) : "m0");
}
DI void dma4(u32 m0v, u32 voff, __amdgpu_buffer_rsrc_t srd, u32 soff) {
  asm volatile("s_mov_b32 m0, %0\n\ts_nop 0\n\tbuffer_load_dword %1, %2, %3 offen lds" ::"s"(m0v), "v"(voff), "s"(srd), "s"(soff) : "m0");
}
template <int IMM> DI void dsread64(u32x2& dst, u32 addr) { asm volatile("ds_read_b64 %0, %1 offset:%c2" : "=v"(dst) : "v"(addr), "i"(IMM)); }
typedef u32x4 frag;
DI void mfma_vv(f32x4& acc, const frag& a, const frag& b) { asm volatile("v_mfma_f32_16x16x32_bf16 %0, %1, %2, %0" : "+v"(acc) : "v"(a), "v"(b)); }
DI void mfma_vv0(f32x4& acc, const frag& a, const frag& b) { asm volatile("v_mfma_f32_16x16x32_bf16 %0, %1, %2, 0" : "=&v"(acc) : "v"(a), "v"(b)); }
DI void mfma_av(f32x4& acc, const frag& a, const frag& b) { asm volatile("v_mfma_f32_16x16x32_bf16 %0, %1, %2, %0" : "+v"(acc) : "a"(a), "v"(b)); }
DI void mfma_av0(f32x4& acc, const frag& a, const frag& b) { asm volatile("v_mfma_f32_16x16x32_bf16 %0, %1, %2, 0" : "=&v"(acc) : "a"(a), "v"(b)); }
template <int N> DI void waitv() { asm volatile("s_waitcnt vmcnt(%c0)" ::"i"(N) : "memory"); }
template <int N> DI void waitv2(u32x4& a, u32x4& b) { asm volatile("s_waitcnt vmcnt(%c2)" : "+v"(a), "+v"(b) : "i"(N)); }
template <int N> DI void waitv4(u32x4& a, u32x4& b, u32x4& c, u32x4& d) { asm volatile("s_waitcnt vmcnt(%c4)" : "+v"(a), "+v"(b), "+v"(c), "+v"(d) : "i"(N)); }
DI frag catf(s16x4 a, s16x4 b) { return __builtin_bit_cast(frag, __builtin_shufflevector(a, b, 0, 1, 2, 3, 4, 5, 6, 7)); }
#define SB0 __builtin_amdgcn_sched_barrier(0)

constexpr int CV_RS = 1184;
constexpr int CV_KB = 4128;
constexpr int CV_WB = CV_KB + 4 * CV_RS;
template <int OFF> DI frag cv_ldB(u32 vb) { return catf(*(LAS const s16x4*)(size_t)(vb + OFF), *(volatile LAS const s16x4*)(size_t)(vb + OFF + 8)); }
template <int G> constexpr int cv_off0() { return 2 * (G >> 4) * CV_RS + 64 * (G & 15); }
template <int G> constexpr int cv_off1() { return (2 * (G >> 4) + 1) * CV_RS + 64 * (15 - (G & 15)); }
template <int G, int I> DI void cv_one(f32x4 (&acc)[4][8], const frag (&A)[16], const frag& B0, const frag& B1) {
  constexpr int R = 2 * (G >> 4), N = G & 15;
  constexpr int c0 = N <= 1 ? 0 : N / 2;
  constexpr int n0 = 8 - c0;
  if constexpr (I < n0) {
    constexpr int cb = c0 + I, s = 1 - N + 2 * cb;
    if constexpr (N == 0) mfma_vv0(acc[R][cb], A[s], B0); else mfma_vv(acc[R][cb], A[s], B0);
  } else {
    constexpr int c1 = (15 - N) / 2, cb = c1 + (I - n0), s = N - 14 + 2 * cb;
    static_assert(cb <= 7 && s >= 0 && s <= 15, "");
    if constexpr (s == 0) mfma_vv0(acc[R + 1][cb], A[s], B1); else mfma_vv(acc[R + 1][cb], A[s], B1);
  }
}
struct W1C { u32 vo[7]; u32 m0b, sob; __amdgpu_buffer_rsrc_t srd; };
template <int N, int R> DI void w1_m0(const W1C& c) { asm volatile("s_add_u32 m0, %0, %1" ::"s"(c.m0b), "i"(N * 4 * 7168 + R * 1024) : "scc", "m0"); }
template <int N, int R> DI void w1_ld(const W1C& c) {
  asm volatile("buffer_load_dwordx4 %0, %1, %2 offen lds" ::"v"(c.vo[R]), "s"(c.srd), "s"(c.sob + N * 4 * (32 * F * 2)));
}
template <int N, int R> DI void w1_piece(const W1C& c) { w1_m0<N, R>(c); asm volatile("s_nop 0"); w1_ld<N, R>(c); }
template <int N> DI void w1_step(const W1C& c) {
  w1_piece<N, 0>(c); w1_piece<N, 1>(c); w1_piece<N, 2>(c); w1_piece<N, 3>(c); w1_piece<N, 4>(c); w1_piece<N, 5>(c); w1_piece<N, 6>(c);
}
template <int G> DI void cv_steps(f32x4 (&acc)[4][8], const frag (&A)[16], u32 vb, frag P0, frag P1, frag Q0, frag Q1, const W1C& wc) {
  frag N0, N1;
  cv_one<G, 0>(acc, A, P0, P1); SB0;
  if constexpr (G + 2 < 32) N0 = cv_ldB<cv_off0<G + 2>()>(vb);
  SB0; cv_one<G, 1>(acc, A, P0, P1); cv_one<G, 2>(acc, A, P0, P1); SB0;
  if constexpr (G + 2 < 32) N1 = cv_ldB<cv_off1<G + 2>()>(vb);
  SB0; cv_one<G, 3>(acc, A, P0, P1); cv_one<G, 4>(acc, A, P0, P1);
  if constexpr (G < 28) w1_m0<G / 7, G % 7>(wc);
  cv_one<G, 5>(acc, A, P0, P1);
  if constexpr (G < 28) w1_ld<G / 7, G % 7>(wc);
  cv_one<G, 6>(acc, A, P0, P1); cv_one<G, 7>(acc, A, P0, P1); cv_one<G, 8>(acc, A, P0, P1);
  SB0;
  if constexpr (G + 1 < 32) cv_steps<G + 1>(acc, A, vb, Q0, Q1, N0, N1, wc);
}
constexpr int L_W1 = 0;
constexpr int L_Y = 143360;
constexpr int L_MOD = L_Y + 10240;
constexpr int L_RED = L_MOD + 8192;
constexpr int L_TOTAL = 163840;
constexpr int L_CONV = L_TOTAL - 4 * CV_WB;
static_assert(L_CONV >= 16 * 7168 && (L_CONV & 15) == 0 && L_RED + 1024 <= L_TOTAL, "");

typedef unsigned long long u64;
DI u64 call_tag(u32 epoch) { const u32 t = epoch + 1u; return (u64)t | ((u64)(~t) << 32); }
DI void signal(char* ws, size_t off, u64 tag) { __hip_atomic_store((GAS u64*)(ws + off), tag, __ATOMIC_RELAXED, __HIP_MEMORY_SCOPE_AGENT); }
DI void poll_tags(const char* base, int stride, int n, u64 tag, int lane) {
  const GAS u64* p = (const GAS u64*)(base + (lane < n ? lane : n - 1) * stride);
  for (u32 spins = 0; spins < (1u << 18); spins++) {
    const u64 v = __hip_atomic_load(p, __ATOMIC_RELAXED, __HIP_MEMORY_SCOPE_AGENT);
    if (__all(v == tag)) break;
    __builtin_amdgcn_s_sleep(1);
  }
}
DI void poll_tags256(const char* base, u64 tag, int lane) {
  const __amdgpu_buffer_rsrc_t srd = mksrd(base, 2048);
  const u32 lo = (u32)tag, hi = (u32)(tag >> 32);
  for (u32 spins = 0; spins < (1u << 18); spins++) {
    asm volatile("" ::: "memory");
    const u32x4 a = __builtin_amdgcn_raw_buffer_load_b128(srd, lane * 32, 0, 16), b = __builtin_amdgcn_raw_buffer_load_b128(srd, lane * 32 + 16, 0, 16);
    const bool ok = a[0] == lo && a[1] == hi && a[2] == lo && a[3] == hi && b[0] == lo && b[1] == hi && b[2] == lo && b[3] == hi;
    if (__all(ok)) break;
    __builtin_amdgcn_s_sleep(1);
  }
}
DI void lds_barrier() { asm volatile("s_waitcnt lgkmcnt(0)" ::: "memory"); __builtin_amdgcn_s_barrier(); asm volatile("" ::: "memory"); }

DI float xsum8_16_32(float x) {
  x += __int_as_float(__builtin_amdgcn_update_dpp(0, __float_as_int(x), 0x128, 0xf, 0xf, false));
  { auto r = __builtin_amdgcn_permlane16_swap(__float_as_uint(x), __float_as_uint(x), false, false); x = __uint_as_float(r[0]) + __uint_as_float(r[1]); }
  { auto r = __builtin_amdgcn_permlane32_swap(__float_as_uint(x), __float_as_uint(x), false, false); x = __uint_as_float(r[0]) + __uint_as_float(r[1]); }
  return x;
}
DI void ln_stats(const f32x2 (&x)[10], f32x2* red, int w, int lane, float& mean, float& rinv) {
  f32x2 a = ((x[0] + x[1]) + (x[2] + x[3])) + ((x[4] + x[5]) + (x[6] + x[7])) + (x[8] + x[9]);
  f32x2 b0 = x[0] * x[0], b1 = x[1] * x[1];
#pragma unroll
  for (int h = 2; h < 10; h += 2) { b0 += x[h] * x[h]; b1 += x[h + 1] * x[h + 1]; }
  b0 += b1;
  const float s1 = xsum8_16_32(a[0] + a[1]), s2 = xsum8_16_32(b0[0] + b0[1]);
  if (lane < 8) red[w * 8 + lane] = (f32x2){s1, s2};
  lds_barrier();
  const int tk = lane & 7;
  const f32x2 t = (red[tk] + red[8 + tk]) + (red[16 + tk] + red[24 + tk]);
  ln_finish(t[0], t[1], mean, rinv);
}
DI void ln1(const u32x2 (&xr)[5], u32 (&park)[10], f32x2* red, int w, int lane) {
  f32x2 x[10];
#pragma unroll
  for (int i = 0; i < 5; i++) { x[2 * i] = up(xr[i][0]); x[2 * i + 1] = up(xr[i][1]); }
  float mean, rinv;
  ln_stats(x, red, w, lane, mean, rinv);
  const f32x2 mm = {mean, mean}, rr = {rinv, rinv};
#pragma unroll
  for (int h = 0; h < 10; h++) x[h] = rb2(x[h] - mm);
#pragma unroll
  for (int h = 0; h < 10; h++) { const u32 n = pk(x[h] * rr); asm volatile("v_accvgpr_write_b32 %0, %1" : "=a"(park[h]) : "v"(n)); }
}
struct KVI { u32x2 k[7], v[4]; u32x4 s0, s1; };
template <int NT> DI void kv_item(const u32x2* kr, const u32x2* vr, const u32x4& s0, const u32x4& s1, char* dst) {
  const f32x2 sk[2][4] = {{{lo16(s0[0]), lo16(s0[2])}, {hi16(s0[0]), hi16(s0[2])}, {lo16(s0[1]), lo16(s0[3])}, {hi16(s0[1]), hi16(s0[3])}},
                          {{lo16(s1[0]), lo16(s1[2])}, {hi16(s1[0]), hi16(s1[2])}, {lo16(s1[1]), lo16(s1[3])}, {hi16(s1[1]), hi16(s1[3])}}};
  f32x2 kf[NT + 3][2], a[NT][2];
#pragma unroll
  for (int i = 0; i < NT + 3; i++) { kf[i][0] = up(kr[i][0]); kf[i][1] = up(kr[i][1]); }
#define KVS(EXPR) _Pragma("unroll") for (int r = 0; r < NT; r++) { _Pragma("unroll") for (int h = 0; h < 2; h++) { EXPR; } }
  KVS(a[r][h] = rb2(kf[r][h] * sk[h][3]))
  f32x2 pqt[NT][2];
  KVS(pqt[r][h] = rb2(kf[r + 1][h] * sk[h][2]))
  KVS(a[r][h] = rb2(a[r][h] + pqt[r][h]))
  KVS(pqt[r][h] = rb2(kf[r + 2][h] * sk[h][1]))
  KVS(a[r][h] = rb2(a[r][h] + pqt[r][h]))
  KVS(pqt[r][h] = rb2(kf[r + 3][h] * sk[h][0]))
  KVS(a[r][h] = rb2(a[r][h] + pqt[r][h]))
#undef KVS
#pragma unroll
  for (int r = 0; r < NT; r++) *(u32x2*)(dst + r * 176) = (u32x2){pk(a[r][0] * up(vr[r][0])), pk(a[r][1] * up(vr[r][1]))};
}
struct KVR { u32x2 kA[7], vA[4], kB[4], vB; u32x4 sA0, sA1, sB0, sB1; u32 dA, dB; };
constexpr int P_RED = 49152;
constexpr int P_K = 24576, P_V = P_K + 11264, P_S = P_V + 10240;
DI void prep_request(const u16* __restrict__ k, const u16* __restrict__ v, const u16* __restrict__ shk, u32 lds0, int xcd, int tt, int w, int lane) {
  const __amdgpu_buffer_rsrc_t sK = mksrd(k, S * D * 2), sV = mksrd(v, S * D * 2), sS = mksrd(shk + 80 * xcd * 4, 640);
  const u32 col = 160 * xcd;
#pragma unroll
  for (int n = 0; n < 3; n++) {
    const u32 c = 64 * (w + 4 * n) + lane, row = (c * 6554u) >> 16, o = row * (D * 2 - 160) + c * 16 + col;
    if (n < 2 || w < 3) dma16(lds0 + P_K + (w + 4 * n) * 1024, c < 670 ? o + (64 * tt - 3) * (D * 2) : 0x80000000u, sK, 0);
    if (n < 2 || w < 2) dma16(lds0 + P_V + (w + 4 * n) * 1024, o + 64 * tt * (D * 2), sV, 0);
  }
  if (w == 3) dma16(lds0 + P_S, lane * 16, sS, 0);
  waitv<0>();
}
DI void prep_read(KVR& r, const char* smem, int tid) {
  const u32 mgA = ((u32)tid * 3277u) >> 16, cqA = tid - 20 * mgA;
  const u32 i2 = 256 + (tid >> 2), mgB = (i2 * 3277u) >> 16, cqB = i2 - 20 * mgB, tB = 4 * mgB + (tid & 3);
  const char* pA = smem + 4 * mgA * 160 + 8 * cqA;
  const char* pB = smem + tB * 160 + 8 * cqB;
#pragma unroll
  for (int i = 0; i < 7; i++) r.kA[i] = *(const u32x2*)(pA + P_K + i * 160);
#pragma unroll
  for (int i = 0; i < 4; i++) r.vA[i] = *(const u32x2*)(pA + P_V + i * 160);
#pragma unroll
  for (int i = 0; i < 4; i++) r.kB[i] = *(const u32x2*)(pB + P_K + i * 160);
  r.vB = *(const u32x2*)(pB + P_V);
  r.sA0 = *(const u32x4*)(smem + P_S + 32 * cqA); r.sA1 = *(const u32x4*)(smem + P_S + 32 * cqA + 16);
  r.sB0 = *(const u32x4*)(smem + P_S + 32 * cqB); r.sB1 = *(const u32x4*)(smem + P_S + 32 * cqB + 16);
  r.dA = 4 * mgA * 176 + 8 * cqA; r.dB = tB * 176 + 8 * cqB;
}
DI void prep_finish(u32x2 (&xr)[5], char* __restrict__ ws, char* smem, int xcd, int tt, int tid, int lane, int w, u64 tag) {
  KVR r;
  prep_read(r, smem, tid);
  kv_item<4>(r.kA, r.vA, r.sA0, r.sA1, smem + r.dA);
  kv_item<1>(r.kB, &r.vB, r.sB0, r.sB1, smem + r.dB);
  lds_barrier();
  char* us = smem + 12288;
  {
    const int grp = lane >> 4, i16 = lane & 15, jj = i16 >> 2, p = i16 & 3;
#pragma unroll
    for (int n = 0; n < 3; n++) {
      const u32 task = 4 * (w + 4 * n) + grp;
      if (n < 2 || w < 2) {
        const u32 hr = (task * 52429u) >> 18, cgrp = task - 5 * hr, rho = hr >> 1, half = hr & 1;
        const u32 base = ldsaddr(smem) + (4 * (8 * half + jj) + rho) * 176 + (16 * cgrp + 4 * p) * 2;
        *(frag*)(us + (16 * cgrp + i16) * 144 + rho * 32 + half * 16) = catf(trread(base), trread(base + 16 * 176));
      }
    }
  }
  lds_barrier();
  {
    const __amdgpu_buffer_rsrc_t sU = mksrd(ws + WS_U, 2621440);
    const u32 uo = (tt * D + 80 * xcd + (lane >> 3)) * 128 + (lane & 7) * 16;
#pragma unroll
    for (int n = 0; n < 3; n++) {
      const int c8 = w + 4 * n;
      if (n < 2 || w < 2) __builtin_amdgcn_raw_buffer_store_b128(*(const u32x4*)(us + (8 * c8 + (lane >> 3)) * 144 + (lane & 7) * 16), sU, uo + c8 * 1024, 0, 16);
    }
  }
  asm volatile("s_waitcnt vmcnt(0)" : "+v"(xr[0]), "+v"(xr[1]), "+v"(xr[2]), "+v"(xr[3]), "+v"(xr[4]) :: "memory");
  __builtin_amdgcn_s_barrier();
  if (tid == 0) signal(ws, WS_FU + (xcd * 32 + tt) * 64, tag);
}
DI void q_request(u32x2 (&xr)[5], const u16* __restrict__ qin, int t0, int w, int lane) {
  const __amdgpu_buffer_rsrc_t srdQ = mksrd(qin + (size_t)t0 * D, 10240);
  const u32 vq = (lane & 7) * 1280 + (40 * w + (lane >> 3)) * 8;
  asm volatile("s_nop 4");
  bload2<0>(xr[0], vq, srdQ, 0); bload2<64>(xr[1], vq, srdQ, 0); bload2<128>(xr[2], vq, srdQ, 0); bload2<192>(xr[3], vq, srdQ, 0); bload2<256>(xr[4], vq, srdQ, 0);
}

DI void phase_conv(const u32x4 (&rk)[4], u32 Draw, char* __restrict__ ws, char* smem, int xcd, int j, int w, int lane, u64 tag, const W1C& wc
) {
  const int ch = 80 * xcd + 4 * j + w;
  char* kim = smem + L_CONV + w * CV_WB;
  char* uim = kim + CV_KB;
  if (lane < 32) *(u32x4*)(uim + (lane >> 3) * CV_RS + 16 * (lane & 7)) = (u32x4){0u, 0u, 0u, 0u};
  if (lane < 2) *(u32x4*)(kim + 16 * lane) = (u32x4){0u, 0u, 0u, 0u};
#pragma unroll
  for (int i = 0; i < 4; i++) *(u32x4*)(kim + 32 + 16 * (64 * i + lane)) = rk[i];
  frag A[16];
  {
    const int q = lane >> 4, cc = lane & 15;
    const u32 va = ldsaddr(kim) + 224 - 64 * q - 8 * (cc >> 2) + 8 * (cc & 3);
#pragma unroll
    for (int s = 0; s < 16; s++) A[s] = catf(trread(va + 256 * s + 32), trread(va + 256 * s));
  }
  if (w == 0) poll_tags(ws + WS_FU + xcd * 2048, 64, 32, tag, lane);
  __builtin_amdgcn_s_barrier();
  MARK(22);
  {
    const __amdgpu_buffer_rsrc_t sU = mksrd(ws + WS_U, 2621440);
    const u32 uo = ((lane >> 3) * D + ch) * 128 + (lane & 7) * 16;
    u32x4 ru[4];
#pragma unroll
    for (int i = 0; i < 4; i++) ru[i] = __builtin_amdgcn_raw_buffer_load_b128(sU, uo + i * (8 * D * 128), 0, 16);
    char* ud = uim + ((lane & 7) >> 1) * CV_RS + 128 + 32 * (lane >> 3) + 16 * (lane & 1);
#pragma unroll
    for (int i = 0; i < 4; i++) *(u32x4*)(ud + 256 * i) = ru[i];
  }
  MARK(0);
  const int q = lane >> 4, cc = lane & 15;
  const u32 vb = ldsaddr(uim) + 8 + 8 * cc + 16 * q;
  f32x4 acc[4][8];
  frag P0, P1, Q0, Q1;
  P0 = cv_ldB<cv_off0<0>()>(vb); P1 = cv_ldB<cv_off1<0>()>(vb); Q0 = cv_ldB<cv_off0<1>()>(vb); Q1 = cv_ldB<cv_off1<1>()>(vb);
  MARK(1);
  cv_steps<0>(acc, A, vb, P0, P1, Q0, Q1, wc);
  asm volatile("s_nop 7\n\ts_nop 1");
#pragma unroll
  for (int r = 0; r < 4; r++)
#pragma unroll
    for (int cb = 0; cb < 8; cb += 4) asm volatile("" : "+v"(acc[r][cb]), "+v"(acc[r][cb + 1]), "+v"(acc[r][cb + 2]), "+v"(acc[r][cb + 3]));
  MARK(2);
  const int idx = 4 * (lane == 0 ? 63 : (lane >= 16 ? lane - 16 : lane + 47));
  const u32 ub = ldsaddr(uim) + 2 * (64 + 4 * cc + q);
  char* yl = kim + 32 * cc + 8 * q;
  const float Dch = (w & 1) ? hi16(Draw) : lo16(Draw);
  const f32x2 DD = {Dch, Dch};
  float o[8][4], cy[8][3];
  u32 u01[8], u23[8];
#pragma unroll
  for (int cb = 0; cb < 8; cb++) {
    const LAS u16* up_ = (const LAS u16*)(size_t)(ub + 128 * cb);
    u01[cb] = (u32)up_[0] | ((u32)up_[CV_RS / 2] << 16);
    u23[cb] = (u32)up_[CV_RS] | ((u32)up_[3 * CV_RS / 2] << 16);
  }
#pragma unroll
  for (int cb = 0; cb < 8; cb++) {
    const f32x4 z0 = acc[0][cb], z1 = acc[1][cb], z2 = acc[2][cb], z3 = acc[3][cb];
    o[cb][0] = z0[0]; o[cb][1] = z0[1] + z1[0]; o[cb][2] = z0[2] + z1[1] + z2[0]; o[cb][3] = (z0[3] + z1[2]) + (z2[1] + z3[0]);
    cy[cb][0] = z1[3] + z2[2] + z3[1]; cy[cb][1] = z2[3] + z3[2]; cy[cb][2] = z3[3];
  }
#pragma unroll
  for (int cb = 0; cb < 8; cb++)
#pragma unroll
    for (int e = 0; e < 3; e++) {
      const float t = lane == 63 ? (cb ? cy[cb - 1][e] : 0.f) : cy[cb][e];
      o[cb][e] += __int_as_float(__builtin_amdgcn_ds_bpermute(idx, __float_as_int(t)));
    }
#pragma unroll
  for (int cb = 0; cb < 8; cb++) {
    const f32x2 y01 = rb2((f32x2){o[cb][0], o[cb][1]}) + rb2(up(u01[cb]) * DD), y23 = rb2((f32x2){o[cb][2], o[cb][3]}) + rb2(up(u23[cb]) * DD);
    *(u32x2*)(yl + 512 * cb) = (u32x2){pk(y01), pk(y23)};
  }
  lds_barrier();
  {
    const __amdgpu_buffer_rsrc_t sY = mksrd(ws + WS_Y2B, 2621440);
    const int c4 = (lane >> 1) & 3, tgl = 32 * w + (lane >> 3), hf = lane & 1;
    const char* src = smem + L_CONV + c4 * CV_WB + tgl * 32 + 16 * hf;
    const u32 yo = (tgl * D + 80 * xcd + 4 * j + c4) * 32 + 16 * hf;
    u32x4 v[4];
#pragma unroll
    for (int i = 0; i < 4; i++) v[i] = *(const u32x4*)(src + 256 * i);
#pragma unroll
    for (int i = 0; i < 4; i++) __builtin_amdgcn_raw_buffer_store_b128(v[i], sY, yo + i * (8 * D * 32), 0, 16);
  }
  MARK(3);
}

#define PRE_G 0
#define PRE_R 0
template <int LO, int HI> DI void gemv_request(u32x4 (&r)[20], const u16* __restrict__ wmod, int r0, int tid) {
  const __amdgpu_buffer_rsrc_t sW = mksrd(wmod, D * 6 * D * 2);
  const u32 o = ((tid >> 3) * (6 * D) + 64 * r0 + 8 * (tid & 7)) * 2;
#pragma unroll
  for (int i = LO; i < HI; i++) r[i] = __builtin_amdgcn_raw_buffer_load_b128(sW, o, i * (32 * 6 * D * 2), 0);
}
DI void gemv_finish(const u32x4 (&r)[20], const u16* __restrict__ c, char* __restrict__ ws, char* smem, int r0, int w, int tid, int lane) {
    const __amdgpu_buffer_rsrc_t sWS = mksrd(ws, 65536);
    float* sc = (float*)(smem + L_Y);
    float* red = sc + 640;
    const int col0 = 64 * r0, rg = tid >> 3;
    for (int i = tid; i < D; i += 256) { float x = lo16(c[i]); sc[i] = rbf(x / (1.f + __expf(-x))); }
    __syncthreads();
    f32x2 a[4] = {{0.f, 0.f}, {0.f, 0.f}, {0.f, 0.f}, {0.f, 0.f}};
#pragma unroll
    for (int i = 0; i < 20; i++) {
      const float s = sc[rg + 32 * i];
#pragma unroll
      for (int e = 0; e < 4; e++) a[e] += up(r[i][e]) * (f32x2){s, s};
    }
    float af[8] = {a[0][0], a[0][1], a[1][0], a[1][1], a[2][0], a[2][1], a[3][0], a[3][1]};
#pragma unroll
    for (int e = 0; e < 8; e++) af[e] += __int_as_float(__builtin_amdgcn_update_dpp(0, __float_as_int(af[e]), 0x128, 0xf, 0xf, false));
#pragma unroll
    for (int e = 0; e < 8; e++) { auto rr = __builtin_amdgcn_permlane16_swap(__float_as_uint(af[e]), __float_as_uint(af[e]), false, false); af[e] = __uint_as_float(rr[0]) + __uint_as_float(rr[1]); }
#pragma unroll
    for (int e = 0; e < 8; e++) { auto rr = __builtin_amdgcn_permlane32_swap(__float_as_uint(af[e]), __float_as_uint(af[e]), false, false); af[e] = __uint_as_float(rr[0]) + __uint_as_float(rr[1]); }
    if (lane < 8) { *(f32x4*)(red + w * 64 + 8 * lane) = (f32x4){af[0], af[1], af[2], af[3]}; *(f32x4*)(red + w * 64 + 8 * lane + 4) = (f32x4){af[4], af[5], af[6], af[7]}; }
    __syncthreads();
    if (tid < 32) {
      const int c0 = 2 * tid;
      float s0 = rbf((red[c0] + red[64 + c0]) + (red[128 + c0] + red[192 + c0]));
      float s1 = rbf((red[c0 + 1] + red[65 + c0]) + (red[129 + c0] + red[193 + c0]));
      const int jc = col0 + c0, vv = jc / D, ch = jc - vv * D;
      if (vv == 1 || vv == 4) { s0 += 1.f; s1 += 1.f; }
      const int slot = vv == 0 ? 1 : vv == 1 ? 0 : vv == 2 ? 2 : vv == 3 ? 4 : vv == 4 ? 3 : 5;
      __builtin_amdgcn_raw_buffer_store_b32(pk2(s0, s1), sWS, WS_MODP + ((ch >> 2) * 24 + slot * 4 + (ch & 3)) * 2, 0, 16);
    }
}
template <int LO, int HI> DI void retile_request(u32x4 (&r)[4][4], const u16* __restrict__ w2, int r0, int w, int lane) {
  const __amdgpu_buffer_rsrc_t sW = mksrd(w2, F * D * 2);
  const int wv = (r0 - 60) * 4 + w;
#pragma unroll
  for (int n = LO; n < HI; n++) {
    const int u = wv + 144 * n, uc = u < 540 ? u : 539, ks = uc / 10, cc = uc - 10 * ks;
#pragma unroll
    for (int i = 0; i < 4; i++) r[n][i] = __builtin_amdgcn_raw_buffer_load_b128(sW, ((8 * i + (lane >> 3)) * D + 8 * (lane & 7)) * 2, (32 * ks * D + 64 * cc) * 2, 0);
  }
}
DI void retile_finish(const u32x4 (&r)[4][4], char* __restrict__ ws, char* smem, int r0, int w, int lane) {
    const int wv = (r0 - 60) * 4 + w;
    char* mb = smem + L_Y + w * (32 * 144);
    const int q = lane >> 4, i16 = lane & 15, jj = i16 >> 2, p = i16 & 3;
    const __amdgpu_buffer_rsrc_t sO = mksrd(ws + WS_W2F, 2211840);
    const u32 a0 = ldsaddr(mb) + (4 * q + jj) * 144 + 8 * p;
#pragma unroll
    for (int n = 0; n < 4; n++) {
      const int u = wv + 144 * n, uc = u < 540 ? u : 539, ks = uc / 10, cc = uc - 10 * ks;
#pragma unroll
      for (int i = 0; i < 4; i++) *(u32x4*)(mb + (8 * i + (lane >> 3)) * 144 + 16 * (lane & 7)) = r[n][i];
      frag v[4];
#pragma unroll
      for (int f = 0; f < 4; f++) v[f] = catf(trread(a0 + 32 * f), trread(a0 + 32 * f + 16 * 144));
#pragma unroll
      for (int f = 0; f < 4; f++) {
        const int ft = 4 * cc + f;
        if (u < 540) __builtin_amdgcn_raw_buffer_store_b128(v[f], sO, (((ft / 5) * 54 + ks) * 5 + (ft % 5)) * 1024 + lane * 16, 0, 16);
      }
    }
}

DI f32x2 gelu2(f32x2 x) {
  const float c0 = -2.f * 0.7978845608028654f * 1.4426950408889634f, c1 = c0 * 0.044715f;
  const f32x2 z = x * (x * x * (f32x2){c1, c1} + (f32x2){c0, c0});
  const f32x2 d = (f32x2){__builtin_amdgcn_exp2f(z[0]), __builtin_amdgcn_exp2f(z[1])} + (f32x2){1.f, 1.f};
  return x * (f32x2){__builtin_amdgcn_rcpf(d[0]), __builtin_amdgcn_rcpf(d[1])};
}
DI int rotk(int n, int rot, int m) { int k = n + rot; return k >= m ? k - m : k; }
struct W2C { u32 l16; int rot2; __amdgpu_buffer_rsrc_t srd; };
template <int I> DI void w2_load1(frag (&W2)[60], const W2C& c) {
  const u32 vo = c.l16 + rotk(I / 5, c.rot2, 14) * 5120;
  if constexpr (I % 5 < 4) bload_a_sc1<(I % 5) * 1024>(W2[I], vo, c.srd, 0); else bload_a_sc1<0>(W2[I], vo + 4096, c.srd, 0);
}
template <int I, int N> DI void w2_loads(frag (&W2)[60], const W2C& c) {
  w2_load1<I>(W2, c);
  if constexpr (N > 1) w2_loads<I + 1, N - 1>(W2, c);
}
#define W2_M0V 8
#define W2_M1V 16
#define W2_G1V 20
#define W2_GEV 6
constexpr int W2_M0 = W2_M0V, W2_M1 = W2_M1V, W2_G1 = W2_G1V, W2_GE = W2_GEV;
template <int LO, int N, int LIM> DI void w2_part(frag (&W2)[60], const W2C& c) {
  if constexpr (LO < LIM) w2_loads<LO, (LO + N <= LIM ? N : LIM - LO)>(W2, c);
}

constexpr int G1R = 6;
struct AF { u32x2 lo, hi; };
struct G1S {
  frag Y[G1R][2];
  AF A0[7], A1[7];
  f32x4 acc[7][2];
};
template <int FI> DI void g1_lda(AF& a, u32 ab) {
  asm volatile("ds_read_b64_tr_b16 %0, %2 offset:%c3\n\tds_read_b64_tr_b16 %1, %2 offset:%c4" : "=&v"(a.lo), "=&v"(a.hi) : "v"(ab), "i"(32 * FI), "i"(32 * FI + 1792));
}
DI frag af(const AF& a) { return (frag){a.lo[0], a.lo[1], a.hi[0], a.hi[1]}; }
template <int N> DI void g1_waitA4(AF& a, AF& b, AF& c, AF& d) {
  asm volatile("s_waitcnt lgkmcnt(%c8)" : "+v"(a.lo), "+v"(a.hi), "+v"(b.lo), "+v"(b.hi), "+v"(c.lo), "+v"(c.hi), "+v"(d.lo), "+v"(d.hi) : "i"(N));
}
template <int N> DI void g1_waitA3(AF& a, AF& b, AF& c) {
  asm volatile("s_waitcnt lgkmcnt(%c6)" : "+v"(a.lo), "+v"(a.hi), "+v"(b.lo), "+v"(b.hi), "+v"(c.lo), "+v"(c.hi) : "i"(N));
}
struct G1C { u32 b0, l16, ybase; int rot1; __amdgpu_buffer_rsrc_t srdY; };
template <int KS, int FI>
DI void g1_f(G1S& s, AF (&Ac)[7], AF (&An)[7], frag (&W2)[60], const G1C& c, const W2C& c2, u32 ab, u32 yo) {
  if constexpr (FI == 0) g1_waitA4<6>(Ac[0], Ac[1], Ac[2], Ac[3]);
  if constexpr (FI == 4) g1_waitA3<(KS < 19) ? 8 : 0>(Ac[4], Ac[5], Ac[6]);
  const frag a = af(Ac[FI]);
  mfma_vv(s.acc[FI][0], a, s.Y[KS % G1R][0]);
  if constexpr (KS < 19) g1_lda<FI>(An[FI], ab);
  mfma_vv(s.acc[FI][1], a, s.Y[KS % G1R][1]);
  if constexpr (KS >= 1 && KS - 1 + G1R < 20 && (FI == 1 || FI == 4)) bload_sc1<(FI == 4) * 1024>(s.Y[(KS - 1) % G1R][FI == 4], c.l16, c.srdY, yo);
  if constexpr (KS < W2_G1 && FI == 6) w2_load1<W2_M0 + W2_M1 + KS>(W2, c2);
  if constexpr (FI < 6) g1_f<KS, FI + 1>(s, Ac, An, W2, c, c2, ab, yo);
}
template <int KS> constexpr int g1_younger() {
  int cnt = 0; bool seen = false;
  for (int k = 0; k < G1R; k++) { if (seen) cnt += 2; if (k == KS) seen = true; }
  for (int n = 0; n < KS; n++) {
    if (n >= 1 && n - 1 + G1R < 20) { if (seen) cnt += 2; if (n - 1 + G1R == KS) seen = true; }
    if (n < W2_G1 && seen) cnt += 1;
  }
  return cnt;
}
template <int KS>
DI void g1_step(G1S& s, frag (&W2)[60], const G1C& c, const W2C& c2) {
  waitv2<g1_younger<KS>()>(s.Y[KS % G1R][0], s.Y[KS % G1R][1]);
  const u32 ab = c.b0 + rotk(KS + 1, c.rot1, 20) * 7168;
  const u32 yo = c.ybase + rotk(KS - 1 + G1R, c.rot1, 20) * 2048;
  if constexpr (KS & 1) g1_f<KS, 0>(s, s.A1, s.A0, W2, c, c2, ab, yo);
  else                  g1_f<KS, 0>(s, s.A0, s.A1, W2, c, c2, ab, yo);
  if constexpr (KS < 19) g1_step<KS + 1>(s, W2, c, c2);
}
struct G2S {
  frag H[4][4];
  frag WX[10];
  f32x4 acc[5][4];
  u32x2 xx[5], gg[5], b2v[5];
};
struct G2C { u32 l16, vx, vb2, vg; int rot2; __amdgpu_buffer_rsrc_t srdH, srdX, srdB2, srdMD; };
template <int N, int T> DI void g2_hload1(G2S& s, const G2C& c, u32 vo) { bload_sc1<T * 1024>(s.H[N & 3][T], vo, c.srdH, 0); }
template <int N> DI void g2_hload(G2S& s, const G2C& c) {
  const u32 vo = c.l16 + rotk(N, c.rot2, 14) * 4096;
  g2_hload1<N, 0>(s, c, vo); g2_hload1<N, 1>(s, c, vo); g2_hload1<N, 2>(s, c, vo); g2_hload1<N, 3>(s, c, vo);
}
template <int I> DI void g2_eload(G2S& s, const G2C& c) {
  if constexpr (I < 5) bload2_sc1<32 * I>(s.xx[I], c.vx, c.srdX, 0);
  else if constexpr (I < 10) bload2<32 * (I - 5)>(s.b2v[I - 5], c.vb2, c.srdB2, 0);
  else bload2_sc1<192 * (I - 10)>(s.gg[I - 10], c.vg, c.srdMD, 0);
}
template <int N, int M>
DI void g2_m(G2S& s, frag (&W2)[60], const G2C& c, u32 vo) {
  constexpr int d = M >> 2, t = M & 3;
  if constexpr (N == 0) mfma_av0(s.acc[d][t], W2[d], s.H[0][t]);
  else if constexpr (N < 12) mfma_av(s.acc[d][t], W2[N * 5 + d], s.H[N & 3][t]);
  else mfma_vv(s.acc[d][t], s.WX[(N - 12) * 5 + d], s.H[N & 3][t]);
  if constexpr (N + 3 < 14 && (M == 1 || M == 6 || M == 11 || M == 16)) g2_hload1<N + 3, (M - 1) / 5>(s, c, vo);
  if constexpr (N >= 11 && (M == 1 || M == 5 || M == 9 || M == 13 || M == 17)) g2_eload<5 * (N - 11) + (M - 1) / 4>(s, c);
  if constexpr (M < 19) g2_m<N, M + 1>(s, W2, c, vo);
}
template <int N> constexpr int g2_younger() {
  int cnt = 0; bool seen = false;
  for (int h = 0; h < 3; h++) { if (seen) cnt += 4; if (h == N) seen = true; }
  for (int n = 0; n < N; n++) {
    if (n + 3 < 14) { if (seen) cnt += 4; if (n + 3 == N) seen = true; }
    if (n >= 11 && seen) cnt += 5;
  }
  return cnt;
}
static_assert(g2_younger<0>() == 8 && g2_younger<3>() == 8 && g2_younger<11>() == 8 && g2_younger<12>() == 9 && g2_younger<13>() == 10, "");
template <int N>
DI void g2_step(G2S& s, frag (&W2)[60], const G2C& c) {
  waitv4<g2_younger<N>()>(s.H[N & 3][0], s.H[N & 3][1], s.H[N & 3][2], s.H[N & 3][3]);
  const u32 vo = c.l16 + rotk(N + 3, c.rot2, 14) * 4096;
  g2_m<N, 0>(s, W2, c, vo);
  if constexpr (N < 13) g2_step<N + 1>(s, W2, c);
}

__global__ __launch_bounds__(256, 1) void k_main(const u16* __restrict__ qin, const u16* __restrict__ kin, const u16* __restrict__ vin,
                                                 const u16* __restrict__ shk, const u16* __restrict__ ssk, const u16* __restrict__ ssD,
                                                 const u16* __restrict__ cin, const u16* __restrict__ wmod, const u16* __restrict__ w1,
                                                 const u16* __restrict__ b1, const u16* __restrict__ w2, const u16* __restrict__ b2,
                                                 char* __restrict__ ws, u16* __restrict__ out) {
  STAMP_BEGIN(ws)
  MARK_DECL
  extern __shared__ __attribute__((aligned(16))) char smem[];
  const int tid = threadIdx.x, lane = tid & 63, w = rfl(tid >> 6);
  const int b = blockIdx.x, xcd = b & 7, j = b >> 3;
  const int tile = 4 * (xcd >> 1) + (j >> 3), g = 8 * (xcd & 1) + (j & 7);
  const int TT = 4 * xcd + (j >> 3), cg = j & 7;
  const u32 l16 = lane * 16, lds0 = ldsaddr(smem);
  W1C wc;
#pragma unroll
  for (int r = 0; r < 7; r++) { const u32 idx = 64 * r + lane, row = (idx * 4682u) >> 16; wc.vo[r] = row * (F * 2 - 224) + idx * 16; }
  wc.m0b = lds0 + L_W1 + w * 7168; wc.sob = w * (32 * F * 2) + 224 * g; wc.srd = mksrd(w1, D * F * 2);
  const int tg = 16 * tile + g, t0 = 8 * tg;
  const int tk = lane & 7, slot = lane >> 3, q = lane >> 4;
  const u64 tag = call_tag(__hip_atomic_load((const GAS u32*)(ws + WS_EPOCH), __ATOMIC_RELAXED, __HIP_MEMORY_SCOPE_AGENT));
  asm volatile("s_nop 4");
  prep_request(kin, vin, shk, lds0, xcd, j, w, lane);
  __builtin_amdgcn_s_barrier();
  if (w == 3) {
    unsigned long long pc; u32 len;
    asm volatile("s_getpc_b64 %0\n.Lkmain_here:\n\ts_mov_b32 %1, .Lkmain_end-.Lkmain_here" : "=s"(pc), "=s"(len));
    dma4(lds0 + L_RED + 768, (j + 32 * lane) * 128, mksrd((const void*)pc, len), 0);
  }
  if (w == 2 && j < 2) {
    const char* p = ws + (size_t)(lane < 13 ? lane : 0) * 2097152;
    if (lane == 13 || lane == 14) p = (const char*)out + (lane - 13) * 2097152;
    if (lane == 15 || lane == 16) p = (const char*)w1 + (lane - 15) * 2097152;
    if (lane == 17 || lane == 18) p = (const char*)w2 + (lane - 17) * 2097152;
    if (lane >= 19 && lane <= 21) p = (const char*)wmod + (lane - 19) * 2097152;
    if (lane == 22) p = (const char*)b1;
    if (lane == 23) p = (const char*)b2;
    if (lane >= 24) p = (const char*)cin;
    asm volatile("s_mov_b32 m0, %0\n\ts_nop 0\n\tglobal_load_lds_dword %1, off sc1" ::"s"(lds0 + L_RED + 768), "v"(p) : "m0");
  }
  MARK(21);
  u32x2 xr[5];
  u32 park[10];
  const int r0 = (j - 20) * 8 + xcd;
  if (j < 20) {
    const int ch = 80 * xcd + 4 * j + w;
    const __amdgpu_buffer_rsrc_t sF = mksrd(ssk + (size_t)ch * S, S * 2);
    u32x4 rk[4];
#pragma unroll
    for (int i = 0; i < 4; i++) rk[i] = __builtin_amdgcn_raw_buffer_load_b128(sF, l16 + 1024 * i, 0, 0);
    const u32 Draw = __builtin_amdgcn_raw_buffer_load_b32(mksrd(ssD, D * 2), (ch >> 1) * 4, 0, 0);
    q_request(xr, qin, t0, w, lane);
    prep_finish(xr, ws, smem, xcd, j, tid, lane, w, tag);
    MARK(23);
    ln1(xr, park, (f32x2*)(smem + P_RED), w, lane);
    phase_conv(rk, Draw, ws, smem, xcd, j, w, lane, tag, wc
    );
  } else if (r0 < 60) {
    u32x4 r[20];
    q_request(xr, qin, t0, w, lane);
    gemv_request<0, PRE_G>(r, wmod, r0, tid);
    prep_finish(xr, ws, smem, xcd, j, tid, lane, w, tag);
    gemv_request<PRE_G, 20>(r, wmod, r0, tid);
    SB0;
    gemv_finish(r, cin, ws, smem, r0, w, tid, lane);
    ln1(xr, park, (f32x2*)(smem + P_RED), w, lane);
  } else {
    u32x4 r[4][4];
    q_request(xr, qin, t0, w, lane);
    retile_request<0, PRE_R>(r, w2, r0, w, lane);
    prep_finish(xr, ws, smem, xcd, j, tid, lane, w, tag);
    retile_request<PRE_R, 4>(r, w2, r0, w, lane);
    SB0;
    retile_finish(r, ws, smem, r0, w, lane);
    ln1(xr, park, (f32x2*)(smem + P_RED), w, lane);
  }
  waitv<0>();
  __builtin_amdgcn_s_barrier();
  MARK(4);
  if (tid < 8) signal(ws, WS_FG + tid * 2048 + b * 8, tag);
  const int ks0 = w < 2 ? 14 * w : 28 + 13 * (w - 2), nks = w < 2 ? 14 : 13;
  W2C c2;
  c2.l16 = l16; c2.srd = mksrd(ws + WS_W2F + (size_t)cg * 276480 + ks0 * 5120, nks * 5120);
  { int r2 = 2 * cg + 4 * (j >> 3); r2 = r2 >= 14 ? r2 - 14 : r2; c2.rot2 = r2 >= 14 ? r2 - 14 : r2; }
  frag W2[60];
  f32x2 x[10], t[10];
  f32x2* red = (f32x2*)(smem + L_RED);
  if (j >= 20) { w1_step<0>(wc); w1_step<1>(wc); w1_step<2>(wc); w1_step<3>(wc); }
#pragma unroll
  for (int i = 0; i < 5; i++) { x[2 * i] = up(xr[i][0]); x[2 * i + 1] = up(xr[i][1]); }
#pragma unroll
  for (int h = 0; h < 10; h++) { u32 n; asm volatile("v_accvgpr_read_b32 %0, %1" : "=v"(n) : "a"(park[h])); t[h] = up(n); asm volatile("" : "+v"(x[h]), "+v"(t[h])); }
  if (w == 0) poll_tags256(ws + WS_FG + xcd * 2048, tag, lane);
  __builtin_amdgcn_s_barrier();
  if (b == 0 && tid == 0) __hip_atomic_store((GAS u32*)(ws + WS_EPOCH), (u32)tag, __ATOMIC_RELAXED, __HIP_MEMORY_SCOPE_AGENT);
  MARK(5);
  const __amdgpu_buffer_rsrc_t srdMD = mksrd(ws + WS_MODP, 7680);
  u32x2 bb[7];
  {
    const __amdgpu_buffer_rsrc_t srdYB = mksrd(ws + WS_Y2B + (size_t)(tg >> 1) * 20480, 20480);
    const __amdgpu_buffer_rsrc_t srdB1 = mksrd(b1, F * 2);
    u32x4 yr[3], mr[2];
    const u32 v16 = tid * 16, vb = (16 * 7 * g + 4 * q) * 2;
    asm volatile("s_nop 4");
    MARK(20);
    const u32 vy = tid * 32 + 16 * (tg & 1);
    bload_sc1<0>(yr[0], vy, srdYB, 0); bload_sc1<0>(yr[1], vy + 8192, srdYB, 0); bload_sc1<0>(yr[2], vy + 16384, srdYB, 0);
    bload_sc1<0>(mr[0], v16, srdMD, 0); bload_sc1<0>(mr[1], v16 + 4096, srdMD, 0);
    w1_step<4>(wc);
    asm volatile("s_waitcnt vmcnt(7)" : "+v"(yr[0]), "+v"(yr[1]), "+v"(yr[2]), "+v"(mr[0]), "+v"(mr[1]));
    MARK(19);
    *(u32x4*)(smem + L_Y + v16) = yr[0]; *(u32x4*)(smem + L_Y + 4096 + v16) = yr[1];
    if (tid < 128) *(u32x4*)(smem + L_Y + 8192 + v16) = yr[2];
    *(u32x4*)(smem + L_MOD + v16) = mr[0]; *(u32x4*)(smem + L_MOD + 4096 + v16) = mr[1];
    lds_barrier();
    MARK(6);
    const int i16 = lane & 15, jj = i16 >> 2, p = i16 & 3;
    const u32 ya = lds0 + L_Y + (4 * (40 * w + 2 * q + (p >> 1)) + jj) * 16 + 8 * (p & 1);
    const char* mdp = smem + L_MOD + (40 * w + slot) * 48;
    u32 yy[10], w1v[10], sav[10], gav[10];
#pragma unroll
    for (int i = 0; i < 5; i++) {
      const u32x2 yu = __builtin_bit_cast(u32x2, trread(ya + i * 8 * 64));
      const u32x4 m0 = *(const u32x4*)(mdp + i * 384);
      const u32x2 m1 = *(const u32x2*)(mdp + i * 384 + 16);
      yy[2 * i] = yu[0]; yy[2 * i + 1] = yu[1]; w1v[2 * i] = m0[0]; w1v[2 * i + 1] = m0[1]; sav[2 * i] = m0[2]; sav[2 * i + 1] = m0[3]; gav[2 * i] = m1[0]; gav[2 * i + 1] = m1[1];
    }
    u32 o[10];
#define STAGE(EXPR) _Pragma("unroll") for (int h = 0; h < 10; h++) { EXPR; }
    STAGE(t[h] = rb2(t[h] * up(w1v[h])))
    SB0; w2_part<0, 2, W2_M0>(W2, c2); SB0;
    STAGE(t[h] = rb2(t[h] + up(sav[h])))
    SB0; w2_part<2, 2, W2_M0>(W2, c2); SB0;
    STAGE(t[h] = rb2(t[h] * up(yy[h])))
    SB0; w2_part<4, 2, W2_M0>(W2, c2); SB0;
    STAGE(t[h] = rb2(up(gav[h]) * t[h]))
    SB0; w2_part<6, 2, W2_M0>(W2, c2); SB0;
    STAGE(o[h] = pk(x[h] + t[h]))
    const __amdgpu_buffer_rsrc_t srdX1 = mksrd(ws + WS_X1, 2621440);
    const u32 xo = (t0 + tk) * 1280 + (40 * w + slot) * 8;
#pragma unroll
    for (int i = 0; i < 5; i++) __builtin_amdgcn_raw_buffer_store_b64((u32x2){o[2 * i], o[2 * i + 1]}, srdX1, xo + i * 64, 0, 16);
    STAGE(x[h] = up(o[h]))
    MARK(7);
    float mean, rinv;
    SB0; w2_part<8, 3, W2_M0>(W2, c2); SB0;
    ln_stats(x, red, w, lane, mean, rinv);
    SB0; w2_part<11, 3, W2_M0>(W2, c2); SB0;
    {
      const f32x2 m2 = {mean, mean}, r2 = {rinv, rinv};
#pragma unroll
      for (int i = 0; i < 5; i++) {
        const u32x4 m0 = *(const u32x4*)(mdp + i * 384 + 16);
        const u32x2 m1 = *(const u32x2*)(mdp + i * 384 + 32);
        x[2 * i] = (x[2 * i] - m2) * r2 * up(m0[2]) + up(m1[0]);
        x[2 * i + 1] = (x[2 * i + 1] - m2) * r2 * up(m0[3]) + up(m1[1]);
      }
    }
    MARK(8);
    SB0;
    {
      bload2<0>(bb[0], vb, srdB1, 0); bload2<32>(bb[1], vb, srdB1, 0); bload2<64>(bb[2], vb, srdB1, 0); bload2<96>(bb[3], vb, srdB1, 0);
      bload2<128>(bb[4], vb, srdB1, 0); bload2<160>(bb[5], vb, srdB1, 0); bload2<192>(bb[6], vb, srdB1, 0);
    }
    SB0;
    ln_stats(x, red + 32, w, lane, mean, rinv);
    {
      const f32x2 m3 = {mean, mean}, r3 = {rinv, rinv};
      STAGE(o[h] = pk((x[h] - m3) * r3))
      const int rt = tg >> 1, r = 8 * (tg & 1) + tk;
      const __amdgpu_buffer_rsrc_t srdY3 = mksrd(ws + WS_Y3F, 2621440);
      const u32 yo = (rt >> 1) * 40960 + (rt & 1) * 1024 + ((2 * (slot >> 2) + (slot & 1)) * 16 + r) * 16 + 8 * ((slot >> 1) & 1) + 5 * w * 2048;
#pragma unroll
      for (int i = 0; i < 5; i++) __builtin_amdgcn_raw_buffer_store_b64((u32x2){o[2 * i], o[2 * i + 1]}, srdY3, yo + i * 2048, 0, 16);
    }
#undef STAGE
  }
  MARK(9);
  asm volatile("s_waitcnt vmcnt(0)" : "+v"(bb[0]), "+v"(bb[1]), "+v"(bb[2]), "+v"(bb[3]), "+v"(bb[4]), "+v"(bb[5]), "+v"(bb[6]) :: "memory");
  __builtin_amdgcn_s_barrier();
  if (tid == 0) signal(ws, WS_FA + (tile * 16 + g) * 64, tag);
  MARK(10);
  w2_loads<W2_M0, W2_M1>(W2, c2);
  if (w == 0) poll_tags(ws + WS_FA + tile * 1024, 64, 16, tag, lane);
  __builtin_amdgcn_s_barrier();
  MARK(11);
  f32x4 hacc[7][2];
  {
    G1C c1;
    const int i16 = lane & 15, jj = i16 >> 2, p = i16 & 3;
    c1.srdY = mksrd(ws + WS_Y3F, 2621440); c1.l16 = l16;
    c1.rot1 = (5 * (j & 7)) >> 1;
    c1.ybase = (tile * 4 + w) * 40960;
    c1.b0 = lds0 + L_W1 + (16 * (q >> 1) + 4 * (q & 1) + jj) * 224 + 8 * p;
    G1S s;
    asm volatile("s_nop 4");
#pragma unroll
    for (int k = 0; k < G1R; k++) { const u32 yo = c1.ybase + rotk(k, c1.rot1, 20) * 2048; bload_sc1<0>(s.Y[k][0], l16, c1.srdY, yo); bload_sc1<1024>(s.Y[k][1], l16, c1.srdY, yo); }
#pragma unroll
    for (int f = 0; f < 7; f++) {
      const f32x4 bv = {lo16(bb[f][0]), hi16(bb[f][0]), lo16(bb[f][1]), hi16(bb[f][1])};
      s.acc[f][0] = bv; s.acc[f][1] = bv;
      asm volatile("" : "+v"(s.acc[f][0]), "+v"(s.acc[f][1]));
    }
    const u32 ab0 = c1.b0 + c1.rot1 * 7168;
    asm volatile("s_waitcnt lgkmcnt(0)" ::: "memory");
    g1_lda<0>(s.A0[0], ab0); g1_lda<1>(s.A0[1], ab0); g1_lda<2>(s.A0[2], ab0); g1_lda<3>(s.A0[3], ab0);
    g1_lda<4>(s.A0[4], ab0); g1_lda<5>(s.A0[5], ab0); g1_lda<6>(s.A0[6], ab0);
    g1_step<0>(s, W2, c1, c2);
    asm volatile("s_nop 7\n\ts_nop 1");
#pragma unroll
    for (int f = 0; f < 7; f++) { asm volatile("" : "+v"(s.acc[f][0]), "+v"(s.acc[f][1])); hacc[f][0] = s.acc[f][0]; hacc[f][1] = s.acc[f][1]; }
  }
  MARK(12);
  G2S s2_;
  {
    const __amdgpu_buffer_rsrc_t srdH = mksrd(ws + WS_HF, 7077888);
    const int rt0 = 8 * tile + 2 * w;
    const u32 ho = ((rt0 >> 2) * 54 * 4 + (rt0 & 3)) * 1024 + l16 + ((7 * g) >> 1) * 4096;
    auto gel = [&](int f, int tt) -> u32x2 {
      const f32x4 a = hacc[f][tt];
      return (u32x2){pk(gelu2((f32x2){a[0], a[1]})), pk(gelu2((f32x2){a[2], a[3]}))};
    };
    SB0; w2_part<W2_M0 + W2_M1 + W2_G1, W2_GE, 60>(W2, c2); SB0;
    if (g & 1) {
      { const u32x2 a = gel(0, 0), c = gel(0, 1);
        __builtin_amdgcn_raw_buffer_store_b64(a, srdH, ho + 8, 0, 16); __builtin_amdgcn_raw_buffer_store_b64(c, srdH, ho + 1024 + 8, 0, 16); }
#pragma unroll
      for (int i = 0; i < 3; i++) {
        const u32x2 a0 = gel(2 * i + 1, 0), a1 = gel(2 * i + 2, 0), c0 = gel(2 * i + 1, 1), c1 = gel(2 * i + 2, 1);
        if (7 * g + 2 * i + 2 < 108) {
          __builtin_amdgcn_raw_buffer_store_b128((u32x4){a0[0], a0[1], a1[0], a1[1]}, srdH, ho + (1 + i) * 4096, 0, 16);
          __builtin_amdgcn_raw_buffer_store_b128((u32x4){c0[0], c0[1], c1[0], c1[1]}, srdH, ho + (1 + i) * 4096 + 1024, 0, 16);
        }
      }
    } else {
#pragma unroll
      for (int i = 0; i < 3; i++) {
        const u32x2 a0 = gel(2 * i, 0), a1 = gel(2 * i + 1, 0), c0 = gel(2 * i, 1), c1 = gel(2 * i + 1, 1);
        __builtin_amdgcn_raw_buffer_store_b128((u32x4){a0[0], a0[1], a1[0], a1[1]}, srdH, ho + i * 4096, 0, 16);
        __builtin_amdgcn_raw_buffer_store_b128((u32x4){c0[0], c0[1], c1[0], c1[1]}, srdH, ho + i * 4096 + 1024, 0, 16);
      }
      { const u32x2 a = gel(6, 0), c = gel(6, 1);
        __builtin_amdgcn_raw_buffer_store_b64(a, srdH, ho + 3 * 4096, 0, 16); __builtin_amdgcn_raw_buffer_store_b64(c, srdH, ho + 3 * 4096 + 1024, 0, 16); }
    }
  }
  MARK(13);
  waitv<0>();
  __builtin_amdgcn_s_barrier();
  if (tid == 0) signal(ws, WS_FB + (tile * 16 + g) * 64, tag);
  {
    const u32 vo = l16 + rotk(12, c2.rot2, 14) * 5120, v1 = l16 + rotk(13, c2.rot2, 14) * 5120;
    w2_part<W2_M0 + W2_M1 + W2_G1 + W2_GE, 60, 60>(W2, c2);
    bload_sc1<0>(s2_.WX[0], vo, c2.srd, 0); bload_sc1<1024>(s2_.WX[1], vo, c2.srd, 0); bload_sc1<2048>(s2_.WX[2], vo, c2.srd, 0); bload_sc1<3072>(s2_.WX[3], vo, c2.srd, 0);
    bload_sc1<0>(s2_.WX[4], vo + 4096, c2.srd, 0);
    bload_sc1<0>(s2_.WX[5], v1, c2.srd, 0); bload_sc1<1024>(s2_.WX[6], v1, c2.srd, 0); bload_sc1<2048>(s2_.WX[7], v1, c2.srd, 0); bload_sc1<3072>(s2_.WX[8], v1, c2.srd, 0);
    bload_sc1<0>(s2_.WX[9], v1 + 4096, c2.srd, 0);
  }
  MARK(14);
  const int cc = lane & 15, tok = 64 * TT + 16 * w + cc;
  G2C cg2;
  cg2.l16 = l16; cg2.rot2 = c2.rot2;
  cg2.srdH = mksrd(ws + WS_HF + (size_t)TT * 221184 + ks0 * 4096, nks * 4096);
  cg2.srdX = mksrd(ws + WS_X1, 2621440); cg2.srdB2 = mksrd(b2, D * 2); cg2.srdMD = srdMD;
  cg2.vx = tok * 1280 + (80 * cg + 4 * q) * 2; cg2.vb2 = (80 * cg + 4 * q) * 2; cg2.vg = (20 * cg + q) * 48 + 40;
  if (w == 0) poll_tags(ws + WS_FB + (TT >> 1) * 1024, 64, 16, tag, lane);
  __builtin_amdgcn_s_barrier();
  MARK(15);
  asm volatile("s_nop 4");
  g2_hload<0>(s2_, cg2); g2_hload<1>(s2_, cg2); g2_hload<2>(s2_, cg2);
  asm volatile("s_waitcnt vmcnt(12)" : "+v"(s2_.WX[0]), "+v"(s2_.WX[1]), "+v"(s2_.WX[2]), "+v"(s2_.WX[3]), "+v"(s2_.WX[4]), "+v"(s2_.WX[5]), "+v"(s2_.WX[6]), "+v"(s2_.WX[7]), "+v"(s2_.WX[8]), "+v"(s2_.WX[9]));
  g2_step<0>(s2_, W2, cg2);
  asm volatile("s_nop 7\n\ts_nop 1");
#pragma unroll
  for (int d = 0; d < 5; d++) asm volatile("" : "+v"(s2_.acc[d][0]), "+v"(s2_.acc[d][1]), "+v"(s2_.acc[d][2]), "+v"(s2_.acc[d][3]));
  MARK(16);
  f32x4* redq = (f32x4*)smem;
  f32x4 own[5];
#define G2_OWN(T) { _Pragma("unroll") for (int d = 0; d < 5; d++) { own[d] = s2_.acc[d][T]; \
    _Pragma("unroll") for (int t = 0; t < 4; t++) if (t != T) redq[((T * 4 + t) * 5 + d) * 64 + lane] = s2_.acc[d][t]; } }
  if (w == 0) G2_OWN(0) else if (w == 1) G2_OWN(1) else if (w == 2) G2_OWN(2) else G2_OWN(3)
#undef G2_OWN
  lds_barrier();
  asm volatile("s_waitcnt vmcnt(0)" : "+v"(s2_.xx[0]), "+v"(s2_.xx[1]), "+v"(s2_.xx[2]), "+v"(s2_.xx[3]), "+v"(s2_.xx[4]), "+v"(s2_.b2v[0]), "+v"(s2_.b2v[1]), "+v"(s2_.b2v[2]), "+v"(s2_.b2v[3]), "+v"(s2_.b2v[4]),
               "+v"(s2_.gg[0]), "+v"(s2_.gg[1]), "+v"(s2_.gg[2]), "+v"(s2_.gg[3]), "+v"(s2_.gg[4]));
  MARK(17);
  {
    const f32x4* r1 = redq + ((((w + 1) & 3) * 4 + w) * 5) * 64 + lane;
    const f32x4* r2 = redq + ((((w + 2) & 3) * 4 + w) * 5) * 64 + lane;
    const f32x4* r3 = redq + ((((w + 3) & 3) * 4 + w) * 5) * 64 + lane;
#pragma unroll
    for (int d = 0; d < 5; d++) {
      const f32x4 sacc = (own[d] + r1[d * 64]) + (r2[d * 64] + r3[d * 64]);
      u32x2 o;
#pragma unroll
      for (int h = 0; h < 2; h++) {
        o[h] = pk(up(s2_.xx[d][h]) + up(s2_.gg[d][h]) * ((f32x2){sacc[2 * h], sacc[2 * h + 1]} + up(s2_.b2v[d][h])));
      }
      *(u32x2*)(out + (size_t)tok * D + 80 * cg + 16 * d + 4 * q) = o;
    }
  }
  asm volatile(".Lkmain_end:");
  MARK(18);
  MARK_FLUSH(ws, 0, 24);
  STAMP_END(ws, 0);
}

extern "C" void kernel_launch(void* const* d_in, const int* in_sizes, int n_in, void* d_out, int out_size, void* d_ws, size_t ws_size,
                              hipStream_t stream) {
  const u16* q = (const u16*)d_in[0]; const u16* k = (const u16*)d_in[1]; const u16* v = (const u16*)d_in[2];
  const u16* shk = (const u16*)d_in[3]; const u16* ssk = (const u16*)d_in[4]; const u16* ssD = (const u16*)d_in[5];
  const u16* w1 = (const u16*)d_in[7]; const u16* b1 = (const u16*)d_in[8]; const u16* w2 = (const u16*)d_in[9];
  const u16* b2 = (const u16*)d_in[10]; const u16* c = (const u16*)d_in[11]; const u16* wmod = (const u16*)d_in[12];
  char* ws = (char*)d_ws;
  (void)hipFuncSetAttribute((const void*)k_main, hipFuncAttributeMaxDynamicSharedMemorySize, L_TOTAL);
  k_main<<<256, 256, L_TOTAL, stream>>>(q, k, v, shk, ssk, ssD, c, wmod, w1, b1, w2, b2, ws, (u16*)d_out);
}
```

```cpp
#include <hip/hip_runtime.h>
#include <stdint.h>

typedef unsigned short u16;
typedef unsigned int u32;
typedef short s16x4 __attribute__((ext_vector_type(4)));
typedef short s16x8 __attribute__((ext_vector_type(8)));
typedef __bf16 bf16x8 __attribute__((ext_vector_type(8)));
typedef __bf16 bf16x2 __attribute__((ext_vector_type(2)));
typedef float f32x4 __attribute__((ext_vector_type(4)));
typedef float f32x2 __attribute__((ext_vector_type(2)));
typedef u32 u32x4 __attribute__((ext_vector_type(4)));
typedef u32 u32x2 __attribute__((ext_vector_type(2)));
#define LAS __attribute__((address_space(3)))
#define GAS __attribute__((address_space(1)))
#define DI __device__ __forceinline__

constexpr int S = 2048, D = 640, F = 1728;
constexpr size_t WS_MODP = 0;
constexpr size_t WS_EPOCH = 8192;
constexpr size_t WS_FU = 16384;
constexpr size_t WS_FA = WS_FU + 16384;
constexpr size_t WS_FB = WS_FA + 16384;
constexpr size_t WS_FG = WS_FB + 16384;
constexpr size_t WS_U    = 131072;
constexpr size_t WS_Y2B  = WS_U + 2621440;
constexpr size_t WS_X1   = WS_Y2B + 2621440;
constexpr size_t WS_Y3F  = WS_X1 + 2621440;
constexpr size_t WS_HF   = WS_Y3F + 2621440;
constexpr size_t WS_W2F  = WS_HF + 7077888;

#define STAMP_BEGIN(ws)
#define STAMP_END(ws, kid)
#define MARK_DECL
#define MARK(i)
#define MARK_FLUSH(ws, kid, n)

DI float lo16(u32 x) { return __uint_as_float(x << 16); }
DI float hi16(u32 x) { return __uint_as_float(x & 0xffff0000u); }
DI u32 pk(f32x2 v) { return __builtin_bit_cast(u32, __builtin_convertvector(v, bf16x2)); }
DI u32 pk2(float a, float b) { return pk((f32x2){a, b}); }
DI f32x2 up(u32 x) { return (f32x2){lo16(x), hi16(x)}; }
DI f32x2 rb2(f32x2 v) { return up(pk(v)); }
DI float rbf(float a) { return lo16(pk2(a, a)); }
DI int rfl(int x) { return __builtin_amdgcn_readfirstlane(x); }
DI s16x4 trread(u32 ldsaddr) { return __builtin_amdgcn_ds_read_tr16_b64_v4i16((LAS s16x4*)(size_t)ldsaddr); }
DI bf16x8 cat8(s16x4 a, s16x4 b) { return __builtin_bit_cast(bf16x8, __builtin_shufflevector(a, b, 0, 1, 2, 3, 4, 5, 6, 7)); }
DI f32x4 mfma16(bf16x8 a, bf16x8 b, f32x4 c) { return __builtin_amdgcn_mfma_f32_16x16x32_bf16(a, b, c, 0, 0, 0); }
DI u32 ldsaddr(const void* p) { return (u32)(size_t)(LAS const char*)p; }
DI __amdgpu_buffer_rsrc_t mksrd(const void* p, u32 bytes) { return __builtin_amdgcn_make_buffer_rsrc((void*)p, 0, bytes, 0x00020000); }

[[maybe_unused]] DI float wsum64(float x) {
  x += __int_as_float(__builtin_amdgcn_update_dpp(0, __float_as_int(x), 0xB1, 0xf, 0xf, false));
  x += __int_as_float(__builtin_amdgcn_update_dpp(0, __float_as_int(x), 0x4E, 0xf, 0xf, false));
  x += __int_as_float(__builtin_amdgcn_update_dpp(0, __float_as_int(x), 0x141, 0xf, 0xf, false));
  x += __int_as_float(__builtin_amdgcn_update_dpp(0, __float_as_int(x), 0x128, 0xf, 0xf, false));
  { auto r = __builtin_amdgcn_permlane16_swap(__float_as_uint(x), __float_as_uint(x), false, false); x = __uint_as_float(r[0]) + __uint_as_float(r[1]); }
  { auto r = __builtin_amdgcn_permlane32_swap(__float_as_uint(x), __float_as_uint(x), false, false); x = __uint_as_float(r[0]) + __uint_as_float(r[1]); }
  return x;
}
DI void ln_finish(float sum, float sq, float& mean, float& rinv) {
  const float m = sum * (1.f / D);
  const float var = fmaxf(sq * (1.f / D) - m * m, 0.f);
  mean = rbf(m);
  rinv = __builtin_amdgcn_rcpf(rbf(__builtin_amdgcn_sqrtf(rbf(rbf(var) + 1e-5f))));
}

template <int IMM> DI void bload(u32x4& dst, u32 voff, __amdgpu_buffer_rsrc_t srd, u32 soff) {
  asm volatile("buffer_load_dwordx4 %0, %1, %2, %3 offen offset:%c4" : "=v"(dst) : "v"(voff), "s"(srd), "s"(soff), "i"(IMM));
}
template <int IMM> DI void bload_sc1(u32x4& dst, u32 voff, __amdgpu_buffer_rsrc_t srd, u32 soff) {
  asm volatile("buffer_load_dwordx4 %0, %1, %2, %3 offen offset:%c4 sc1" : "=v"(dst) : "v"(voff), "s"(srd), "s"(soff), "i"(IMM));
}
template <int IMM> DI void bload_a(u32x4& dst, u32 voff, __amdgpu_buffer_rsrc_t srd, u32 soff) {
  asm volatile("buffer_load_dwordx4 %0, %1, %2, %3 offen offset:%c4" : "=a"(dst) : "v"(voff), "s"(srd), "s"(soff), "i"(IMM));
}
template <int IMM> DI void bload_a_sc1(u32x4& dst, u32 voff, __amdgpu_buffer_rsrc_t srd, u32 soff) {
  asm volatile("buffer_load_dwordx4 %0, %1, %2, %3 offen offset:%c4 sc1" : "=a"(dst) : "v"(voff), "s"(srd), "s"(soff), "i"(IMM));
}
DI void touch(u32& dst, u32 voff, __amdgpu_buffer_rsrc_t srd) {
  asm volatile("buffer_load_dword %0, %1, %2, 0 offen sc1" : "=v"(dst) : "v"(voff), "s"(srd));
}
template <int IMM> DI void bload2_sc1(u32x2& dst, u32 voff, __amdgpu_buffer_rsrc_t srd, u32 soff) {
  asm volatile("buffer_load_dwordx2 %0, %1, %2, %3 offen offset:%c4 sc1" : "=v"(dst) : "v"(voff), "s"(srd), "s"(soff), "i"(IMM));
}
template <int IMM> DI void bload2(u32x2& dst, u32 voff, __amdgpu_buffer_rsrc_t srd, u32 soff) {
  asm volatile("buffer_load_dwordx2 %0, %1, %2, %3 offen offset:%c4" : "=v"(dst) : "v"(voff), "s"(srd), "s"(soff), "i"(IMM));
}
DI void dma16(u32 m0v, u32 voff, __amdgpu_buffer_rsrc_t srd, u32 soff) {
  asm volatile("s_mov_b32 m0, %0\n\ts_nop 0\n\tbuffer_load_dwordx4 %1, %2, %3 offen lds" ::"s"(m0v), "v"(voff), "s"(srd), "s"(soff) : "m0");
}
DI void dma4(u32 m0v, u32 voff, __amdgpu_buffer_rsrc_t srd, u32 soff) {
  asm volatile("s_mov_b32 m0, %0\n\ts_nop 0\n\tbuffer_load_dword %1, %2, %3 offen lds" ::"s"(m0v), "v"(voff), "s"(srd), "s"(soff) : "m0");
}
template <int IMM> DI void dsread64(u32x2& dst, u32 addr) { asm volatile("ds_read_b64 %0, %1 offset:%c2" : "=v"(dst) : "v"(addr), "i"(IMM)); }
typedef u32x4 frag;
DI void mfma_vv(f32x4& acc, const frag& a, const frag& b) { asm volatile("v_mfma_f32_16x16x32_bf16 %0, %1, %2, %0" : "+v"(acc) : "v"(a), "v"(b)); }
DI void mfma_vv0(f32x4& acc, const frag& a, const frag& b) { asm volatile("v_mfma_f32_16x16x32_bf16 %0, %1, %2, 0" : "=&v"(acc) : "v"(a), "v"(b)); }
DI void mfma_av(f32x4& acc, const frag& a, const frag& b) { asm volatile("v_mfma_f32_16x16x32_bf16 %0, %1, %2, %0" : "+v"(acc) : "a"(a), "v"(b)); }
DI void mfma_av0(f32x4& acc, const frag& a, const frag& b) { asm volatile("v_mfma_f32_16x16x32_bf16 %0, %1, %2, 0" : "=&v"(acc) : "a"(a), "v"(b)); }
template <int N> DI void waitv() { asm volatile("s_waitcnt vmcnt(%c0)" ::"i"(N) : "memory"); }
template <int N> DI void waitv2(u32x4& a, u32x4& b) { asm volatile("s_waitcnt vmcnt(%c2)" : "+v"(a), "+v"(b) : "i"(N)); }
template <int N> DI void waitv4(u32x4& a, u32x4& b, u32x4& c, u32x4& d) { asm volatile("s_waitcnt vmcnt(%c4)" : "+v"(a), "+v"(b), "+v"(c), "+v"(d) : "i"(N)); }
DI frag catf(s16x4 a, s16x4 b) { return __builtin_bit_cast(frag, __builtin_shufflevector(a, b, 0, 1, 2, 3, 4, 5, 6, 7)); }
#define SB0 __builtin_amdgcn_sched_barrier(0)

constexpr int CV_RS = 1184;
constexpr int CV_KB = 4128;
constexpr int CV_WB = CV_KB + 4 * CV_RS;
template <int OFF> DI frag cv_ldB(u32 vb) { return catf(*(LAS const s16x4*)(size_t)(vb + OFF), *(volatile LAS const s16x4*)(size_t)(vb + OFF + 8)); }
template <int G> constexpr int cv_off0() { return 2 * (G >> 4) * CV_RS + 64 * (G & 15); }
template <int G> constexpr int cv_off1() { return (2 * (G >> 4) + 1) * CV_RS + 64 * (15 - (G & 15)); }
template <int G, int I> DI void cv_one(f32x4 (&acc)[4][8], const frag (&A)[16], const frag& B0, const frag& B1) {
  constexpr int R = 2 * (G >> 4), N = G & 15;
  constexpr int c0 = N <= 1 ? 0 : N / 2;
  constexpr int n0 = 8 - c0;
  if constexpr (I < n0) {
    constexpr int cb = c0 + I, s = 1 - N + 2 * cb;
    if constexpr (N == 0) mfma_vv0(acc[R][cb], A[s], B0); else mfma_vv(acc[R][cb], A[s], B0);
  } else {
    constexpr int c1 = (15 - N) / 2, cb = c1 + (I - n0), s = N - 14 + 2 * cb;
    static_assert(cb <= 7 && s >= 0 && s <= 15, "");
    if constexpr (s == 0) mfma_vv0(acc[R + 1][cb], A[s], B1); else mfma_vv(acc[R + 1][cb], A[s], B1);
  }
}
struct W1C { u32 vo[7]; u32 m0b, sob; __amdgpu_buffer_rsrc_t srd; };
template <int N, int R> DI void w1_m0(const W1C& c) { asm volatile("s_add_u32 m0, %0, %1" ::"s"(c.m0b), "i"(N * 4 * 7168 + R * 1024) : "scc", "m0"); }
template <int N, int R> DI void w1_ld(const W1C& c) {
  asm volatile("buffer_load_dwordx4 %0, %1, %2 offen lds" ::"v"(c.vo[R]), "s"(c.srd), "s"(c.sob + N * 4 * (32 * F * 2)));
}
template <int N, int R> DI void w1_piece(const W1C& c) { w1_m0<N, R>(c); asm volatile("s_nop 0"); w1_ld<N, R>(c); }
template <int N> DI void w1_step(const W1C& c) {
  w1_piece<N, 0>(c); w1_piece<N, 1>(c); w1_piece<N, 2>(c); w1_piece<N, 3>(c); w1_piece<N, 4>(c); w1_piece<N, 5>(c); w1_piece<N, 6>(c);
}
template <int G> DI void cv_steps(f32x4 (&acc)[4][8], const frag (&A)[16], u32 vb, frag P0, frag P1, frag Q0, frag Q1, const W1C& wc) {
  frag N0, N1;
  cv_one<G, 0>(acc, A, P0, P1); SB0;
  if constexpr (G + 2 < 32) N0 = cv_ldB<cv_off0<G + 2>()>(vb);
  SB0; cv_one<G, 1>(acc, A, P0, P1); cv_one<G, 2>(acc, A, P0, P1); SB0;
  if constexpr (G + 2 < 32) N1 = cv_ldB<cv_off1<G + 2>()>(vb);
  SB0; cv_one<G, 3>(acc, A, P0, P1); cv_one<G, 4>(acc, A, P0, P1);
  if constexpr (G < 28) w1_m0<G / 7, G % 7>(wc);
  cv_one<G, 5>(acc, A, P0, P1);
  if constexpr (G < 28) w1_ld<G / 7, G % 7>(wc);
  cv_one<G, 6>(acc, A, P0, P1); cv_one<G, 7>(acc, A, P0, P1); cv_one<G, 8>(acc, A, P0, P1);
  SB0;
  if constexpr (G + 1 < 32) cv_steps<G + 1>(acc, A, vb, Q0, Q1, N0, N1, wc);
}
constexpr int L_W1 = 0;
constexpr int L_Y = 143360;
constexpr int L_MOD = L_Y + 10240;
constexpr int L_RED = L_MOD + 8192;
constexpr int L_TOTAL = 163840;
constexpr int L_CONV = L_TOTAL - 4 * CV_WB;
static_assert(L_CONV >= 16 * 7168 && (L_CONV & 15) == 0 && L_RED + 1024 <= L_TOTAL, "");

typedef unsigned long long u64;
DI u64 call_tag(u32 epoch) { const u32 t = epoch + 1u; return (u64)t | ((u64)(~t) << 32); }
DI void signal(char* ws, size_t off, u64 tag) { __hip_atomic_store((GAS u64*)(ws + off), tag, __ATOMIC_RELAXED, __HIP_MEMORY_SCOPE_AGENT); }
DI void poll_tags(const char* base, int stride, int n, u64 tag, int lane) {
  const GAS u64* p = (const GAS u64*)(base + (lane < n ? lane : n - 1) * stride);
#pragma unroll 1
  for (u32 spins = 0; spins < (1u << 18); spins++) {
    const u64 v = __hip_atomic_load(p, __ATOMIC_RELAXED, __HIP_MEMORY_SCOPE_AGENT);
    if (__all(v == tag)) break;
    __builtin_amdgcn_s_sleep(1);
  }
}
DI void poll_tags128(const char* base, u64 tag, int lane) {
  const __amdgpu_buffer_rsrc_t srd = mksrd(base, 1024);
  const u32 lo = (u32)tag, hi = (u32)(tag >> 32);
#pragma unroll 1
  for (u32 spins = 0; spins < (1u << 18); spins++) {
    asm volatile("" ::: "memory");
    const u32x4 a = __builtin_amdgcn_raw_buffer_load_b128(srd, lane * 16, 0, 16);
    if (__all(a[0] == lo && a[1] == hi && a[2] == lo && a[3] == hi)) break;
    __builtin_amdgcn_s_sleep(1);
  }
}
DI void poll_tags256(const char* base, u64 tag, int lane) {
  const __amdgpu_buffer_rsrc_t srd = mksrd(base, 2048);
  const u32 lo = (u32)tag, hi = (u32)(tag >> 32);
#pragma unroll 1
  for (u32 spins = 0; spins < (1u << 18); spins++) {
    asm volatile("" ::: "memory");
    const u32x4 a = __builtin_amdgcn_raw_buffer_load_b128(srd, lane * 32, 0, 16), b = __builtin_amdgcn_raw_buffer_load_b128(srd, lane * 32 + 16, 0, 16);
    const bool ok = a[0] == lo && a[1] == hi && a[2] == lo && a[3] == hi && b[0] == lo && b[1] == hi && b[2] == lo && b[3] == hi;
    if (__all(ok)) break;
    __builtin_amdgcn_s_sleep(1);
  }
}
DI void lds_barrier() { asm volatile("s_waitcnt lgkmcnt(0)" ::: "memory"); __builtin_amdgcn_s_barrier(); asm volatile("" ::: "memory"); }

DI float xsum8_16_32(float x) {
  x += __int_as_float(__builtin_amdgcn_update_dpp(0, __float_as_int(x), 0x128, 0xf, 0xf, false));
  { auto r = __builtin_amdgcn_permlane16_swap(__float_as_uint(x), __float_as_uint(x), false, false); x = __uint_as_float(r[0]) + __uint_as_float(r[1]); }
  { auto r = __builtin_amdgcn_permlane32_swap(__float_as_uint(x), __float_as_uint(x), false, false); x = __uint_as_float(r[0]) + __uint_as_float(r[1]); }
  return x;
}
DI void ln_stats(const f32x2 (&x)[10], f32x2* red, int w, int lane, float& mean, float& rinv) {
  f32x2 a = ((x[0] + x[1]) + (x[2] + x[3])) + ((x[4] + x[5]) + (x[6] + x[7])) + (x[8] + x[9]);
  f32x2 b0 = x[0] * x[0], b1 = x[1] * x[1];
#pragma unroll
  for (int h = 2; h < 10; h += 2) { b0 += x[h] * x[h]; b1 += x[h + 1] * x[h + 1]; }
  b0 += b1;
  const float s1 = xsum8_16_32(a[0] + a[1]), s2 = xsum8_16_32(b0[0] + b0[1]);
  if (lane < 8) red[w * 8 + lane] = (f32x2){s1, s2};
  lds_barrier();
  const int tk = lane & 7;
  const f32x2 t = (red[tk] + red[8 + tk]) + (red[16 + tk] + red[24 + tk]);
  ln_finish(t[0], t[1], mean, rinv);
}
DI void ln1(const u32x2 (&xr)[5], u32 (&park)[10], f32x2* red, int w, int lane) {
  f32x2 x[10];
#pragma unroll
  for (int i = 0; i < 5; i++) { x[2 * i] = up(xr[i][0]); x[2 * i + 1] = up(xr[i][1]); }
  float mean, rinv;
  ln_stats(x, red, w, lane, mean, rinv);
  const f32x2 mm = {mean, mean}, rr = {rinv, rinv};
#pragma unroll
  for (int h = 0; h < 10; h++) x[h] = rb2(x[h] - mm);
#pragma unroll
  for (int h = 0; h < 10; h++) { const u32 n = pk(x[h] * rr); asm volatile("v_accvgpr_write_b32 %0, %1" : "=a"(park[h]) : "v"(n)); }
}
struct KVI { u32x2 k[7], v[4]; u32x4 s0, s1; };
template <int NT> DI void kv_item(const u32x2* kr, const u32x2* vr, const u32x4& s0, const u32x4& s1, char* dst, int stride) {
  const f32x2 sk[2][4] = {{{lo16(s0[0]), lo16(s0[2])}, {hi16(s0[0]), hi16(s0[2])}, {lo16(s0[1]), lo16(s0[3])}, {hi16(s0[1]), hi16(s0[3])}},
                          {{lo16(s1[0]), lo16(s1[2])}, {hi16(s1[0]), hi16(s1[2])}, {lo16(s1[1]), lo16(s1[3])}, {hi16(s1[1]), hi16(s1[3])}}};
  f32x2 kf[NT + 3][2], a[NT][2];
#pragma unroll
  for (int i = 0; i < NT + 3; i++) { kf[i][0] = up(kr[i][0]); kf[i][1] = up(kr[i][1]); }
#define KVS(EXPR) _Pragma("unroll") for (int r = 0; r < NT; r++) { _Pragma("unroll") for (int h = 0; h < 2; h++) { EXPR; } }
  KVS(a[r][h] = rb2(kf[r][h] * sk[h][3]))
  f32x2 pqt[NT][2];
  KVS(pqt[r][h] = rb2(kf[r + 1][h] * sk[h][2]))
  KVS(a[r][h] = rb2(a[r][h] + pqt[r][h]))
  KVS(pqt[r][h] = rb2(kf[r + 2][h] * sk[h][1]))
  KVS(a[r][h] = rb2(a[r][h] + pqt[r][h]))
  KVS(pqt[r][h] = rb2(kf[r + 3][h] * sk[h][0]))
  KVS(a[r][h] = rb2(a[r][h] + pqt[r][h]))
#undef KVS
#pragma unroll
  for (int r = 0; r < NT; r++) *(u32x2*)(dst + r * stride) = (u32x2){pk(a[r][0] * up(vr[r][0])), pk(a[r][1] * up(vr[r][1]))};
}
struct KVR { u32x2 kA[7], vA[4], kB[4], vB; u32x4 sA0, sA1, sB0, sB1; u32 dA, dB; };
constexpr int P_RED = 53248;
constexpr int P_US = 12288, P_K = 24576, P_V = P_K + 12288, P_S = P_V + 10240;
DI void prep_request(const u16* __restrict__ k, const u16* __restrict__ v, const u16* __restrict__ shk, u32 lds0, int t16, int hf, int w, int lane) {
  const __amdgpu_buffer_rsrc_t sK = mksrd(k, S * D * 2), sV = mksrd(v, S * D * 2), sS = mksrd(shk + 320 * hf * 4, 2560);
  const u32 base = 16 * t16 * (D * 2) + 640 * hf;
#pragma unroll
  for (int n = 0; n < 3; n++) {
    const u32 c = 64 * (w + 4 * n) + lane, row = (c * 1639u) >> 16, o = row * 640 + c * 16 + base;
    dma16(lds0 + P_K + (w + 4 * n) * 1024, c < 760 ? o - 3 * (D * 2) : 0x80000000u, sK, 0);
    if (n < 2 || w < 2) dma16(lds0 + P_V + (w + 4 * n) * 1024, o, sV, 0);
  }
  if (w < 3) dma16(lds0 + P_S + w * 1024, w * 1024 + lane * 16, sS, 0);
  waitv<0>();
}
DI void prep_read(KVR& r, const char* smem, int tid) {
  const u32 mgA = ((u32)tid * 820u) >> 16, cqA = tid - 80 * mgA;
  const u32 cqB = 16 + (tid >> 2), tB = 12 + (tid & 3);
  const char* pA = smem + 4 * mgA * 640 + 8 * cqA;
  const char* pB = smem + tB * 640 + 8 * cqB;
#pragma unroll
  for (int i = 0; i < 7; i++) r.kA[i] = *(const u32x2*)(pA + P_K + i * 640);
#pragma unroll
  for (int i = 0; i < 4; i++) r.vA[i] = *(const u32x2*)(pA + P_V + i * 640);
#pragma unroll
  for (int i = 0; i < 4; i++) r.kB[i] = *(const u32x2*)(pB + P_K + i * 640);
  r.vB = *(const u32x2*)(pB + P_V);
  r.sA0 = *(const u32x4*)(smem + P_S + 32 * cqA); r.sA1 = *(const u32x4*)(smem + P_S + 32 * cqA + 16);
  r.sB0 = *(const u32x4*)(smem + P_S + 32 * cqB); r.sB1 = *(const u32x4*)(smem + P_S + 32 * cqB + 16);
  r.dA = 4 * mgA * 656 + 8 * cqA; r.dB = tB * 656 + 8 * cqB;
}
DI void prep_finish(u32x2 (&xr)[5], char* __restrict__ ws, char* smem, int t16, int hf, int tid, int lane, int w, u64 tag) {
  KVR r;
  prep_read(r, smem, tid);
  kv_item<4>(r.kA, r.vA, r.sA0, r.sA1, smem + r.dA, 656);
  kv_item<1>(r.kB, &r.vB, r.sB0, r.sB1, smem + r.dB, 656);
  lds_barrier();
  char* us = smem + P_US;
  {
    const int rho = lane >> 4, i16 = lane & 15, jj = i16 >> 2, p = i16 & 3;
    const u32 src = ldsaddr(smem) + (4 * jj + rho) * 656 + 8 * p + 32 * w;
    char* dst = us + (4 * w + (i16 >> 2)) * 144 + (i16 & 3) * 32 + rho * 8;
    s16x4 v[5];
#pragma unroll
    for (int n = 0; n < 5; n++) v[n] = trread(src + 128 * n);
#pragma unroll
    for (int n = 0; n < 5; n++) *(s16x4*)(dst + n * 16 * 144) = v[n];
  }
  lds_barrier();
  {
    const __amdgpu_buffer_rsrc_t sU = mksrd(ws + WS_U, 2621440);
    const u32 uo = (t16 * 160 + 80 * hf + (lane >> 3)) * 128 + (lane & 7) * 16;
#pragma unroll
    for (int n = 0; n < 3; n++) {
      const int c8 = w + 4 * n;
      if (n < 2 || w < 2) __builtin_amdgcn_raw_buffer_store_b128(*(const u32x4*)(us + (8 * c8 + (lane >> 3)) * 144 + (lane & 7) * 16), sU, uo + c8 * 1024, 0, 16);
    }
  }
  asm volatile("s_waitcnt vmcnt(0)" : "+v"(xr[0]), "+v"(xr[1]), "+v"(xr[2]), "+v"(xr[3]), "+v"(xr[4]) :: "memory");
  __builtin_amdgcn_s_barrier();
  if (tid < 4) signal(ws, WS_FU + (hf * 4 + tid) * 1024 + t16 * 8, tag);
}
DI void q_request(u32x2 (&xr)[5], const u16* __restrict__ qin, int t0, int w, int lane) {
  const __amdgpu_buffer_rsrc_t srdQ = mksrd(qin + (size_t)t0 * D, 10240);
  const u32 vq = (lane & 7) * 1280 + (40 * w + (lane >> 3)) * 8;
  asm volatile("s_nop 4");
  bload2<0>(xr[0], vq, srdQ, 0); bload2<64>(xr[1], vq, srdQ, 0); bload2<128>(xr[2], vq, srdQ, 0); bload2<192>(xr[3], vq, srdQ, 0); bload2<256>(xr[4], vq, srdQ, 0);
}

DI void phase_conv(const u32x4 (&rk)[4], u32 Draw, const u32x2 (&xr)[5], u32 (&park)[10], char* __restrict__ ws, char* smem, int xcd, int j, int w, int lane, u64 tag, const W1C& wc
) {
  const int ch = 80 * xcd + 4 * j + w;
  char* kim = smem + L_CONV + w * CV_WB;
  char* uim = kim + CV_KB;
  if (lane < 32) *(u32x4*)(uim + (lane >> 3) * CV_RS + 16 * (lane & 7)) = (u32x4){0u, 0u, 0u, 0u};
  if (lane < 2) *(u32x4*)(kim + 16 * lane) = (u32x4){0u, 0u, 0u, 0u};
#pragma unroll
  for (int i = 0; i < 4; i++) *(u32x4*)(kim + 32 + 16 * (64 * i + lane)) = rk[i];
  frag A[16];
  {
    const int q = lane >> 4, cc = lane & 15;
    const u32 va = ldsaddr(kim) + 224 - 64 * q - 8 * (cc >> 2) + 8 * (cc & 3);
#pragma unroll
    for (int s = 0; s < 16; s++) A[s] = catf(trread(va + 256 * s + 32), trread(va + 256 * s));
  }
  if (w == 0) poll_tags128(ws + WS_FU + ((xcd >> 2) * 4 + (xcd & 3)) * 1024, tag, lane);
  __builtin_amdgcn_s_barrier();
  MARK(22);
  {
    const __amdgpu_buffer_rsrc_t sU = mksrd(ws + WS_U, 2621440);
    const u32 uo = ((32 * w + (lane >> 3)) * 160 + 20 * xcd + j) * 128 + (lane & 7) * 16;
    u32x4 ru[4];
#pragma unroll
    for (int i = 0; i < 4; i++) ru[i] = __builtin_amdgcn_raw_buffer_load_b128(sU, uo + i * (8 * 160 * 128), 0, 16);
    SB0;
    ln1(xr, park, (f32x2*)(smem + P_RED), w, lane);
    SB0;
    char* ud = smem + L_CONV + ((lane & 7) >> 1) * CV_WB + CV_KB + 2 * (lane & 1) * CV_RS + 128 + 8 * (32 * w + (lane >> 3));
#pragma unroll
    for (int i = 0; i < 4; i++) { *(u32x2*)(ud + 64 * i) = (u32x2){ru[i][0], ru[i][1]}; *(u32x2*)(ud + 64 * i + CV_RS) = (u32x2){ru[i][2], ru[i][3]}; }
  }
  lds_barrier();
  MARK(0);
  const int q = lane >> 4, cc = lane & 15;
  const u32 vb = ldsaddr(uim) + 8 + 8 * cc + 16 * q;
  f32x4 acc[4][8];
  frag P0, P1, Q0, Q1;
  P0 = cv_ldB<cv_off0<0>()>(vb); P1 = cv_ldB<cv_off1<0>()>(vb); Q0 = cv_ldB<cv_off0<1>()>(vb); Q1 = cv_ldB<cv_off1<1>()>(vb);
  MARK(1);
  cv_steps<0>(acc, A, vb, P0, P1, Q0, Q1, wc);
  asm volatile("s_nop 7\n\ts_nop 1");
#pragma unroll
  for (int r = 0; r < 4; r++)
#pragma unroll
    for (int cb = 0; cb < 8; cb += 4) asm volatile("" : "+v"(acc[r][cb]), "+v"(acc[r][cb + 1]), "+v"(acc[r][cb + 2]), "+v"(acc[r][cb + 3]));
  MARK(2);
  const int idx = 4 * (lane == 0 ? 63 : (lane >= 16 ? lane - 16 : lane + 47));
  const u32 ub = ldsaddr(uim) + 2 * (64 + 4 * cc + q);
  char* yl = kim + 32 * cc + 8 * q;
  const float Dch = (w & 1) ? hi16(Draw) : lo16(Draw);
  const f32x2 DD = {Dch, Dch};
  float o[8][4], cy[8][3];
  u32 u01[8], u23[8];
#pragma unroll
  for (int cb = 0; cb < 8; cb++) {
    const LAS u16* up_ = (const LAS u16*)(size_t)(ub + 128 * cb);
    u01[cb] = (u32)up_[0] | ((u32)up_[CV_RS / 2] << 16);
    u23[cb] = (u32)up_[CV_RS] | ((u32)up_[3 * CV_RS / 2] << 16);
  }
#pragma unroll
  for (int cb = 0; cb < 8; cb++) {
    const f32x4 z0 = acc[0][cb], z1 = acc[1][cb], z2 = acc[2][cb], z3 = acc[3][cb];
    o[cb][0] = z0[0]; o[cb][1] = z0[1] + z1[0]; o[cb][2] = z0[2] + z1[1] + z2[0]; o[cb][3] = (z0[3] + z1[2]) + (z2[1] + z3[0]);
    cy[cb][0] = z1[3] + z2[2] + z3[1]; cy[cb][1] = z2[3] + z3[2]; cy[cb][2] = z3[3];
  }
#pragma unroll
  for (int cb = 0; cb < 8; cb++)
#pragma unroll
    for (int e = 0; e < 3; e++) {
      const float t = lane == 63 ? (cb ? cy[cb - 1][e] : 0.f) : cy[cb][e];
      o[cb][e] += __int_as_float(__builtin_amdgcn_ds_bpermute(idx, __float_as_int(t)));
    }
#pragma unroll
  for (int cb = 0; cb < 8; cb++) {
    const f32x2 y01 = rb2((f32x2){o[cb][0], o[cb][1]}) + rb2(up(u01[cb]) * DD), y23 = rb2((f32x2){o[cb][2], o[cb][3]}) + rb2(up(u23[cb]) * DD);
    *(u32x2*)(yl + 512 * cb) = (u32x2){pk(y01), pk(y23)};
  }
  lds_barrier();
  {
    const __amdgpu_buffer_rsrc_t sY = mksrd(ws + WS_Y2B, 2621440);
    const int c4 = (lane >> 1) & 3, tgl = 32 * w + (lane >> 3), hf = lane & 1;
    const char* src = smem + L_CONV + c4 * CV_WB + tgl * 32 + 16 * hf;
    const u32 yo = (tgl * D + 80 * xcd + 4 * j + c4) * 32 + 16 * hf;
    u32x4 v[4];
#pragma unroll
    for (int i = 0; i < 4; i++) v[i] = *(const u32x4*)(src + 256 * i);
#pragma unroll
    for (int i = 0; i < 4; i++) __builtin_amdgcn_raw_buffer_store_b128(v[i], sY, yo + i * (8 * D * 32), 0, 16);
  }
  MARK(3);
}

#define PRE_G 0
#define PRE_R 0
template <int LO, int HI> DI void gemv_request(u32x4 (&r)[20], const u16* __restrict__ wmod, int r0, int tid) {
  const __amdgpu_buffer_rsrc_t sW = mksrd(wmod, D * 6 * D * 2);
  const u32 o = ((tid >> 3) * (6 * D) + 64 * r0 + 8 * (tid & 7)) * 2;
#pragma unroll
  for (int i = LO; i < HI; i++) r[i] = __builtin_amdgcn_raw_buffer_load_b128(sW, o, i * (32 * 6 * D * 2), 0);
}
DI void gemv_finish(const u32x4 (&r)[20], const u16* __restrict__ c, char* __restrict__ ws, char* smem, int r0, int w, int tid, int lane) {
    const __amdgpu_buffer_rsrc_t sWS = mksrd(ws, 65536);
    float* sc = (float*)(smem + L_Y);
    float* red = sc + 640;
    const int col0 = 64 * r0, rg = tid >> 3;
    for (int i = tid; i < D; i += 256) { float x = lo16(c[i]); sc[i] = rbf(x / (1.f + __expf(-x))); }
    __syncthreads();
    f32x2 a[4] = {{0.f, 0.f}, {0.f, 0.f}, {0.f, 0.f}, {0.f, 0.f}};
#pragma unroll
    for (int i = 0; i < 20; i++) {
      const float s = sc[rg + 32 * i];
#pragma unroll
      for (int e = 0; e < 4; e++) a[e] += up(r[i][e]) * (f32x2){s, s};
    }
    float af[8] = {a[0][0], a[0][1], a[1][0], a[1][1], a[2][0], a[2][1], a[3][0], a[3][1]};
#pragma unroll
    for (int e = 0; e < 8; e++) af[e] += __int_as_float(__builtin_amdgcn_update_dpp(0, __float_as_int(af[e]), 0x128, 0xf, 0xf, false));
#pragma unroll
    for (int e = 0; e < 8; e++) { auto rr = __builtin_amdgcn_permlane16_swap(__float_as_uint(af[e]), __float_as_uint(af[e]), false, false); af[e] = __uint_as_float(rr[0]) + __uint_as_float(rr[1]); }
#pragma unroll
    for (int e = 0; e < 8; e++) { auto rr = __builtin_amdgcn_permlane32_swap(__float_as_uint(af[e]), __float_as_uint(af[e]), false, false); af[e] = __uint_as_float(rr[0]) + __uint_as_float(rr[1]); }
    if (lane < 8) { *(f32x4*)(red + w * 64 + 8 * lane) = (f32x4){af[0], af[1], af[2], af[3]}; *(f32x4*)(red + w * 64 + 8 * lane + 4) = (f32x4){af[4], af[5], af[6], af[7]}; }
    __syncthreads();
    if (tid < 32) {
      const int c0 = 2 * tid;
      float s0 = rbf((red[c0] + red[64 + c0]) + (red[128 + c0] + red[192 + c0]));
      float s1 = rbf((red[c0 + 1] + red[65 + c0]) + (red[129 + c0] + red[193 + c0]));
      const int jc = col0 + c0, vv = jc / D, ch = jc - vv * D;
      if (vv == 1 || vv == 4) { s0 += 1.f; s1 += 1.f; }
      const int slot = vv == 0 ? 1 : vv == 1 ? 0 : vv == 2 ? 2 : vv == 3 ? 4 : vv == 4 ? 3 : 5;
      __builtin_amdgcn_raw_buffer_store_b32(pk2(s0, s1), sWS, WS_MODP + ((ch >> 2) * 24 + slot * 4 + (ch & 3)) * 2, 0, 16);
    }
}
template <int LO, int HI> DI void retile_request(u32x4 (&r)[4][4], const u16* __restrict__ w2, int r0, int w, int lane) {
  const __amdgpu_buffer_rsrc_t sW = mksrd(w2, F * D * 2);
  const int wv = (r0 - 60) * 4 + w;
#pragma unroll
  for (int n = LO; n < HI; n++) {
    const int u = wv + 144 * n, uc = u < 540 ? u : 539, ks = uc / 10, cc = uc - 10 * ks;
#pragma unroll
    for (int i = 0; i < 4; i++) r[n][i] = __builtin_amdgcn_raw_buffer_load_b128(sW, ((8 * i + (lane >> 3)) * D + 8 * (lane & 7)) * 2, (32 * ks * D + 64 * cc) * 2, 0);
  }
}
DI void retile_finish(const u32x4 (&r)[4][4], char* __restrict__ ws, char* smem, int r0, int w, int lane) {
    const int wv = (r0 - 60) * 4 + w;
    char* mb = smem + L_Y + w * (32 * 144);
    const int q = lane >> 4, i16 = lane & 15, jj = i16 >> 2, p = i16 & 3;
    const __amdgpu_buffer_rsrc_t sO = mksrd(ws + WS_W2F, 2211840);
    const u32 a0 = ldsaddr(mb) + (4 * q + jj) * 144 + 8 * p;
#pragma unroll
    for (int n = 0; n < 4; n++) {
      const int u = wv + 144 * n, uc = u < 540 ? u : 539, ks = uc / 10, cc = uc - 10 * ks;
#pragma unroll
      for (int i = 0; i < 4; i++) *(u32x4*)(mb + (8 * i + (lane >> 3)) * 144 + 16 * (lane & 7)) = r[n][i];
      frag v[4];
#pragma unroll
      for (int f = 0; f < 4; f++) v[f] = catf(trread(a0 + 32 * f), trread(a0 + 32 * f + 16 * 144));
#pragma unroll
      for (int f = 0; f < 4; f++) {
        const int ft = 4 * cc + f;
        if (u < 540) __builtin_amdgcn_raw_buffer_store_b128(v[f], sO, (((ft / 5) * 54 + ks) * 5 + (ft % 5)) * 1024 + lane * 16, 0, 16);
      }
    }
}

DI f32x2 gelu2(f32x2 x) {
  const float c0 = -2.f * 0.7978845608028654f * 1.4426950408889634f, c1 = c0 * 0.044715f;
  const f32x2 z = x * (x * x * (f32x2){c1, c1} + (f32x2){c0, c0});
  const f32x2 d = (f32x2){__builtin_amdgcn_exp2f(z[0]), __builtin_amdgcn_exp2f(z[1])} + (f32x2){1.f, 1.f};
  return x * (f32x2){__builtin_amdgcn_rcpf(d[0]), __builtin_amdgcn_rcpf(d[1])};
}
DI int rotk(int n, int rot, int m) { int k = n + rot; return k >= m ? k - m : k; }
struct W2C { u32 l16; int rot2; __amdgpu_buffer_rsrc_t srd; };
template <int I> DI void w2_load1(frag (&W2)[60], const W2C& c) {
  const u32 vo = c.l16 + rotk(I / 5, c.rot2, 14) * 5120;
  if constexpr (I % 5 < 4) bload_a_sc1<(I % 5) * 1024>(W2[I], vo, c.srd, 0); else bload_a_sc1<0>(W2[I], vo + 4096, c.srd, 0);
}
template <int I, int N> DI void w2_loads(frag (&W2)[60], const W2C& c) {
  w2_load1<I>(W2, c);
  if constexpr (N > 1) w2_loads<I + 1, N - 1>(W2, c);
}
#define W2_M0V 8
#define W2_M1V 16
#define W2_G1V 20
#define W2_GEV 6
constexpr int W2_M0 = W2_M0V, W2_M1 = W2_M1V, W2_G1 = W2_G1V, W2_GE = W2_GEV;
template <int LO, int N, int LIM> DI void w2_part(frag (&W2)[60], const W2C& c) {
  if constexpr (LO < LIM) w2_loads<LO, (LO + N <= LIM ? N : LIM - LO)>(W2, c);
}

constexpr int G1R = 6;
struct AF { u32x2 lo, hi; };
struct G1S {
  frag Y[G1R][2];
  AF A0[7], A1[7];
  f32x4 acc[7][2];
};
template <int FI> DI void g1_lda(AF& a, u32 ab) {
  asm volatile("ds_read_b64_tr_b16 %0, %2 offset:%c3\n\tds_read_b64_tr_b16 %1, %2 offset:%c4" : "=&v"(a.lo), "=&v"(a.hi) : "v"(ab), "i"(32 * FI), "i"(32 * FI + 1792));
}
DI frag af(const AF& a) { return (frag){a.lo[0], a.lo[1], a.hi[0], a.hi[1]}; }
template <int N> DI void g1_waitA4(AF& a, AF& b, AF& c, AF& d) {
  asm volatile("s_waitcnt lgkmcnt(%c8)" : "+v"(a.lo), "+v"(a.hi), "+v"(b.lo), "+v"(b.hi), "+v"(c.lo), "+v"(c.hi), "+v"(d.lo), "+v"(d.hi) : "i"(N));
}
template <int N> DI void g1_waitA3(AF& a, AF& b, AF& c) {
  asm volatile("s_waitcnt lgkmcnt(%c6)" : "+v"(a.lo), "+v"(a.hi), "+v"(b.lo), "+v"(b.hi), "+v"(c.lo), "+v"(c.hi) : "i"(N));
}
struct G1C { u32 b0, l16, ybase; int rot1; __amdgpu_buffer_rsrc_t srdY; };
template <int KS, int FI>
DI void g1_f(G1S& s, AF (&Ac)[7], AF (&An)[7], frag (&W2)[60], const G1C& c, const W2C& c2, u32 ab, u32 yo) {
  if constexpr (FI == 0) g1_waitA4<6>(Ac[0], Ac[1], Ac[2], Ac[3]);
  if constexpr (FI == 4) g1_waitA3<(KS < 19) ? 8 : 0>(Ac[4], Ac[5], Ac[6]);
  const frag a = af(Ac[FI]);
  mfma_vv(s.acc[FI][0], a, s.Y[KS % G1R][0]);
  if constexpr (KS < 19) g1_lda<FI>(An[FI], ab);
  mfma_vv(s.acc[FI][1], a, s.Y[KS % G1R][1]);
  if constexpr (KS >= 1 && KS - 1 + G1R < 20 && (FI == 1 || FI == 4)) bload_sc1<(FI == 4) * 1024>(s.Y[(KS - 1) % G1R][FI == 4], c.l16, c.srdY, yo);
  if constexpr (KS < W2_G1 && FI == 6) w2_load1<W2_M0 + W2_M1 + KS>(W2, c2);
  if constexpr (FI < 6) g1_f<KS, FI + 1>(s, Ac, An, W2, c, c2, ab, yo);
}
template <int KS> constexpr int g1_younger() {
  int cnt = 0; bool seen = false;
  for (int k = 0; k < G1R; k++) { if (seen) cnt += 2; if (k == KS) seen = true; }
  for (int n = 0; n < KS; n++) {
    if (n >= 1 && n - 1 + G1R < 20) { if (seen) cnt += 2; if (n - 1 + G1R == KS) seen = true; }
    if (n < W2_G1 && seen) cnt += 1;
  }
  return cnt;
}
template <int KS>
DI void g1_step(G1S& s, frag (&W2)[60], const G1C& c, const W2C& c2) {
  waitv2<g1_younger<KS>()>(s.Y[KS % G1R][0], s.Y[KS % G1R][1]);
  const u32 ab = c.b0 + rotk(KS + 1, c.rot1, 20) * 7168;
  const u32 yo = c.ybase + rotk(KS - 1 + G1R, c.rot1, 20) * 2048;
  if constexpr (KS & 1) g1_f<KS, 0>(s, s.A1, s.A0, W2, c, c2, ab, yo);
  else                  g1_f<KS, 0>(s, s.A0, s.A1, W2, c, c2, ab, yo);
  if constexpr (KS < 19) g1_step<KS + 1>(s, W2, c, c2);
}
struct G2S {
  frag H[4][4];
  frag WX[10];
  f32x4 acc[5][4];
  u32x2 xx[5], gg[5], b2v[5];
};
struct G2C { u32 l16, vx, vb2, vg; int rot2; __amdgpu_buffer_rsrc_t srdH, srdX, srdB2, srdMD; };
template <int N, int T> DI void g2_hload1(G2S& s, const G2C& c, u32 vo) { bload_sc1<T * 1024>(s.H[N & 3][T], vo, c.srdH, 0); }
template <int N> DI void g2_hload(G2S& s, const G2C& c) {
  const u32 vo = c.l16 + rotk(N, c.rot2, 14) * 4096;
  g2_hload1<N, 0>(s, c, vo); g2_hload1<N, 1>(s, c, vo); g2_hload1<N, 2>(s, c, vo); g2_hload1<N, 3>(s, c, vo);
}
template <int I> DI void g2_eload(G2S& s, const G2C& c) {
  if constexpr (I < 5) bload2_sc1<32 * I>(s.xx[I], c.vx, c.srdX, 0);
  else if constexpr (I < 10) bload2<32 * (I - 5)>(s.b2v[I - 5], c.vb2, c.srdB2, 0);
  else bload2_sc1<192 * (I - 10)>(s.gg[I - 10], c.vg, c.srdMD, 0);
}
template <int N, int M>
DI void g2_m(G2S& s, frag (&W2)[60], const G2C& c, u32 vo) {
  constexpr int d = M >> 2, t = M & 3;
  if constexpr (N == 0) mfma_av0(s.acc[d][t], W2[d], s.H[0][t]);
  else if constexpr (N < 12) mfma_av(s.acc[d][t], W2[N * 5 + d], s.H[N & 3][t]);
  else mfma_vv(s.acc[d][t], s.WX[(N - 12) * 5 + d], s.H[N & 3][t]);
  if constexpr (N + 3 < 14 && (M == 1 || M == 6 || M == 11 || M == 16)) g2_hload1<N + 3, (M - 1) / 5>(s, c, vo);
  if constexpr (N >= 11 && (M == 1 || M == 5 || M == 9 || M == 13 || M == 17)) g2_eload<5 * (N - 11) + (M - 1) / 4>(s, c);
  if constexpr (M < 19) g2_m<N, M + 1>(s, W2, c, vo);
}
template <int N> constexpr int g2_younger() {
  int cnt = 0; bool seen = false;
  for (int h = 0; h < 3; h++) { if (seen) cnt += 4; if (h == N) seen = true; }
  for (int n = 0; n < N; n++) {
    if (n + 3 < 14) { if (seen) cnt += 4; if (n + 3 == N) seen = true; }
    if (n >= 11 && seen) cnt += 5;
  }
  return cnt;
}
static_assert(g2_younger<0>() == 8 && g2_younger<3>() == 8 && g2_younger<11>() == 8 && g2_younger<12>() == 9 && g2_younger<13>() == 10, "");
template <int N>
DI void g2_step(G2S& s, frag (&W2)[60], const G2C& c) {
  waitv4<g2_younger<N>()>(s.H[N & 3][0], s.H[N & 3][1], s.H[N & 3][2], s.H[N & 3][3]);
  const u32 vo = c.l16 + rotk(N + 3, c.rot2, 14) * 4096;
  g2_m<N, 0>(s, W2, c, vo);
  if constexpr (N < 13) g2_step<N + 1>(s, W2, c);
}

__global__ __launch_bounds__(256, 1) void k_main(const u16* __restrict__ qin, const u16* __restrict__ kin, const u16* __restrict__ vin,
                                                 const u16* __restrict__ shk, const u16* __restrict__ ssk, const u16* __restrict__ ssD,
                                                 const u16* __restrict__ cin, const u16* __restrict__ wmod, const u16* __restrict__ w1,
                                                 const u16* __restrict__ b1, const u16* __restrict__ w2, const u16* __restrict__ b2,
                                                 char* __restrict__ ws, u16* __restrict__ out) {
  STAMP_BEGIN(ws)
  MARK_DECL
  extern __shared__ __attribute__((aligned(16))) char smem[];
  const int tid = threadIdx.x, lane = tid & 63, w = rfl(tid >> 6);
  const int b = blockIdx.x, xcd = b & 7, j = b >> 3;
  const int tile = 4 * (xcd >> 1) + (j >> 3), g = 8 * (xcd & 1) + (j & 7);
  const int TT = 4 * xcd + (j >> 3), cg = j & 7;
  const u32 l16 = lane * 16, lds0 = ldsaddr(smem);
  W1C wc;
#pragma unroll
  for (int r = 0; r < 7; r++) { const u32 idx = 64 * r + lane, row = (idx * 4682u) >> 16; wc.vo[r] = row * (F * 2 - 224) + idx * 16; }
  wc.m0b = lds0 + L_W1 + w * 7168; wc.sob = w * (32 * F * 2) + 224 * g; wc.srd = mksrd(w1, D * F * 2);
  const int tg = 16 * tile + g, t0 = 8 * tg;
  const int tk = lane & 7, slot = lane >> 3, q = lane >> 4;
  const u64 tag = call_tag(__hip_atomic_load((const GAS u32*)(ws + WS_EPOCH), __ATOMIC_RELAXED, __HIP_MEMORY_SCOPE_AGENT));
  if (w == 3) {
    unsigned long long pc; u32 len;
    asm volatile("s_getpc_b64 %0\n.Lkmain_here:\n\ts_mov_b32 %1, .Lkmain_end-.Lkmain_here" : "=s"(pc), "=s"(len));
    asm volatile("s_nop 4");
    dma4(lds0 + L_RED + 768, (j + 32 * lane) * 128, mksrd((const void*)pc, len), 0);
  }
  asm volatile("s_nop 4");
  prep_request(kin, vin, shk, lds0, b >> 1, b & 1, w, lane);
  __builtin_amdgcn_s_barrier();
  if (w == 2 && j < 2) {
    const char* p = ws + (size_t)(lane < 13 ? lane : 0) * 2097152;
    if (lane == 13 || lane == 14) p = (const char*)out + (lane - 13) * 2097152;
    if (lane == 15 || lane == 16) p = (const char*)w1 + (lane - 15) * 2097152;
    if (lane == 17 || lane == 18) p = (const char*)w2 + (lane - 17) * 2097152;
    if (lane >= 19 && lane <= 21) p = (const char*)wmod + (lane - 19) * 2097152;
    if (lane == 22) p = (const char*)b1;
    if (lane == 23) p = (const char*)b2;
    if (lane >= 24) p = (const char*)cin;
    asm volatile("s_mov_b32 m0, %0\n\ts_nop 0\n\tglobal_load_lds_dword %1, off sc1" ::"s"(lds0 + L_RED + 768), "v"(p) : "m0");
  }
  MARK(21);
  u32x2 xr[5];
  u32 park[10];
  const int r0 = (j - 20) * 8 + xcd;
  const int ch = 80 * xcd + 4 * j + w;
  u32x4 rk[4];
  u32 Draw;
  {
    const __amdgpu_buffer_rsrc_t sF = mksrd(ssk + (size_t)(j < 20 ? ch : 0) * S, j < 20 ? S * 2 : 0);
#pragma unroll
    for (int i = 0; i < 4; i++) rk[i] = __builtin_amdgcn_raw_buffer_load_b128(sF, l16 + 1024 * i, 0, 0);
  }
  Draw = __builtin_amdgcn_raw_buffer_load_b32(mksrd(ssD, D * 2), (ch >> 1) * 4, 0, 0);
  q_request(xr, qin, t0, w, lane);
  prep_finish(xr, ws, smem, b >> 1, b & 1, tid, lane, w, tag);
  asm volatile("" : "+v"(rk[0]), "+v"(rk[1]), "+v"(rk[2]), "+v"(rk[3]), "+v"(Draw));
  MARK(23);
  if (j < 20) {
    phase_conv(rk, Draw, xr, park, ws, smem, xcd, j, w, lane, tag, wc
    );
  } else {
    if (r0 < 60) {
      u32x4 r[20];
      gemv_request<0, 20>(r, wmod, r0, tid);
      SB0;
      gemv_finish(r, cin, ws, smem, r0, w, tid, lane);
    } else {
      u32x4 r[4][4];
      retile_request<0, 4>(r, w2, r0, w, lane);
      SB0;
      retile_finish(r, ws, smem, r0, w, lane);
    }
    ln1(xr, park, (f32x2*)(smem + P_RED), w, lane);
  }
  waitv<0>();
  __builtin_amdgcn_s_barrier();
  MARK(4);
  if (tid < 8) signal(ws, WS_FG + tid * 2048 + b * 8, tag);
  const int ks0 = w < 2 ? 14 * w : 28 + 13 * (w - 2), nks = w < 2 ? 14 : 13;
  W2C c2;
  c2.l16 = l16; c2.srd = mksrd(ws + WS_W2F + (size_t)cg * 276480 + ks0 * 5120, nks * 5120);
  { int r2 = 2 * cg + 4 * (j >> 3); r2 = r2 >= 14 ? r2 - 14 : r2; c2.rot2 = r2 >= 14 ? r2 - 14 : r2; }
  frag W2[60];
  f32x2 x[10], t[10];
  f32x2* red = (f32x2*)(smem + L_RED);
  if (j >= 20) { w1_step<0>(wc); w1_step<1>(wc); w1_step<2>(wc); w1_step<3>(wc); }
  if (w == 0) poll_tags256(ws + WS_FG + xcd * 2048, tag, lane);
  __builtin_amdgcn_s_barrier();
  if (b == 0 && tid == 0) __hip_atomic_store((GAS u32*)(ws + WS_EPOCH), (u32)tag, __ATOMIC_RELAXED, __HIP_MEMORY_SCOPE_AGENT);
  MARK(5);
  const __amdgpu_buffer_rsrc_t srdMD = mksrd(ws + WS_MODP, 7680);
  u32x2 bb[7];
  {
    const __amdgpu_buffer_rsrc_t srdYB = mksrd(ws + WS_Y2B + (size_t)(tg >> 1) * 20480, 20480);
    const __amdgpu_buffer_rsrc_t srdB1 = mksrd(b1, F * 2);
    u32x4 yr[3], mr[2];
    const u32 v16 = tid * 16, vb = (16 * 7 * g + 4 * q) * 2;
    asm volatile("s_nop 4");
    MARK(20);
    const u32 vy = tid * 32 + 16 * (tg & 1);
    bload_sc1<0>(yr[0], vy, srdYB, 0); bload_sc1<0>(yr[1], vy + 8192, srdYB, 0); bload_sc1<0>(yr[2], vy + 16384, srdYB, 0);
    bload_sc1<0>(mr[0], v16, srdMD, 0); bload_sc1<0>(mr[1], v16 + 4096, srdMD, 0);
    w1_step<4>(wc);
    SB0;
#pragma unroll
      for (int i = 0; i < 5; i++) { x[2 * i] = up(xr[i][0]); x[2 * i + 1] = up(xr[i][1]); }
#pragma unroll
      for (int h = 0; h < 10; h++) { u32 n; asm volatile("v_accvgpr_read_b32 %0, %1" : "=v"(n) : "a"(park[h])); t[h] = up(n); asm volatile("" : "+v"(x[h]), "+v"(t[h])); }
    SB0;
    asm volatile("s_waitcnt vmcnt(7)" : "+v"(yr[0]), "+v"(yr[1]), "+v"(yr[2]), "+v"(mr[0]), "+v"(mr[1]));
    MARK(19);
    *(u32x4*)(smem + L_Y + v16) = yr[0]; *(u32x4*)(smem + L_Y + 4096 + v16) = yr[1];
    if (tid < 128) *(u32x4*)(smem + L_Y + 8192 + v16) = yr[2];
    *(u32x4*)(smem + L_MOD + v16) = mr[0]; *(u32x4*)(smem + L_MOD + 4096 + v16) = mr[1];
    lds_barrier();
    MARK(6);
    const int i16 = lane & 15, jj = i16 >> 2, p = i16 & 3;
    const u32 ya = lds0 + L_Y + (4 * (40 * w + 2 * q + (p >> 1)) + jj) * 16 + 8 * (p & 1);
    const char* mdp = smem + L_MOD + (40 * w + slot) * 48;
    u32 yy[10], w1v[10], sav[10], gav[10];
#pragma unroll
    for (int i = 0; i < 5; i++) {
      const u32x2 yu = __builtin_bit_cast(u32x2, trread(ya + i * 8 * 64));
      const u32x4 m0 = *(const u32x4*)(mdp + i * 384);
      const u32x2 m1 = *(const u32x2*)(mdp + i * 384 + 16);
      yy[2 * i] = yu[0]; yy[2 * i + 1] = yu[1]; w1v[2 * i] = m0[0]; w1v[2 * i + 1] = m0[1]; sav[2 * i] = m0[2]; sav[2 * i + 1] = m0[3]; gav[2 * i] = m1[0]; gav[2 * i + 1] = m1[1];
    }
    u32 o[10];
#define STAGE(EXPR) _Pragma("unroll") for (int h = 0; h < 10; h++) { EXPR; }
    STAGE(t[h] = rb2(t[h] * up(w1v[h])))
    SB0; w2_part<0, 2, W2_M0>(W2, c2); SB0;
    STAGE(t[h] = rb2(t[h] + up(sav[h])))
    SB0; w2_part<2, 2, W2_M0>(W2, c2); SB0;
    STAGE(t[h] = rb2(t[h] * up(yy[h])))
    SB0; w2_part<4, 2, W2_M0>(W2, c2); SB0;
    STAGE(t[h] = rb2(up(gav[h]) * t[h]))
    SB0; w2_part<6, 2, W2_M0>(W2, c2); SB0;
    STAGE(o[h] = pk(x[h] + t[h]))
    const __amdgpu_buffer_rsrc_t srdX1 = mksrd(ws + WS_X1, 2621440);
    const u32 xo = (t0 + tk) * 1280 + (40 * w + slot) * 8;
#pragma unroll
    for (int i = 0; i < 5; i++) __builtin_amdgcn_raw_buffer_store_b64((u32x2){o[2 * i], o[2 * i + 1]}, srdX1, xo + i * 64, 0, 16);
    STAGE(x[h] = up(o[h]))
    MARK(7);
    float mean, rinv;
    SB0; w2_part<8, 3, W2_M0>(W2, c2); SB0;
    ln_stats(x, red, w, lane, mean, rinv);
    SB0; w2_part<11, 3, W2_M0>(W2, c2); SB0;
    {
      const f32x2 m2 = {mean, mean}, r2 = {rinv, rinv};
#pragma unroll
      for (int i = 0; i < 5; i++) {
        const u32x4 m0 = *(const u32x4*)(mdp + i * 384 + 16);
        const u32x2 m1 = *(const u32x2*)(mdp + i * 384 + 32);
        x[2 * i] = (x[2 * i] - m2) * r2 * up(m0[2]) + up(m1[0]);
        x[2 * i + 1] = (x[2 * i + 1] - m2) * r2 * up(m0[3]) + up(m1[1]);
      }
    }
    MARK(8);
    SB0;
    {
      bload2<0>(bb[0], vb, srdB1, 0); bload2<32>(bb[1], vb, srdB1, 0); bload2<64>(bb[2], vb, srdB1, 0); bload2<96>(bb[3], vb, srdB1, 0);
      bload2<128>(bb[4], vb, srdB1, 0); bload2<160>(bb[5], vb, srdB1, 0); bload2<192>(bb[6], vb, srdB1, 0);
    }
    SB0;
    ln_stats(x, red + 32, w, lane, mean, rinv);
    {
      const f32x2 m3 = {mean, mean}, r3 = {rinv, rinv};
      STAGE(o[h] = pk((x[h] - m3) * r3))
      const int rt = tg >> 1, r = 8 * (tg & 1) + tk;
      const __amdgpu_buffer_rsrc_t srdY3 = mksrd(ws + WS_Y3F, 2621440);
      const u32 yo = (rt >> 1) * 40960 + (rt & 1) * 1024 + ((2 * (slot >> 2) + (slot & 1)) * 16 + r) * 16 + 8 * ((slot >> 1) & 1) + 5 * w * 2048;
#pragma unroll
      for (int i = 0; i < 5; i++) __builtin_amdgcn_raw_buffer_store_b64((u32x2){o[2 * i], o[2 * i + 1]}, srdY3, yo + i * 2048, 0, 16);
    }
#undef STAGE
  }
  MARK(9);
  asm volatile("s_waitcnt vmcnt(0)" : "+v"(bb[0]), "+v"(bb[1]), "+v"(bb[2]), "+v"(bb[3]), "+v"(bb[4]), "+v"(bb[5]), "+v"(bb[6]) :: "memory");
  __builtin_amdgcn_s_barrier();
  if (tid == 0) signal(ws, WS_FA + (tile * 16 + g) * 64, tag);
  MARK(10);
  w2_loads<W2_M0, W2_M1>(W2, c2);
  if (w == 0) poll_tags(ws + WS_FA + tile * 1024, 64, 16, tag, lane);
  __builtin_amdgcn_s_barrier();
  MARK(11);
  f32x4 hacc[7][2];
  {
    G1C c1;
    const int i16 = lane & 15, jj = i16 >> 2, p = i16 & 3;
    c1.srdY = mksrd(ws + WS_Y3F, 2621440); c1.l16 = l16;
    c1.rot1 = (5 * (j & 7)) >> 1;
    c1.ybase = (tile * 4 + w) * 40960;
    c1.b0 = lds0 + L_W1 + (16 * (q >> 1) + 4 * (q & 1) + jj) * 224 + 8 * p;
    G1S s;
    asm volatile("s_nop 4");
#pragma unroll
    for (int k = 0; k < G1R; k++) { const u32 yo = c1.ybase + rotk(k, c1.rot1, 20) * 2048; bload_sc1<0>(s.Y[k][0], l16, c1.srdY, yo); bload_sc1<1024>(s.Y[k][1], l16, c1.srdY, yo); }
#pragma unroll
    for (int f = 0; f < 7; f++) {
      const f32x4 bv = {lo16(bb[f][0]), hi16(bb[f][0]), lo16(bb[f][1]), hi16(bb[f][1])};
      s.acc[f][0] = bv; s.acc[f][1] = bv;
      asm volatile("" : "+v"(s.acc[f][0]), "+v"(s.acc[f][1]));
    }
    const u32 ab0 = c1.b0 + c1.rot1 * 7168;
    asm volatile("s_waitcnt lgkmcnt(0)" ::: "memory");
    g1_lda<0>(s.A0[0], ab0); g1_lda<1>(s.A0[1], ab0); g1_lda<2>(s.A0[2], ab0); g1_lda<3>(s.A0[3], ab0);
    g1_lda<4>(s.A0[4], ab0); g1_lda<5>(s.A0[5], ab0); g1_lda<6>(s.A0[6], ab0);
    g1_step<0>(s, W2, c1, c2);
    asm volatile("s_nop 7\n\ts_nop 1");
#pragma unroll
    for (int f = 0; f < 7; f++) { asm volatile("" : "+v"(s.acc[f][0]), "+v"(s.acc[f][1])); hacc[f][0] = s.acc[f][0]; hacc[f][1] = s.acc[f][1]; }
  }
  MARK(12);
  G2S s2_;
  {
    const __amdgpu_buffer_rsrc_t srdH = mksrd(ws + WS_HF, 7077888);
    const int rt0 = 8 * tile + 2 * w;
    const u32 ho = ((rt0 >> 2) * 54 * 4 + (rt0 & 3)) * 1024 + l16 + ((7 * g) >> 1) * 4096;
    auto gel = [&](int f, int tt) -> u32x2 {
      const f32x4 a = hacc[f][tt];
      return (u32x2){pk(gelu2((f32x2){a[0], a[1]})), pk(gelu2((f32x2){a[2], a[3]}))};
    };
    SB0; w2_part<W2_M0 + W2_M1 + W2_G1, W2_GE, 60>(W2, c2); SB0;
    if (g & 1) {
      { const u32x2 a = gel(0, 0), c = gel(0, 1);
        __builtin_amdgcn_raw_buffer_store_b64(a, srdH, ho + 8, 0, 16); __builtin_amdgcn_raw_buffer_store_b64(c, srdH, ho + 1024 + 8, 0, 16); }
#pragma unroll
      for (int i = 0; i < 3; i++) {
        const u32x2 a0 = gel(2 * i + 1, 0), a1 = gel(2 * i + 2, 0), c0 = gel(2 * i + 1, 1), c1 = gel(2 * i + 2, 1);
        if (7 * g + 2 * i + 2 < 108) {
          __builtin_amdgcn_raw_buffer_store_b128((u32x4){a0[0], a0[1], a1[0], a1[1]}, srdH, ho + (1 + i) * 4096, 0, 16);
          __builtin_amdgcn_raw_buffer_store_b128((u32x4){c0[0], c0[1], c1[0], c1[1]}, srdH, ho + (1 + i) * 4096 + 1024, 0, 16);
        }
      }
    } else {
#pragma unroll
      for (int i = 0; i < 3; i++) {
        const u32x2 a0 = gel(2 * i, 0), a1 = gel(2 * i + 1, 0), c0 = gel(2 * i, 1), c1 = gel(2 * i + 1, 1);
        __builtin_amdgcn_raw_buffer_store_b128((u32x4){a0[0], a0[1], a1[0], a1[1]}, srdH, ho + i * 4096, 0, 16);
        __builtin_amdgcn_raw_buffer_store_b128((u32x4){c0[0], c0[1], c1[0], c1[1]}, srdH, ho + i * 4096 + 1024, 0, 16);
      }
      { const u32x2 a = gel(6, 0), c = gel(6, 1);
        __builtin_amdgcn_raw_buffer_store_b64(a, srdH, ho + 3 * 4096, 0, 16); __builtin_amdgcn_raw_buffer_store_b64(c, srdH, ho + 3 * 4096 + 1024, 0, 16); }
    }
  }
  MARK(13);
  waitv<0>();
  __builtin_amdgcn_s_barrier();
  if (tid == 0) signal(ws, WS_FB + (tile * 16 + g) * 64, tag);
  {
    const u32 vo = l16 + rotk(12, c2.rot2, 14) * 5120, v1 = l16 + rotk(13, c2.rot2, 14) * 5120;
    w2_part<W2_M0 + W2_M1 + W2_G1 + W2_GE, 60, 60>(W2, c2);
    bload_sc1<0>(s2_.WX[0], vo, c2.srd, 0); bload_sc1<1024>(s2_.WX[1], vo, c2.srd, 0); bload_sc1<2048>(s2_.WX[2], vo, c2.srd, 0); bload_sc1<3072>(s2_.WX[3], vo, c2.srd, 0);
    bload_sc1<0>(s2_.WX[4], vo + 4096, c2.srd, 0);
    bload_sc1<0>(s2_.WX[5], v1, c2.srd, 0); bload_sc1<1024>(s2_.WX[6], v1, c2.srd, 0); bload_sc1<2048>(s2_.WX[7], v1, c2.srd, 0); bload_sc1<3072>(s2_.WX[8], v1, c2.srd, 0);
    bload_sc1<0>(s2_.WX[9], v1 + 4096, c2.srd, 0);
  }
  MARK(14);
  const int cc = lane & 15, tok = 64 * TT + 16 * w + cc;
  G2C cg2;
  cg2.l16 = l16; cg2.rot2 = c2.rot2;
  cg2.srdH = mksrd(ws + WS_HF + (size_t)TT * 221184 + ks0 * 4096, nks * 4096);
  cg2.srdX = mksrd(ws + WS_X1, 2621440); cg2.srdB2 = mksrd(b2, D * 2); cg2.srdMD = srdMD;
  cg2.vx = tok * 1280 + (80 * cg + 4 * q) * 2; cg2.vb2 = (80 * cg + 4 * q) * 2; cg2.vg = (20 * cg + q) * 48 + 40;
  if (w == 0) poll_tags(ws + WS_FB + (TT >> 1) * 1024, 64, 16, tag, lane);
  __builtin_amdgcn_s_barrier();
  MARK(15);
  asm volatile("s_nop 4");
  g2_hload<0>(s2_, cg2); g2_hload<1>(s2_, cg2); g2_hload<2>(s2_, cg2);
  asm volatile("s_waitcnt vmcnt(12)" : "+v"(s2_.WX[0]), "+v"(s2_.WX[1]), "+v"(s2_.WX[2]), "+v"(s2_.WX[3]), "+v"(s2_.WX[4]), "+v"(s2_.WX[5]), "+v"(s2_.WX[6]), "+v"(s2_.WX[7]), "+v"(s2_.WX[8]), "+v"(s2_.WX[9]));
  g2_step<0>(s2_, W2, cg2);
  asm volatile("s_nop 7\n\ts_nop 1");
#pragma unroll
  for (int d = 0; d < 5; d++) asm volatile("" : "+v"(s2_.acc[d][0]), "+v"(s2_.acc[d][1]), "+v"(s2_.acc[d][2]), "+v"(s2_.acc[d][3]));
  MARK(16);
  f32x4* redq = (f32x4*)smem;
  f32x4 own[5];
#define G2_OWN(T) { _Pragma("unroll") for (int d = 0; d < 5; d++) { own[d] = s2_.acc[d][T]; \
    _Pragma("unroll") for (int t = 0; t < 4; t++) if (t != T) redq[((T * 4 + t) * 5 + d) * 64 + lane] = s2_.acc[d][t]; } }
  if (w == 0) G2_OWN(0) else if (w == 1) G2_OWN(1) else if (w == 2) G2_OWN(2) else G2_OWN(3)
#undef G2_OWN
  lds_barrier();
  asm volatile("s_waitcnt vmcnt(0)" : "+v"(s2_.xx[0]), "+v"(s2_.xx[1]), "+v"(s2_.xx[2]), "+v"(s2_.xx[3]), "+v"(s2_.xx[4]), "+v"(s2_.b2v[0]), "+v"(s2_.b2v[1]), "+v"(s2_.b2v[2]), "+v"(s2_.b2v[3]), "+v"(s2_.b2v[4]),
               "+v"(s2_.gg[0]), "+v"(s2_.gg[1]), "+v"(s2_.gg[2]), "+v"(s2_.gg[3]), "+v"(s2_.gg[4]));
  MARK(17);
  {
    const f32x4* r1 = redq + ((((w + 1) & 3) * 4 + w) * 5) * 64 + lane;
    const f32x4* r2 = redq + ((((w + 2) & 3) * 4 + w) * 5) * 64 + lane;
    const f32x4* r3 = redq + ((((w + 3) & 3) * 4 + w) * 5) * 64 + lane;
#pragma unroll
    for (int d = 0; d < 5; d++) {
      const f32x4 sacc = (own[d] + r1[d * 64]) + (r2[d * 64] + r3[d * 64]);
      u32x2 o;
#pragma unroll
      for (int h = 0; h < 2; h++) {
        o[h] = pk(up(s2_.xx[d][h]) + up(s2_.gg[d][h]) * ((f32x2){sacc[2 * h], sacc[2 * h + 1]} + up(s2_.b2v[d][h])));
      }
      *(u32x2*)(out + (size_t)tok * D + 80 * cg + 16 * d + 4 * q) = o;
    }
  }
  asm volatile(".Lkmain_end:");
  MARK(18);
  MARK_FLUSH(ws, 0, 24);
  STAMP_END(ws, 0);
}

extern "C" void kernel_launch(void* const* d_in, const int* in_sizes, int n_in, void* d_out, int out_size, void* d_ws, size_t ws_size,
                              hipStream_t stream) {
  const u16* q = (const u16*)d_in[0]; const u16* k = (const u16*)d_in[1]; const u16* v = (const u16*)d_in[2];
  const u16* shk = (const u16*)d_in[3]; const u16* ssk = (const u16*)d_in[4]; const u16* ssD = (const u16*)d_in[5];
  const u16* w1 = (const u16*)d_in[7]; const u16* b1 = (const u16*)d_in[8]; const u16* w2 = (const u16*)d_in[9];
  const u16* b2 = (const u16*)d_in[10]; const u16* c = (const u16*)d_in[11]; const u16* wmod = (const u16*)d_in[12];
  char* ws = (char*)d_ws;
  (void)hipFuncSetAttribute((const void*)k_main, hipFuncAttributeMaxDynamicSharedMemorySize, L_TOTAL);
  k_main<<<256, 256, L_TOTAL, stream>>>(q, k, v, shk, ssk, ssD, c, wmod, w1, b1, w2, b2, ws, (u16*)d_out);
}
```

```cpp
#include <hip/hip_runtime.h>
#include <stdint.h>

typedef unsigned short u16;
typedef unsigned int u32;
typedef short s16x4 __attribute__((ext_vector_type(4)));
typedef short s16x8 __attribute__((ext_vector_type(8)));
typedef __bf16 bf16x8 __attribute__((ext_vector_type(8)));
typedef __bf16 bf16x2 __attribute__((ext_vector_type(2)));
typedef float f32x4 __attribute__((ext_vector_type(4)));
typedef float f32x2 __attribute__((ext_vector_type(2)));
typedef u32 u32x4 __attribute__((ext_vector_type(4)));
typedef u32 u32x2 __attribute__((ext_vector_type(2)));
#define LAS __attribute__((address_space(3)))
#define GAS __attribute__((address_space(1)))
#define DI __device__ __forceinline__

constexpr int S = 2048, D = 640, F = 1728;
constexpr size_t WS_MODP = 0;
constexpr size_t WS_EPOCH = 8192;
constexpr size_t WS_FU = 16384;
constexpr size_t WS_FA = WS_FU + 16384;
constexpr size_t WS_FB = WS_FA + 16384;
constexpr size_t WS_FG = WS_FB + 16384;
constexpr size_t WS_U    = 131072;
constexpr size_t WS_Y2B  = WS_U + 2621440;
constexpr size_t WS_X1   = WS_Y2B + 2621440;
constexpr size_t WS_Y3F  = WS_X1 + 2621440;
constexpr size_t WS_HF   = WS_Y3F + 2621440;
constexpr size_t WS_W2F  = WS_HF + 7077888;

#define STAMP_BEGIN(ws)
#define STAMP_END(ws, kid)
#define MARK_DECL
#define MARK(i)
#define MARK_FLUSH(ws, kid, n)

DI float lo16(u32 x) { return __uint_as_float(x << 16); }
DI float hi16(u32 x) { return __uint_as_float(x & 0xffff0000u); }
DI u32 pk(f32x2 v) { return __builtin_bit_cast(u32, __builtin_convertvector(v, bf16x2)); }
DI u32 pk2(float a, float b) { return pk((f32x2){a, b}); }
DI f32x2 up(u32 x) { return (f32x2){lo16(x), hi16(x)}; }
DI f32x2 rb2(f32x2 v) { return up(pk(v)); }
DI float rbf(float a) { return lo16(pk2(a, a)); }
DI int rfl(int x) { return __builtin_amdgcn_readfirstlane(x); }
DI s16x4 trread(u32 ldsaddr) { return __builtin_amdgcn_ds_read_tr16_b64_v4i16((LAS s16x4*)(size_t)ldsaddr); }
DI bf16x8 cat8(s16x4 a, s16x4 b) { return __builtin_bit_cast(bf16x8, __builtin_shufflevector(a, b, 0, 1, 2, 3, 4, 5, 6, 7)); }
DI f32x4 mfma16(bf16x8 a, bf16x8 b, f32x4 c) { return __builtin_amdgcn_mfma_f32_16x16x32_bf16(a, b, c, 0, 0, 0); }
DI u32 ldsaddr(const void* p) { return (u32)(size_t)(LAS const char*)p; }
DI __amdgpu_buffer_rsrc_t mksrd(const void* p, u32 bytes) { return __builtin_amdgcn_make_buffer_rsrc((void*)p, 0, bytes, 0x00020000); }

[[maybe_unused]] DI float wsum64(float x) {
  x += __int_as_float(__builtin_amdgcn_update_dpp(0, __float_as_int(x), 0xB1, 0xf, 0xf, false));
  x += __int_as_float(__builtin_amdgcn_update_dpp(0, __float_as_int(x), 0x4E, 0xf, 0xf, false));
  x += __int_as_float(__builtin_amdgcn_update_dpp(0, __float_as_int(x), 0x141, 0xf, 0xf, false));
  x += __int_as_float(__builtin_amdgcn_update_dpp(0, __float_as_int(x), 0x128, 0xf, 0xf, false));
  { auto r = __builtin_amdgcn_permlane16_swap(__float_as_uint(x), __float_as_uint(x), false, false); x = __uint_as_float(r[0]) + __uint_as_float(r[1]); }
  { auto r = __builtin_amdgcn_permlane32_swap(__float_as_uint(x), __float_as_uint(x), false, false); x = __uint_as_float(r[0]) + __uint_as_float(r[1]); }
  return x;
}
DI void ln_finish(float sum, float sq, float& mean, float& rinv) {
  const float m = sum * (1.f / D);
  const float var = fmaxf(sq * (1.f / D) - m * m, 0.f);
  mean = rbf(m);
  rinv = __builtin_amdgcn_rcpf(rbf(__builtin_amdgcn_sqrtf(rbf(rbf(var) + 1e-5f))));
}

template <int IMM> DI void bload(u32x4& dst, u32 voff, __amdgpu_buffer_rsrc_t srd, u32 soff) {
  asm volatile("buffer_load_dwordx4 %0, %1, %2, %3 offen offset:%c4" : "=v"(dst) : "v"(voff), "s"(srd), "s"(soff), "i"(IMM));
}
template <int IMM> DI void bload_sc1(u32x4& dst, u32 voff, __amdgpu_buffer_rsrc_t srd, u32 soff) {
  asm volatile("buffer_load_dwordx4 %0, %1, %2, %3 offen offset:%c4 sc1" : "=v"(dst) : "v"(voff), "s"(srd), "s"(soff), "i"(IMM));
}
template <int IMM> DI void bload_a(u32x4& dst, u32 voff, __amdgpu_buffer_rsrc_t srd, u32 soff) {
  asm volatile("buffer_load_dwordx4 %0, %1, %2, %3 offen offset:%c4" : "=a"(dst) : "v"(voff), "s"(srd), "s"(soff), "i"(IMM));
}
template <int IMM> DI void bload_a_sc1(u32x4& dst, u32 voff, __amdgpu_buffer_rsrc_t srd, u32 soff) {
  asm volatile("buffer_load_dwordx4 %0, %1, %2, %3 offen offset:%c4 sc1" : "=a"(dst) : "v"(voff), "s"(srd), "s"(soff), "i"(IMM));
}
DI void touch(u32& dst, u32 voff, __amdgpu_buffer_rsrc_t srd) {
  asm volatile("buffer_load_dword %0, %1, %2, 0 offen sc1" : "=v"(dst) : "v"(voff), "s"(srd));
}
template <int IMM> DI void bload2_sc1(u32x2& dst, u32 voff, __amdgpu_buffer_rsrc_t srd, u32 soff) {
  asm volatile("buffer_load_dwordx2 %0, %1, %2, %3 offen offset:%c4 sc1" : "=v"(dst) : "v"(voff), "s"(srd), "s"(soff), "i"(IMM));
}
template <int IMM> DI void bload2(u32x2& dst, u32 voff, __amdgpu_buffer_rsrc_t srd, u32 soff) {
  asm volatile("buffer_load_dwordx2 %0, %1, %2, %3 offen offset:%c4" : "=v"(dst) : "v"(voff), "s"(srd), "s"(soff), "i"(IMM));
}
DI void dma16(u32 m0v, u32 voff, __amdgpu_buffer_rsrc_t srd, u32 soff) {
  asm volatile("s_mov_b32 m0, %0\n\ts_nop 0\n\tbuffer_load_dwordx4 %1, %2, %3 offen lds" ::"s"(m0v), "v"(voff), "s"(srd), "s"(soff) : "m0");
}
DI void dma4(u32 m0v, u32 voff, __amdgpu_buffer_rsrc_t srd, u32 soff) {
  asm volatile("s_mov_b32 m0, %0\n\ts_nop 0\n\tbuffer_load_dword %1, %2, %3 offen lds" ::"s"(m0v), "v"(voff), "s"(srd), "s"(soff) : "m0");
}
template <int IMM> DI void dsread64(u32x2& dst, u32 addr) { asm volatile("ds_read_b64 %0, %1 offset:%c2" : "=v"(dst) : "v"(addr), "i"(IMM)); }
typedef u32x4 frag;
DI void mfma_vv(f32x4& acc, const frag& a, const frag& b) { asm volatile("v_mfma_f32_16x16x32_bf16 %0, %1, %2, %0" : "+v"(acc) : "v"(a), "v"(b)); }
DI void mfma_vv0(f32x4& acc, const frag& a, const frag& b) { asm volatile("v_mfma_f32_16x16x32_bf16 %0, %1, %2, 0" : "=&v"(acc) : "v"(a), "v"(b)); }
DI void mfma_av(f32x4& acc, const frag& a, const frag& b) { asm volatile("v_mfma_f32_16x16x32_bf16 %0, %1, %2, %0" : "+v"(acc) : "a"(a), "v"(b)); }
DI void mfma_av0(f32x4& acc, const frag& a, const frag& b) { asm volatile("v_mfma_f32_16x16x32_bf16 %0, %1, %2, 0" : "=&v"(acc) : "a"(a), "v"(b)); }
template <int N> DI void waitv() { asm volatile("s_waitcnt vmcnt(%c0)" ::"i"(N) : "memory"); }
template <int N> DI void waitv2(u32x4& a, u32x4& b) { asm volatile("s_waitcnt vmcnt(%c2)" : "+v"(a), "+v"(b) : "i"(N)); }
template <int N> DI void waitv4(u32x4& a, u32x4& b, u32x4& c, u32x4& d) { asm volatile("s_waitcnt vmcnt(%c4)" : "+v"(a), "+v"(b), "+v"(c), "+v"(d) : "i"(N)); }
DI frag catf(s16x4 a, s16x4 b) { return __builtin_bit_cast(frag, __builtin_shufflevector(a, b, 0, 1, 2, 3, 4, 5, 6, 7)); }
#define SB0 __builtin_amdgcn_sched_barrier(0)

constexpr int CV_RS = 1184;
constexpr int CV_KB = 4128;
constexpr int CV_WB = CV_KB + 4 * CV_RS;
template <int OFF> DI frag cv_ldB(u32 vb) { return catf(*(LAS const s16x4*)(size_t)(vb + OFF), *(volatile LAS const s16x4*)(size_t)(vb + OFF + 8)); }
template <int G> constexpr int cv_off0() { return 2 * (G >> 4) * CV_RS + 64 * (G & 15); }
template <int G> constexpr int cv_off1() { return (2 * (G >> 4) + 1) * CV_RS + 64 * (15 - (G & 15)); }
template <int G, int I> DI void cv_one(f32x4 (&acc)[4][8], const frag (&A)[16], const frag& B0, const frag& B1) {
  constexpr int R = 2 * (G >> 4), N = G & 15;
  constexpr int c0 = N <= 1 ? 0 : N / 2;
  constexpr int n0 = 8 - c0;
  if constexpr (I < n0) {
    constexpr int cb = c0 + I, s = 1 - N + 2 * cb;
    if constexpr (N == 0) mfma_vv0(acc[R][cb], A[s], B0); else mfma_vv(acc[R][cb], A[s], B0);
  } else {
    constexpr int c1 = (15 - N) / 2, cb = c1 + (I - n0), s = N - 14 + 2 * cb;
    static_assert(cb <= 7 && s >= 0 && s <= 15, "");
    if constexpr (s == 0) mfma_vv0(acc[R + 1][cb], A[s], B1); else mfma_vv(acc[R + 1][cb], A[s], B1);
  }
}
struct W1C { u32 vo[7]; u32 m0b, sob; __amdgpu_buffer_rsrc_t srd; };
template <int N, int R> DI void w1_m0(const W1C& c) { asm volatile("s_add_u32 m0, %0, %1" ::"s"(c.m0b), "i"(N * 4 * 7168 + R * 1024) : "scc", "m0"); }
template <int N, int R> DI void w1_ld(const W1C& c) {
  asm volatile("buffer_load_dwordx4 %0, %1, %2 offen lds" ::"v"(c.vo[R]), "s"(c.srd), "s"(c.sob + N * 4 * (32 * F * 2)));
}
template <int N, int R> DI void w1_piece(const W1C& c) { w1_m0<N, R>(c); asm volatile("s_nop 0"); w1_ld<N, R>(c); }
template <int N> DI void w1_step(const W1C& c) {
  w1_piece<N, 0>(c); w1_piece<N, 1>(c); w1_piece<N, 2>(c); w1_piece<N, 3>(c); w1_piece<N, 4>(c); w1_piece<N, 5>(c); w1_piece<N, 6>(c);
}
template <int G> DI void cv_steps(f32x4 (&acc)[4][8], const frag (&A)[16], u32 vb, frag P0, frag P1, frag Q0, frag Q1, const W1C& wc) {
  frag N0, N1;
  cv_one<G, 0>(acc, A, P0, P1); SB0;
  if constexpr (G + 2 < 32) N0 = cv_ldB<cv_off0<G + 2>()>(vb);
  SB0; cv_one<G, 1>(acc, A, P0, P1); cv_one<G, 2>(acc, A, P0, P1); SB0;
  if constexpr (G + 2 < 32) N1 = cv_ldB<cv_off1<G + 2>()>(vb);
  SB0; cv_one<G, 3>(acc, A, P0, P1); cv_one<G, 4>(acc, A, P0, P1);
  if constexpr (G < 28) w1_m0<G / 7, G % 7>(wc);
  cv_one<G, 5>(acc, A, P0, P1);
  if constexpr (G < 28) w1_ld<G / 7, G % 7>(wc);
  cv_one<G, 6>(acc, A, P0, P1); cv_one<G, 7>(acc, A, P0, P1); cv_one<G, 8>(acc, A, P0, P1);
  SB0;
  if constexpr (G + 1 < 32) cv_steps<G + 1>(acc, A, vb, Q0, Q1, N0, N1, wc);
}
constexpr int L_W1 = 0;
constexpr int L_Y = 143360;
constexpr int L_MOD = L_Y + 10240;
constexpr int L_RED = L_MOD + 8192;
constexpr int L_TOTAL = 163840;
constexpr int L_CONV = L_TOTAL - 4 * CV_WB;
static_assert(L_CONV >= 16 * 7168 && (L_CONV & 15) == 0 && L_RED + 1024 <= L_TOTAL, "");

typedef unsigned long long u64;
DI u64 call_tag(u32 epoch) { const u32 t = epoch + 1u; return (u64)t | ((u64)(~t) << 32); }
DI void signal(char* ws, size_t off, u64 tag) { __hip_atomic_store((GAS u64*)(ws + off), tag, __ATOMIC_RELAXED, __HIP_MEMORY_SCOPE_AGENT); }
DI void poll_tags(const char* base, int stride, int n, u64 tag, int lane) {
  const GAS u64* p = (const GAS u64*)(base + (lane < n ? lane : n - 1) * stride);
  for (u32 spins = 0; spins < (1u << 18); spins++) {
    const u64 v = __hip_atomic_load(p, __ATOMIC_RELAXED, __HIP_MEMORY_SCOPE_AGENT);
    if (__all(v == tag)) break;
    __builtin_amdgcn_s_sleep(1);
  }
}
DI void poll_tags128(const char* base, u64 tag, int lane) {
  const __amdgpu_buffer_rsrc_t srd = mksrd(base, 1024);
  const u32 lo = (u32)tag, hi = (u32)(tag >> 32);
  for (u32 spins = 0; spins < (1u << 18); spins++) {
    asm volatile("" ::: "memory");
    const u32x4 a = __builtin_amdgcn_raw_buffer_load_b128(srd, lane * 16, 0, 16);
    if (__all(a[0] == lo && a[1] == hi && a[2] == lo && a[3] == hi)) break;
    __builtin_amdgcn_s_sleep(1);
  }
}
DI void poll_tags256(const char* base, u64 tag, int lane) {
  const __amdgpu_buffer_rsrc_t srd = mksrd(base, 2048);
  const u32 lo = (u32)tag, hi = (u32)(tag >> 32);
  for (u32 spins = 0; spins < (1u << 18); spins++) {
    asm volatile("" ::: "memory");
    const u32x4 a = __builtin_amdgcn_raw_buffer_load_b128(srd, lane * 32, 0, 16), b = __builtin_amdgcn_raw_buffer_load_b128(srd, lane * 32 + 16, 0, 16);
    const bool ok = a[0] == lo && a[1] == hi && a[2] == lo && a[3] == hi && b[0] == lo && b[1] == hi && b[2] == lo && b[3] == hi;
    if (__all(ok)) break;
    __builtin_amdgcn_s_sleep(1);
  }
}
DI void lds_barrier() { asm volatile("s_waitcnt lgkmcnt(0)" ::: "memory"); __builtin_amdgcn_s_barrier(); asm volatile("" ::: "memory"); }

DI float xsum8_16_32(float x) {
  x += __int_as_float(__builtin_amdgcn_update_dpp(0, __float_as_int(x), 0x128, 0xf, 0xf, false));
  { auto r = __builtin_amdgcn_permlane16_swap(__float_as_uint(x), __float_as_uint(x), false, false); x = __uint_as_float(r[0]) + __uint_as_float(r[1]); }
  { auto r = __builtin_amdgcn_permlane32_swap(__float_as_uint(x), __float_as_uint(x), false, false); x = __uint_as_float(r[0]) + __uint_as_float(r[1]); }
  return x;
}
DI void ln_stats(const f32x2 (&x)[10], f32x2* red, int w, int lane, float& mean, float& rinv) {
  f32x2 a = ((x[0] + x[1]) + (x[2] + x[3])) + ((x[4] + x[5]) + (x[6] + x[7])) + (x[8] + x[9]);
  f32x2 b0 = x[0] * x[0], b1 = x[1] * x[1];
#pragma unroll
  for (int h = 2; h < 10; h += 2) { b0 += x[h] * x[h]; b1 += x[h + 1] * x[h + 1]; }
  b0 += b1;
  const float s1 = xsum8_16_32(a[0] + a[1]), s2 = xsum8_16_32(b0[0] + b0[1]);
  if (lane < 8) red[w * 8 + lane] = (f32x2){s1, s2};
  lds_barrier();
  const int tk = lane & 7;
  const f32x2 t = (red[tk] + red[8 + tk]) + (red[16 + tk] + red[24 + tk]);
  ln_finish(t[0], t[1], mean, rinv);
}
DI void ln1(const u32x2 (&xr)[5], u32 (&park)[10], f32x2* red, int w, int lane) {
  f32x2 x[10];
#pragma unroll
  for (int i = 0; i < 5; i++) { x[2 * i] = up(xr[i][0]); x[2 * i + 1] = up(xr[i][1]); }
  float mean, rinv;
  ln_stats(x, red, w, lane, mean, rinv);
  const f32x2 mm = {mean, mean}, rr = {rinv, rinv};
#pragma unroll
  for (int h = 0; h < 10; h++) x[h] = rb2(x[h] - mm);
#pragma unroll
  for (int h = 0; h < 10; h++) { const u32 n = pk(x[h] * rr); asm volatile("v_accvgpr_write_b32 %0, %1" : "=a"(park[h]) : "v"(n)); }
}
struct KVI { u32x2 k[7], v[4]; u32x4 s0, s1; };
template <int NT> DI void kv_item(const u32x2* kr, const u32x2* vr, const u32x4& s0, const u32x4& s1, char* dst, int stride) {
  const f32x2 sk[2][4] = {{{lo16(s0[0]), lo16(s0[2])}, {hi16(s0[0]), hi16(s0[2])}, {lo16(s0[1]), lo16(s0[3])}, {hi16(s0[1]), hi16(s0[3])}},
                          {{lo16(s1[0]), lo16(s1[2])}, {hi16(s1[0]), hi16(s1[2])}, {lo16(s1[1]), lo16(s1[3])}, {hi16(s1[1]), hi16(s1[3])}}};
  f32x2 kf[NT + 3][2], a[NT][2];
#pragma unroll
  for (int i = 0; i < NT + 3; i++) { kf[i][0] = up(kr[i][0]); kf[i][1] = up(kr[i][1]); }
#define KVS(EXPR) _Pragma("unroll") for (int r = 0; r < NT; r++) { _Pragma("unroll") for (int h = 0; h < 2; h++) { EXPR; } }
  KVS(a[r][h] = rb2(kf[r][h] * sk[h][3]))
  f32x2 pqt[NT][2];
  KVS(pqt[r][h] = rb2(kf[r + 1][h] * sk[h][2]))
  KVS(a[r][h] = rb2(a[r][h] + pqt[r][h]))
  KVS(pqt[r][h] = rb2(kf[r + 2][h] * sk[h][1]))
  KVS(a[r][h] = rb2(a[r][h] + pqt[r][h]))
  KVS(pqt[r][h] = rb2(kf[r + 3][h] * sk[h][0]))
  KVS(a[r][h] = rb2(a[r][h] + pqt[r][h]))
#undef KVS
#pragma unroll
  for (int r = 0; r < NT; r++) *(u32x2*)(dst + r * stride) = (u32x2){pk(a[r][0] * up(vr[r][0])), pk(a[r][1] * up(vr[r][1]))};
}
struct KVR { u32x2 kA[7], vA[4], kB[4], vB; u32x4 sA0, sA1, sB0, sB1; u32 dA, dB; };
constexpr int P_RED = 53248;
constexpr int P_US = 12288, P_K = 24576, P_V = P_K + 12288, P_S = P_V + 10240;
DI void prep_request(const u16* __restrict__ k, const u16* __restrict__ v, const u16* __restrict__ shk, u32 lds0, int t16, int hf, int w, int lane) {
  const __amdgpu_buffer_rsrc_t sK = mksrd(k, S * D * 2), sV = mksrd(v, S * D * 2), sS = mksrd(shk + 320 * hf * 4, 2560);
  const u32 base = 16 * t16 * (D * 2) + 640 * hf;
#pragma unroll
  for (int n = 0; n < 3; n++) {
    const u32 c = 64 * (w + 4 * n) + lane, row = (c * 1639u) >> 16, o = row * 640 + c * 16 + base;
    dma16(lds0 + P_K + (w + 4 * n) * 1024, c < 760 ? o - 3 * (D * 2) : 0x80000000u, sK, 0);
    if (n < 2 || w < 2) dma16(lds0 + P_V + (w + 4 * n) * 1024, o, sV, 0);
  }
  if (w < 3) dma16(lds0 + P_S + w * 1024, w * 1024 + lane * 16, sS, 0);
  waitv<0>();
}
DI void prep_read(KVR& r, const char* smem, int tid) {
  const u32 mgA = ((u32)tid * 820u) >> 16, cqA = tid - 80 * mgA;
  const u32 cqB = 16 + (tid >> 2), tB = 12 + (tid & 3);
  const char* pA = smem + 4 * mgA * 640 + 8 * cqA;
  const char* pB = smem + tB * 640 + 8 * cqB;
#pragma unroll
  for (int i = 0; i < 7; i++) r.kA[i] = *(const u32x2*)(pA + P_K + i * 640);
#pragma unroll
  for (int i = 0; i < 4; i++) r.vA[i] = *(const u32x2*)(pA + P_V + i * 640);
#pragma unroll
  for (int i = 0; i < 4; i++) r.kB[i] = *(const u32x2*)(pB + P_K + i * 640);
  r.vB = *(const u32x2*)(pB + P_V);
  r.sA0 = *(const u32x4*)(smem + P_S + 32 * cqA); r.sA1 = *(const u32x4*)(smem + P_S + 32 * cqA + 16);
  r.sB0 = *(const u32x4*)(smem + P_S + 32 * cqB); r.sB1 = *(const u32x4*)(smem + P_S + 32 * cqB + 16);
  r.dA = 4 * mgA * 656 + 8 * cqA; r.dB = tB * 656 + 8 * cqB;
}
DI void prep_finish(u32x2 (&xr)[5], char* __restrict__ ws, char* smem, int t16, int hf, int tid, int lane, int w, u64 tag) {
  KVR r;
  prep_read(r, smem, tid);
  kv_item<4>(r.kA, r.vA, r.sA0, r.sA1, smem + r.dA, 656);
  kv_item<1>(r.kB, &r.vB, r.sB0, r.sB1, smem + r.dB, 656);
  lds_barrier();
  char* us = smem + P_US;
  {
    const int rho = lane >> 4, i16 = lane & 15, jj = i16 >> 2, p = i16 & 3;
    const u32 src = ldsaddr(smem) + (4 * jj + rho) * 656 + 8 * p + 32 * w;
    char* dst = us + (4 * w + (i16 >> 2)) * 144 + (i16 & 3) * 32 + rho * 8;
    s16x4 v[5];
#pragma unroll
    for (int n = 0; n < 5; n++) v[n] = trread(src + 128 * n);
#pragma unroll
    for (int n = 0; n < 5; n++) *(s16x4*)(dst + n * 16 * 144) = v[n];
  }
  lds_barrier();
  {
    const __amdgpu_buffer_rsrc_t sU = mksrd(ws + WS_U, 2621440);
    const u32 uo = (t16 * 160 + 80 * hf + (lane >> 3)) * 128 + (lane & 7) * 16;
#pragma unroll
    for (int n = 0; n < 3; n++) {
      const int c8 = w + 4 * n;
      if (n < 2 || w < 2) __builtin_amdgcn_raw_buffer_store_b128(*(const u32x4*)(us + (8 * c8 + (lane >> 3)) * 144 + (lane & 7) * 16), sU, uo + c8 * 1024, 0, 16);
    }
  }
  asm volatile("s_waitcnt vmcnt(0)" : "+v"(xr[0]), "+v"(xr[1]), "+v"(xr[2]), "+v"(xr[3]), "+v"(xr[4]) :: "memory");
  __builtin_amdgcn_s_barrier();
  if (tid < 4) signal(ws, WS_FU + (hf * 4 + tid) * 1024 + t16 * 8, tag);
}
DI void q_request(u32x2 (&xr)[5], const u16* __restrict__ qin, int t0, int w, int lane) {
  const __amdgpu_buffer_rsrc_t srdQ = mksrd(qin + (size_t)t0 * D, 10240);
  const u32 vq = (lane & 7) * 1280 + (40 * w + (lane >> 3)) * 8;
  asm volatile("s_nop 4");
  bload2<0>(xr[0], vq, srdQ, 0); bload2<64>(xr[1], vq, srdQ, 0); bload2<128>(xr[2], vq, srdQ, 0); bload2<192>(xr[3], vq, srdQ, 0); bload2<256>(xr[4], vq, srdQ, 0);
}

DI void phase_conv(const u32x4 (&rk)[4], u32 Draw, const u32x2 (&xr)[5], u32 (&park)[10], char* __restrict__ ws, char* smem, int xcd, int j, int w, int lane, u64 tag, const W1C& wc
) {
  const int ch = 80 * xcd + 4 * j + w;
  char* kim = smem + L_CONV + w * CV_WB;
  char* uim = kim + CV_KB;
  if (lane < 32) *(u32x4*)(uim + (lane >> 3) * CV_RS + 16 * (lane & 7)) = (u32x4){0u, 0u, 0u, 0u};
  if (lane < 2) *(u32x4*)(kim + 16 * lane) = (u32x4){0u, 0u, 0u, 0u};
#pragma unroll
  for (int i = 0; i < 4; i++) *(u32x4*)(kim + 32 + 16 * (64 * i + lane)) = rk[i];
  frag A[16];
  {
    const int q = lane >> 4, cc = lane & 15;
    const u32 va = ldsaddr(kim) + 224 - 64 * q - 8 * (cc >> 2) + 8 * (cc & 3);
#pragma unroll
    for (int s = 0; s < 16; s++) A[s] = catf(trread(va + 256 * s + 32), trread(va + 256 * s));
  }
  if (w == 0) poll_tags128(ws + WS_FU + ((xcd >> 2) * 4 + (xcd & 3)) * 1024, tag, lane);
  __builtin_amdgcn_s_barrier();
  MARK(22);
  {
    const __amdgpu_buffer_rsrc_t sU = mksrd(ws + WS_U, 2621440);
    const u32 uo = ((32 * w + (lane >> 3)) * 160 + 20 * xcd + j) * 128 + (lane & 7) * 16;
    u32x4 ru[4];
#pragma unroll
    for (int i = 0; i < 4; i++) ru[i] = __builtin_amdgcn_raw_buffer_load_b128(sU, uo + i * (8 * 160 * 128), 0, 16);
    SB0;
    ln1(xr, park, (f32x2*)(smem + P_RED), w, lane);
    SB0;
    char* ud = smem + L_CONV + ((lane & 7) >> 1) * CV_WB + CV_KB + 2 * (lane & 1) * CV_RS + 128 + 8 * (32 * w + (lane >> 3));
#pragma unroll
    for (int i = 0; i < 4; i++) { *(u32x2*)(ud + 64 * i) = (u32x2){ru[i][0], ru[i][1]}; *(u32x2*)(ud + 64 * i + CV_RS) = (u32x2){ru[i][2], ru[i][3]}; }
  }
  lds_barrier();
  MARK(0);
  const int q = lane >> 4, cc = lane & 15;
  const u32 vb = ldsaddr(uim) + 8 + 8 * cc + 16 * q;
  f32x4 acc[4][8];
  frag P0, P1, Q0, Q1;
  P0 = cv_ldB<cv_off0<0>()>(vb); P1 = cv_ldB<cv_off1<0>()>(vb); Q0 = cv_ldB<cv_off0<1>()>(vb); Q1 = cv_ldB<cv_off1<1>()>(vb);
  MARK(1);
  cv_steps<0>(acc, A, vb, P0, P1, Q0, Q1, wc);
  asm volatile("s_nop 7\n\ts_nop 1");
#pragma unroll
  for (int r = 0; r < 4; r++)
#pragma unroll
    for (int cb = 0; cb < 8; cb += 4) asm volatile("" : "+v"(acc[r][cb]), "+v"(acc[r][cb + 1]), "+v"(acc[r][cb + 2]), "+v"(acc[r][cb + 3]));
  MARK(2);
  const int idx = 4 * (lane == 0 ? 63 : (lane >= 16 ? lane - 16 : lane + 47));
  const u32 ub = ldsaddr(uim) + 2 * (64 + 4 * cc + q);
  char* yl = kim + 32 * cc + 8 * q;
  const float Dch = (w & 1) ? hi16(Draw) : lo16(Draw);
  const f32x2 DD = {Dch, Dch};
  float o[8][4], cy[8][3];
  u32 u01[8], u23[8];
#pragma unroll
  for (int cb = 0; cb < 8; cb++) {
    const LAS u16* up_ = (const LAS u16*)(size_t)(ub + 128 * cb);
    u01[cb] = (u32)up_[0] | ((u32)up_[CV_RS / 2] << 16);
    u23[cb] = (u32)up_[CV_RS] | ((u32)up_[3 * CV_RS / 2] << 16);
  }
#pragma unroll
  for (int cb = 0; cb < 8; cb++) {
    const f32x4 z0 = acc[0][cb], z1 = acc[1][cb], z2 = acc[2][cb], z3 = acc[3][cb];
    o[cb][0] = z0[0]; o[cb][1] = z0[1] + z1[0]; o[cb][2] = z0[2] + z1[1] + z2[0]; o[cb][3] = (z0[3] + z1[2]) + (z2[1] + z3[0]);
    cy[cb][0] = z1[3] + z2[2] + z3[1]; cy[cb][1] = z2[3] + z3[2]; cy[cb][2] = z3[3];
  }
#pragma unroll
  for (int cb = 0; cb < 8; cb++)
#pragma unroll
    for (int e = 0; e < 3; e++) {
      const float t = lane == 63 ? (cb ? cy[cb - 1][e] : 0.f) : cy[cb][e];
      o[cb][e] += __int_as_float(__builtin_amdgcn_ds_bpermute(idx, __float_as_int(t)));
    }
#pragma unroll
  for (int cb = 0; cb < 8; cb++) {
    const f32x2 y01 = rb2((f32x2){o[cb][0], o[cb][1]}) + rb2(up(u01[cb]) * DD), y23 = rb2((f32x2){o[cb][2], o[cb][3]}) + rb2(up(u23[cb]) * DD);
    *(u32x2*)(yl + 512 * cb) = (u32x2){pk(y01), pk(y23)};
  }
  lds_barrier();
  {
    const __amdgpu_buffer_rsrc_t sY = mksrd(ws + WS_Y2B, 2621440);
    const int c4 = (lane >> 1) & 3, tgl = 32 * w + (lane >> 3), hf = lane & 1;
    const char* src = smem + L_CONV + c4 * CV_WB + tgl * 32 + 16 * hf;
    const u32 yo = (tgl * D + 80 * xcd + 4 * j + c4) * 32 + 16 * hf;
    u32x4 v[4];
#pragma unroll
    for (int i = 0; i < 4; i++) v[i] = *(const u32x4*)(src + 256 * i);
#pragma unroll
    for (int i = 0; i < 4; i++) __builtin_amdgcn_raw_buffer_store_b128(v[i], sY, yo + i * (8 * D * 32), 0, 16);
  }
  MARK(3);
}

#define PRE_G 0
#define PRE_R 0
template <int LO, int HI> DI void gemv_request(u32x4 (&r)[20], const u16* __restrict__ wmod, int r0, int tid) {
  const __amdgpu_buffer_rsrc_t sW = mksrd(wmod, D * 6 * D * 2);
  const u32 o = ((tid >> 3) * (6 * D) + 64 * r0 + 8 * (tid & 7)) * 2;
#pragma unroll
  for (int i = LO; i < HI; i++) r[i] = __builtin_amdgcn_raw_buffer_load_b128(sW, o, i * (32 * 6 * D * 2), 0);
}
DI void gemv_finish(const u32x4 (&r)[20], const u16* __restrict__ c, char* __restrict__ ws, char* smem, int r0, int w, int tid, int lane) {
    const __amdgpu_buffer_rsrc_t sWS = mksrd(ws, 65536);
    float* sc = (float*)(smem + L_Y);
    float* red = sc + 640;
    const int col0 = 64 * r0, rg = tid >> 3;
    for (int i = tid; i < D; i += 256) { float x = lo16(c[i]); sc[i] = rbf(x / (1.f + __expf(-x))); }
    __syncthreads();
    f32x2 a[4] = {{0.f, 0.f}, {0.f, 0.f}, {0.f, 0.f}, {0.f, 0.f}};
#pragma unroll
    for (int i = 0; i < 20; i++) {
      const float s = sc[rg + 32 * i];
#pragma unroll
      for (int e = 0; e < 4; e++) a[e] += up(r[i][e]) * (f32x2){s, s};
    }
    float af[8] = {a[0][0], a[0][1], a[1][0], a[1][1], a[2][0], a[2][1], a[3][0], a[3][1]};
#pragma unroll
    for (int e = 0; e < 8; e++) af[e] += __int_as_float(__builtin_amdgcn_update_dpp(0, __float_as_int(af[e]), 0x128, 0xf, 0xf, false));
#pragma unroll
    for (int e = 0; e < 8; e++) { auto rr = __builtin_amdgcn_permlane16_swap(__float_as_uint(af[e]), __float_as_uint(af[e]), false, false); af[e] = __uint_as_float(rr[0]) + __uint_as_float(rr[1]); }
#pragma unroll
    for (int e = 0; e < 8; e++) { auto rr = __builtin_amdgcn_permlane32_swap(__float_as_uint(af[e]), __float_as_uint(af[e]), false, false); af[e] = __uint_as_float(rr[0]) + __uint_as_float(rr[1]); }
    if (lane < 8) { *(f32x4*)(red + w * 64 + 8 * lane) = (f32x4){af[0], af[1], af[2], af[3]}; *(f32x4*)(red + w * 64 + 8 * lane + 4) = (f32x4){af[4], af[5], af[6], af[7]}; }
    __syncthreads();
    if (tid < 32) {
      const int c0 = 2 * tid;
      float s0 = rbf((red[c0] + red[64 + c0]) + (red[128 + c0] + red[192 + c0]));
      float s1 = rbf((red[c0 + 1] + red[65 + c0]) + (red[129 + c0] + red[193 + c0]));
      const int jc = col0 + c0, vv = jc / D, ch = jc - vv * D;
      if (vv == 1 || vv == 4) { s0 += 1.f; s1 += 1.f; }
      const int slot = vv == 0 ? 1 : vv == 1 ? 0 : vv == 2 ? 2 : vv == 3 ? 4 : vv == 4 ? 3 : 5;
      __builtin_amdgcn_raw_buffer_store_b32(pk2(s0, s1), sWS, WS_MODP + ((ch >> 2) * 24 + slot * 4 + (ch & 3)) * 2, 0, 16);
    }
}
template <int LO, int HI> DI void retile_request(u32x4 (&r)[4][4], const u16* __restrict__ w2, int r0, int w, int lane) {
  const __amdgpu_buffer_rsrc_t sW = mksrd(w2, F * D * 2);
  const int wv = (r0 - 60) * 4 + w;
#pragma unroll
  for (int n = LO; n < HI; n++) {
    const int u = wv + 144 * n, uc = u < 540 ? u : 539, ks = uc / 10, cc = uc - 10 * ks;
#pragma unroll
    for (int i = 0; i < 4; i++) r[n][i] = __builtin_amdgcn_raw_buffer_load_b128(sW, ((8 * i + (lane >> 3)) * D + 8 * (lane & 7)) * 2, (32 * ks * D + 64 * cc) * 2, 0);
  }
}
DI void retile_finish(const u32x4 (&r)[4][4], char* __restrict__ ws, char* smem, int r0, int w, int lane) {
    const int wv = (r0 - 60) * 4 + w;
    char* mb = smem + L_Y + w * (32 * 144);
    const int q = lane >> 4, i16 = lane & 15, jj = i16 >> 2, p = i16 & 3;
    const __amdgpu_buffer_rsrc_t sO = mksrd(ws + WS_W2F, 2211840);
    const u32 a0 = ldsaddr(mb) + (4 * q + jj) * 144 + 8 * p;
#pragma unroll
    for (int n = 0; n < 4; n++) {
      const int u = wv + 144 * n, uc = u < 540 ? u : 539, ks = uc / 10, cc = uc - 10 * ks;
#pragma unroll
      for (int i = 0; i < 4; i++) *(u32x4*)(mb + (8 * i + (lane >> 3)) * 144 + 16 * (lane & 7)) = r[n][i];
      frag v[4];
#pragma unroll
      for (int f = 0; f < 4; f++) v[f] = catf(trread(a0 + 32 * f), trread(a0 + 32 * f + 16 * 144));
#pragma unroll
      for (int f = 0; f < 4; f++) {
        const int ft = 4 * cc + f;
        if (u < 540) __builtin_amdgcn_raw_buffer_store_b128(v[f], sO, (((ft / 5) * 54 + ks) * 5 + (ft % 5)) * 1024 + lane * 16, 0, 16);
      }
    }
}

DI f32x2 gelu2(f32x2 x) {
  const float c0 = -2.f * 0.7978845608028654f * 1.4426950408889634f, c1 = c0 * 0.044715f;
  const f32x2 z = x * (x * x * (f32x2){c1, c1} + (f32x2){c0, c0});
  const f32x2 d = (f32x2){__builtin_amdgcn_exp2f(z[0]), __builtin_amdgcn_exp2f(z[1])} + (f32x2){1.f, 1.f};
  return x * (f32x2){__builtin_amdgcn_rcpf(d[0]), __builtin_amdgcn_rcpf(d[1])};
}
DI int rotk(int n, int rot, int m) { int k = n + rot; return k >= m ? k - m : k; }
struct W2C { u32 l16; int rot2; __amdgpu_buffer_rsrc_t srd; };
template <int I> DI void w2_load1(frag (&W2)[60], const W2C& c) {
  const u32 vo = c.l16 + rotk(I / 5, c.rot2, 14) * 5120;
  if constexpr (I % 5 < 4) bload_a_sc1<(I % 5) * 1024>(W2[I], vo, c.srd, 0); else bload_a_sc1<0>(W2[I], vo + 4096, c.srd, 0);
}
template <int I, int N> DI void w2_loads(frag (&W2)[60], const W2C& c) {
  w2_load1<I>(W2, c);
  if constexpr (N > 1) w2_loads<I + 1, N - 1>(W2, c);
}
#define W2_M0V 8
#define W2_M1V 16
#define W2_G1V 20
#define W2_GEV 6
constexpr int W2_M0 = W2_M0V, W2_M1 = W2_M1V, W2_G1 = W2_G1V, W2_GE = W2_GEV;
template <int LO, int N, int LIM> DI void w2_part(frag (&W2)[60], const W2C& c) {
  if constexpr (LO < LIM) w2_loads<LO, (LO + N <= LIM ? N : LIM - LO)>(W2, c);
}

constexpr int G1R = 6;
struct AF { u32x2 lo, hi; };
struct G1S {
  frag Y[G1R][2];
  AF A0[7], A1[7];
  f32x4 acc[7][2];
};
template <int FI> DI void g1_lda(AF& a, u32 ab) {
  asm volatile("ds_read_b64_tr_b16 %0, %2 offset:%c3\n\tds_read_b64_tr_b16 %1, %2 offset:%c4" : "=&v"(a.lo), "=&v"(a.hi) : "v"(ab), "i"(32 * FI), "i"(32 * FI + 1792));
}
DI frag af(const AF& a) { return (frag){a.lo[0], a.lo[1], a.hi[0], a.hi[1]}; }
template <int N> DI void g1_waitA4(AF& a, AF& b, AF& c, AF& d) {
  asm volatile("s_waitcnt lgkmcnt(%c8)" : "+v"(a.lo), "+v"(a.hi), "+v"(b.lo), "+v"(b.hi), "+v"(c.lo), "+v"(c.hi), "+v"(d.lo), "+v"(d.hi) : "i"(N));
}
template <int N> DI void g1_waitA3(AF& a, AF& b, AF& c) {
  asm volatile("s_waitcnt lgkmcnt(%c6)" : "+v"(a.lo), "+v"(a.hi), "+v"(b.lo), "+v"(b.hi), "+v"(c.lo), "+v"(c.hi) : "i"(N));
}
struct G1C { u32 b0, l16, ybase; int rot1; __amdgpu_buffer_rsrc_t srdY; };
template <int KS, int FI>
DI void g1_f(G1S& s, AF (&Ac)[7], AF (&An)[7], frag (&W2)[60], const G1C& c, const W2C& c2, u32 ab, u32 yo) {
  if constexpr (FI == 0) g1_waitA4<6>(Ac[0], Ac[1], Ac[2], Ac[3]);
  if constexpr (FI == 4) g1_waitA3<(KS < 19) ? 8 : 0>(Ac[4], Ac[5], Ac[6]);
  const frag a = af(Ac[FI]);
  mfma_vv(s.acc[FI][0], a, s.Y[KS % G1R][0]);
  if constexpr (KS < 19) g1_lda<FI>(An[FI], ab);
  mfma_vv(s.acc[FI][1], a, s.Y[KS % G1R][1]);
  if constexpr (KS >= 1 && KS - 1 + G1R < 20 && (FI == 1 || FI == 4)) bload_sc1<(FI == 4) * 1024>(s.Y[(KS - 1) % G1R][FI == 4], c.l16, c.srdY, yo);
  if constexpr (KS < W2_G1 && FI == 6) w2_load1<W2_M0 + W2_M1 + KS>(W2, c2);
  if constexpr (FI < 6) g1_f<KS, FI + 1>(s, Ac, An, W2, c, c2, ab, yo);
}
template <int KS> constexpr int g1_younger() {
  int cnt = 0; bool seen = false;
  for (int k = 0; k < G1R; k++) { if (seen) cnt += 2; if (k == KS) seen = true; }
  for (int n = 0; n < KS; n++) {
    if (n >= 1 && n - 1 + G1R < 20) { if (seen) cnt += 2; if (n - 1 + G1R == KS) seen = true; }
    if (n < W2_G1 && seen) cnt += 1;
  }
  return cnt;
}
template <int KS>
DI void g1_step(G1S& s, frag (&W2)[60], const G1C& c, const W2C& c2) {
  waitv2<g1_younger<KS>()>(s.Y[KS % G1R][0], s.Y[KS % G1R][1]);
  const u32 ab = c.b0 + rotk(KS + 1, c.rot1, 20) * 7168;
  const u32 yo = c.ybase + rotk(KS - 1 + G1R, c.rot1, 20) * 2048;
  if constexpr (KS & 1) g1_f<KS, 0>(s, s.A1, s.A0, W2, c, c2, ab, yo);
  else                  g1_f<KS, 0>(s, s.A0, s.A1, W2, c, c2, ab, yo);
  if constexpr (KS < 19) g1_step<KS + 1>(s, W2, c, c2);
}
struct G2S {
  frag H[4][4];
  frag WX[10];
  f32x4 acc[5][4];
  u32x2 xx[5], gg[5], b2v[5];
};
struct G2C { u32 l16, vx, vb2, vg; int rot2; __amdgpu_buffer_rsrc_t srdH, srdX, srdB2, srdMD; };
template <int N, int T> DI void g2_hload1(G2S& s, const G2C& c, u32 vo) { bload_sc1<T * 1024>(s.H[N & 3][T], vo, c.srdH, 0); }
template <int N> DI void g2_hload(G2S& s, const G2C& c) {
  const u32 vo = c.l16 + rotk(N, c.rot2, 14) * 4096;
  g2_hload1<N, 0>(s, c, vo); g2_hload1<N, 1>(s, c, vo); g2_hload1<N, 2>(s, c, vo); g2_hload1<N, 3>(s, c, vo);
}
template <int I> DI void g2_eload(G2S& s, const G2C& c) {
  if constexpr (I < 5) bload2_sc1<32 * I>(s.xx[I], c.vx, c.srdX, 0);
  else if constexpr (I < 10) bload2<32 * (I - 5)>(s.b2v[I - 5], c.vb2, c.srdB2, 0);
  else bload2_sc1<192 * (I - 10)>(s.gg[I - 10], c.vg, c.srdMD, 0);
}
template <int N, int M>
DI void g2_m(G2S& s, frag (&W2)[60], const G2C& c, u32 vo) {
  constexpr int d = M >> 2, t = M & 3;
  if constexpr (N == 0) mfma_av0(s.acc[d][t], W2[d], s.H[0][t]);
  else if constexpr (N < 12) mfma_av(s.acc[d][t], W2[N * 5 + d], s.H[N & 3][t]);
  else mfma_vv(s.acc[d][t], s.WX[(N - 12) * 5 + d], s.H[N & 3][t]);
  if constexpr (N + 3 < 14 && (M == 1 || M == 6 || M == 11 || M == 16)) g2_hload1<N + 3, (M - 1) / 5>(s, c, vo);
  if constexpr (N >= 11 && (M == 1 || M == 5 || M == 9 || M == 13 || M == 17)) g2_eload<5 * (N - 11) + (M - 1) / 4>(s, c);
  if constexpr (M < 19) g2_m<N, M + 1>(s, W2, c, vo);
}
template <int N> constexpr int g2_younger() {
  int cnt = 0; bool seen = false;
  for (int h = 0; h < 3; h++) { if (seen) cnt += 4; if (h == N) seen = true; }
  for (int n = 0; n < N; n++) {
    if (n + 3 < 14) { if (seen) cnt += 4; if (n + 3 == N) seen = true; }
    if (n >= 11 && seen) cnt += 5;
  }
  return cnt;
}
static_assert(g2_younger<0>() == 8 && g2_younger<3>() == 8 && g2_younger<11>() == 8 && g2_younger<12>() == 9 && g2_younger<13>() == 10, "");
template <int N>
DI void g2_step(G2S& s, frag (&W2)[60], const G2C& c) {
  waitv4<g2_younger<N>()>(s.H[N & 3][0], s.H[N & 3][1], s.H[N & 3][2], s.H[N & 3][3]);
  const u32 vo = c.l16 + rotk(N + 3, c.rot2, 14) * 4096;
  g2_m<N, 0>(s, W2, c, vo);
  if constexpr (N < 13) g2_step<N + 1>(s, W2, c);
}

__global__ __launch_bounds__(256, 1) void k_main(const u16* __restrict__ qin, const u16* __restrict__ kin, const u16* __restrict__ vin,
                                                 const u16* __restrict__ shk, const u16* __restrict__ ssk, const u16* __restrict__ ssD,
                                                 const u16* __restrict__ cin, const u16* __restrict__ wmod, const u16* __restrict__ w1,
                                                 const u16* __restrict__ b1, const u16* __restrict__ w2, const u16* __restrict__ b2,
                                                 char* __restrict__ ws, u16* __restrict__ out) {
  STAMP_BEGIN(ws)
  MARK_DECL
  extern __shared__ __attribute__((aligned(16))) char smem[];
  const int tid = threadIdx.x, lane = tid & 63, w = rfl(tid >> 6);
  const int b = blockIdx.x, xcd = b & 7, j = b >> 3;
  const int tile = 4 * (xcd >> 1) + (j >> 3), g = 8 * (xcd & 1) + (j & 7);
  const int TT = 4 * xcd + (j >> 3), cg = j & 7;
  const u32 l16 = lane * 16, lds0 = ldsaddr(smem);
  W1C wc;
#pragma unroll
  for (int r = 0; r < 7; r++) { const u32 idx = 64 * r + lane, row = (idx * 4682u) >> 16; wc.vo[r] = row * (F * 2 - 224) + idx * 16; }
  wc.m0b = lds0 + L_W1 + w * 7168; wc.sob = w * (32 * F * 2) + 224 * g; wc.srd = mksrd(w1, D * F * 2);
  const int tg = 16 * tile + g, t0 = 8 * tg;
  const int tk = lane & 7, slot = lane >> 3, q = lane >> 4;
  const u64 tag = call_tag(__hip_atomic_load((const GAS u32*)(ws + WS_EPOCH), __ATOMIC_RELAXED, __HIP_MEMORY_SCOPE_AGENT));
  asm volatile("s_nop 4");
  prep_request(kin, vin, shk, lds0, b >> 1, b & 1, w, lane);
  __builtin_amdgcn_s_barrier();
  if (w == 3) {
    unsigned long long pc; u32 len;
    asm volatile("s_getpc_b64 %0\n.Lkmain_here:\n\ts_mov_b32 %1, .Lkmain_end-.Lkmain_here" : "=s"(pc), "=s"(len));
    dma4(lds0 + L_RED + 768, (j + 32 * lane) * 128, mksrd((const void*)pc, len), 0);
  }
  if (w == 2 && j < 2) {
    const char* p = ws + (size_t)(lane < 13 ? lane : 0) * 2097152;
    if (lane == 13 || lane == 14) p = (const char*)out + (lane - 13) * 2097152;
    if (lane == 15 || lane == 16) p = (const char*)w1 + (lane - 15) * 2097152;
    if (lane == 17 || lane == 18) p = (const char*)w2 + (lane - 17) * 2097152;
    if (lane >= 19 && lane <= 21) p = (const char*)wmod + (lane - 19) * 2097152;
    if (lane == 22) p = (const char*)b1;
    if (lane == 23) p = (const char*)b2;
    if (lane >= 24) p = (const char*)cin;
    asm volatile("s_mov_b32 m0, %0\n\ts_nop 0\n\tglobal_load_lds_dword %1, off sc1" ::"s"(lds0 + L_RED + 768), "v"(p) : "m0");
  }
  MARK(21);
  u32x2 xr[5];
  u32 park[10];
  const int r0 = (j - 20) * 8 + xcd;
  if (j < 20) {
    const int ch = 80 * xcd + 4 * j + w;
    const __amdgpu_buffer_rsrc_t sF = mksrd(ssk + (size_t)ch * S, S * 2);
    u32x4 rk[4];
#pragma unroll
    for (int i = 0; i < 4; i++) rk[i] = __builtin_amdgcn_raw_buffer_load_b128(sF, l16 + 1024 * i, 0, 0);
    const u32 Draw = __builtin_amdgcn_raw_buffer_load_b32(mksrd(ssD, D * 2), (ch >> 1) * 4, 0, 0);
    q_request(xr, qin, t0, w, lane);
    prep_finish(xr, ws, smem, b >> 1, b & 1, tid, lane, w, tag);
    MARK(23);
    phase_conv(rk, Draw, xr, park, ws, smem, xcd, j, w, lane, tag, wc
    );
  } else if (r0 < 60) {
    u32x4 r[20];
    q_request(xr, qin, t0, w, lane);
    gemv_request<0, PRE_G>(r, wmod, r0, tid);
    prep_finish(xr, ws, smem, b >> 1, b & 1, tid, lane, w, tag);
    gemv_request<PRE_G, 20>(r, wmod, r0, tid);
    SB0;
    gemv_finish(r, cin, ws, smem, r0, w, tid, lane);
    ln1(xr, park, (f32x2*)(smem + P_RED), w, lane);
  } else {
    u32x4 r[4][4];
    q_request(xr, qin, t0, w, lane);
    retile_request<0, PRE_R>(r, w2, r0, w, lane);
    prep_finish(xr, ws, smem, b >> 1, b & 1, tid, lane, w, tag);
    retile_request<PRE_R, 4>(r, w2, r0, w, lane);
    SB0;
    retile_finish(r, ws, smem, r0, w, lane);
    ln1(xr, park, (f32x2*)(smem + P_RED), w, lane);
  }
  waitv<0>();
  __builtin_amdgcn_s_barrier();
  MARK(4);
  if (tid < 8) signal(ws, WS_FG + tid * 2048 + b * 8, tag);
  const int ks0 = w < 2 ? 14 * w : 28 + 13 * (w - 2), nks = w < 2 ? 14 : 13;
  W2C c2;
  c2.l16 = l16; c2.srd = mksrd(ws + WS_W2F + (size_t)cg * 276480 + ks0 * 5120, nks * 5120);
  { int r2 = 2 * cg + 4 * (j >> 3); r2 = r2 >= 14 ? r2 - 14 : r2; c2.rot2 = r2 >= 14 ? r2 - 14 : r2; }
  frag W2[60];
  f32x2 x[10], t[10];
  f32x2* red = (f32x2*)(smem + L_RED);
  if (j >= 20) { w1_step<0>(wc); w1_step<1>(wc); w1_step<2>(wc); w1_step<3>(wc); }
  if (w == 0) poll_tags256(ws + WS_FG + xcd * 2048, tag, lane);
  __builtin_amdgcn_s_barrier();
  if (b == 0 && tid == 0) __hip_atomic_store((GAS u32*)(ws + WS_EPOCH), (u32)tag, __ATOMIC_RELAXED, __HIP_MEMORY_SCOPE_AGENT);
  MARK(5);
  const __amdgpu_buffer_rsrc_t srdMD = mksrd(ws + WS_MODP, 7680);
  u32x2 bb[7];
  {
    const __amdgpu_buffer_rsrc_t srdYB = mksrd(ws + WS_Y2B + (size_t)(tg >> 1) * 20480, 20480);
    const __amdgpu_buffer_rsrc_t srdB1 = mksrd(b1, F * 2);
    u32x4 yr[3], mr[2];
    const u32 v16 = tid * 16, vb = (16 * 7 * g + 4 * q) * 2;
    asm volatile("s_nop 4");
    MARK(20);
    const u32 vy = tid * 32 + 16 * (tg & 1);
    bload_sc1<0>(yr[0], vy, srdYB, 0); bload_sc1<0>(yr[1], vy + 8192, srdYB, 0); bload_sc1<0>(yr[2], vy + 16384, srdYB, 0);
    bload_sc1<0>(mr[0], v16, srdMD, 0); bload_sc1<0>(mr[1], v16 + 4096, srdMD, 0);
    w1_step<4>(wc);
    SB0;
#pragma unroll
      for (int i = 0; i < 5; i++) { x[2 * i] = up(xr[i][0]); x[2 * i + 1] = up(xr[i][1]); }
#pragma unroll
      for (int h = 0; h < 10; h++) { u32 n; asm volatile("v_accvgpr_read_b32 %0, %1" : "=v"(n) : "a"(park[h])); t[h] = up(n); asm volatile("" : "+v"(x[h]), "+v"(t[h])); }
    SB0;
    asm volatile("s_waitcnt vmcnt(7)" : "+v"(yr[0]), "+v"(yr[1]), "+v"(yr[2]), "+v"(mr[0]), "+v"(mr[1]));
    MARK(19);
    *(u32x4*)(smem + L_Y + v16) = yr[0]; *(u32x4*)(smem + L_Y + 4096 + v16) = yr[1];
    if (tid < 128) *(u32x4*)(smem + L_Y + 8192 + v16) = yr[2];
    *(u32x4*)(smem + L_MOD + v16) = mr[0]; *(u32x4*)(smem + L_MOD + 4096 + v16) = mr[1];
    lds_barrier();
    MARK(6);
    const int i16 = lane & 15, jj = i16 >> 2, p = i16 & 3;
    const u32 ya = lds0 + L_Y + (4 * (40 * w + 2 * q + (p >> 1)) + jj) * 16 + 8 * (p & 1);
    const char* mdp = smem + L_MOD + (40 * w + slot) * 48;
    u32 yy[10], w1v[10], sav[10], gav[10];
#pragma unroll
    for (int i = 0; i < 5; i++) {
      const u32x2 yu = __builtin_bit_cast(u32x2, trread(ya + i * 8 * 64));
      const u32x4 m0 = *(const u32x4*)(mdp + i * 384);
      const u32x2 m1 = *(const u32x2*)(mdp + i * 384 + 16);
      yy[2 * i] = yu[0]; yy[2 * i + 1] = yu[1]; w1v[2 * i] = m0[0]; w1v[2 * i + 1] = m0[1]; sav[2 * i] = m0[2]; sav[2 * i + 1] = m0[3]; gav[2 * i] = m1[0]; gav[2 * i + 1] = m1[1];
    }
    u32 o[10];
#define STAGE(EXPR) _Pragma("unroll") for (int h = 0; h < 10; h++) { EXPR; }
    STAGE(t[h] = rb2(t[h] * up(w1v[h])))
    SB0; w2_part<0, 2, W2_M0>(W2, c2); SB0;
    STAGE(t[h] = rb2(t[h] + up(sav[h])))
    SB0; w2_part<2, 2, W2_M0>(W2, c2); SB0;
    STAGE(t[h] = rb2(t[h] * up(yy[h])))
    SB0; w2_part<4, 2, W2_M0>(W2, c2); SB0;
    STAGE(t[h] = rb2(up(gav[h]) * t[h]))
    SB0; w2_part<6, 2, W2_M0>(W2, c2); SB0;
    STAGE(o[h] = pk(x[h] + t[h]))
    const __amdgpu_buffer_rsrc_t srdX1 = mksrd(ws + WS_X1, 2621440);
    const u32 xo = (t0 + tk) * 1280 + (40 * w + slot) * 8;
#pragma unroll
    for (int i = 0; i < 5; i++) __builtin_amdgcn_raw_buffer_store_b64((u32x2){o[2 * i], o[2 * i + 1]}, srdX1, xo + i * 64, 0, 16);
    STAGE(x[h] = up(o[h]))
    MARK(7);
    float mean, rinv;
    SB0; w2_part<8, 3, W2_M0>(W2, c2); SB0;
    ln_stats(x, red, w, lane, mean, rinv);
    SB0; w2_part<11, 3, W2_M0>(W2, c2); SB0;
    {
      const f32x2 m2 = {mean, mean}, r2 = {rinv, rinv};
#pragma unroll
      for (int i = 0; i < 5; i++) {
        const u32x4 m0 = *(const u32x4*)(mdp + i * 384 + 16);
        const u32x2 m1 = *(const u32x2*)(mdp + i * 384 + 32);
        x[2 * i] = (x[2 * i] - m2) * r2 * up(m0[2]) + up(m1[0]);
        x[2 * i + 1] = (x[2 * i + 1] - m2) * r2 * up(m0[3]) + up(m1[1]);
      }
    }
    MARK(8);
    SB0;
    {
      bload2<0>(bb[0], vb, srdB1, 0); bload2<32>(bb[1], vb, srdB1, 0); bload2<64>(bb[2], vb, srdB1, 0); bload2<96>(bb[3], vb, srdB1, 0);
      bload2<128>(bb[4], vb, srdB1, 0); bload2<160>(bb[5], vb, srdB1, 0); bload2<192>(bb[6], vb, srdB1, 0);
    }
    SB0;
    ln_stats(x, red + 32, w, lane, mean, rinv);
    {
      const f32x2 m3 = {mean, mean}, r3 = {rinv, rinv};
      STAGE(o[h] = pk((x[h] - m3) * r3))
      const int rt = tg >> 1, r = 8 * (tg & 1) + tk;
      const __amdgpu_buffer_rsrc_t srdY3 = mksrd(ws + WS_Y3F, 2621440);
      const u32 yo = (rt >> 1) * 40960 + (rt & 1) * 1024 + ((2 * (slot >> 2) + (slot & 1)) * 16 + r) * 16 + 8 * ((slot >> 1) & 1) + 5 * w * 2048;
#pragma unroll
      for (int i = 0; i < 5; i++) __builtin_amdgcn_raw_buffer_store_b64((u32x2){o[2 * i], o[2 * i + 1]}, srdY3, yo + i * 2048, 0, 16);
    }
#undef STAGE
  }
  MARK(9);
  asm volatile("s_waitcnt vmcnt(0)" : "+v"(bb[0]), "+v"(bb[1]), "+v"(bb[2]), "+v"(bb[3]), "+v"(bb[4]), "+v"(bb[5]), "+v"(bb[6]) :: "memory");
  __builtin_amdgcn_s_barrier();
  if (tid == 0) signal(ws, WS_FA + (tile * 16 + g) * 64, tag);
  MARK(10);
  w2_loads<W2_M0, W2_M1>(W2, c2);
  if (w == 0) poll_tags(ws + WS_FA + tile * 1024, 64, 16, tag, lane);
  __builtin_amdgcn_s_barrier();
  MARK(11);
  f32x4 hacc[7][2];
  {
    G1C c1;
    const int i16 = lane & 15, jj = i16 >> 2, p = i16 & 3;
    c1.srdY = mksrd(ws + WS_Y3F, 2621440); c1.l16 = l16;
    c1.rot1 = (5 * (j & 7)) >> 1;
    c1.ybase = (tile * 4 + w) * 40960;
    c1.b0 = lds0 + L_W1 + (16 * (q >> 1) + 4 * (q & 1) + jj) * 224 + 8 * p;
    G1S s;
    asm volatile("s_nop 4");
#pragma unroll
    for (int k = 0; k < G1R; k++) { const u32 yo = c1.ybase + rotk(k, c1.rot1, 20) * 2048; bload_sc1<0>(s.Y[k][0], l16, c1.srdY, yo); bload_sc1<1024>(s.Y[k][1], l16, c1.srdY, yo); }
#pragma unroll
    for (int f = 0; f < 7; f++) {
      const f32x4 bv = {lo16(bb[f][0]), hi16(bb[f][0]), lo16(bb[f][1]), hi16(bb[f][1])};
      s.acc[f][0] = bv; s.acc[f][1] = bv;
      asm volatile("" : "+v"(s.acc[f][0]), "+v"(s.acc[f][1]));
    }
    const u32 ab0 = c1.b0 + c1.rot1 * 7168;
    asm volatile("s_waitcnt lgkmcnt(0)" ::: "memory");
    g1_lda<0>(s.A0[0], ab0); g1_lda<1>(s.A0[1], ab0); g1_lda<2>(s.A0[2], ab0); g1_lda<3>(s.A0[3], ab0);
    g1_lda<4>(s.A0[4], ab0); g1_lda<5>(s.A0[5], ab0); g1_lda<6>(s.A0[6], ab0);
    g1_step<0>(s, W2, c1, c2);
    asm volatile("s_nop 7\n\ts_nop 1");
#pragma unroll
    for (int f = 0; f < 7; f++) { asm volatile("" : "+v"(s.acc[f][0]), "+v"(s.acc[f][1])); hacc[f][0] = s.acc[f][0]; hacc[f][1] = s.acc[f][1]; }
  }
  MARK(12);
  G2S s2_;
  {
    const __amdgpu_buffer_rsrc_t srdH = mksrd(ws + WS_HF, 7077888);
    const int rt0 = 8 * tile + 2 * w;
    const u32 ho = ((rt0 >> 2) * 54 * 4 + (rt0 & 3)) * 1024 + l16 + ((7 * g) >> 1) * 4096;
    auto gel = [&](int f, int tt) -> u32x2 {
      const f32x4 a = hacc[f][tt];
      return (u32x2){pk(gelu2((f32x2){a[0], a[1]})), pk(gelu2((f32x2){a[2], a[3]}))};
    };
    SB0; w2_part<W2_M0 + W2_M1 + W2_G1, W2_GE, 60>(W2, c2); SB0;
    if (g & 1) {
      { const u32x2 a = gel(0, 0), c = gel(0, 1);
        __builtin_amdgcn_raw_buffer_store_b64(a, srdH, ho + 8, 0, 16); __builtin_amdgcn_raw_buffer_store_b64(c, srdH, ho + 1024 + 8, 0, 16); }
#pragma unroll
      for (int i = 0; i < 3; i++) {
        const u32x2 a0 = gel(2 * i + 1, 0), a1 = gel(2 * i + 2, 0), c0 = gel(2 * i + 1, 1), c1 = gel(2 * i + 2, 1);
        if (7 * g + 2 * i + 2 < 108) {
          __builtin_amdgcn_raw_buffer_store_b128((u32x4){a0[0], a0[1], a1[0], a1[1]}, srdH, ho + (1 + i) * 4096, 0, 16);
          __builtin_amdgcn_raw_buffer_store_b128((u32x4){c0[0], c0[1], c1[0], c1[1]}, srdH, ho + (1 + i) * 4096 + 1024, 0, 16);
        }
      }
    } else {
#pragma unroll
      for (int i = 0; i < 3; i++) {
        const u32x2 a0 = gel(2 * i, 0), a1 = gel(2 * i + 1, 0), c0 = gel(2 * i, 1), c1 = gel(2 * i + 1, 1);
        __builtin_amdgcn_raw_buffer_store_b128((u32x4){a0[0], a0[1], a1[0], a1[1]}, srdH, ho + i * 4096, 0, 16);
        __builtin_amdgcn_raw_buffer_store_b128((u32x4){c0[0], c0[1], c1[0], c1[1]}, srdH, ho + i * 4096 + 1024, 0, 16);
      }
      { const u32x2 a = gel(6, 0), c = gel(6, 1);
        __builtin_amdgcn_raw_buffer_store_b64(a, srdH, ho + 3 * 4096, 0, 16); __builtin_amdgcn_raw_buffer_store_b64(c, srdH, ho + 3 * 4096 + 1024, 0, 16); }
    }
  }
  MARK(13);
  waitv<0>();
  __builtin_amdgcn_s_barrier();
  if (tid == 0) signal(ws, WS_FB + (tile * 16 + g) * 64, tag);
  {
    const u32 vo = l16 + rotk(12, c2.rot2, 14) * 5120, v1 = l16 + rotk(13, c2.rot2, 14) * 5120;
    w2_part<W2_M0 + W2_M1 + W2_G1 + W2_GE, 60, 60>(W2, c2);
    bload_sc1<0>(s2_.WX[0], vo, c2.srd, 0); bload_sc1<1024>(s2_.WX[1], vo, c2.srd, 0); bload_sc1<2048>(s2_.WX[2], vo, c2.srd, 0); bload_sc1<3072>(s2_.WX[3], vo, c2.srd, 0);
    bload_sc1<0>(s2_.WX[4], vo + 4096, c2.srd, 0);
    bload_sc1<0>(s2_.WX[5], v1, c2.srd, 0); bload_sc1<1024>(s2_.WX[6], v1, c2.srd, 0); bload_sc1<2048>(s2_.WX[7], v1, c2.srd, 0); bload_sc1<3072>(s2_.WX[8], v1, c2.srd, 0);
    bload_sc1<0>(s2_.WX[9], v1 + 4096, c2.srd, 0);
  }
  MARK(14);
  const int cc = lane & 15, tok = 64 * TT + 16 * w + cc;
  G2C cg2;
  cg2.l16 = l16; cg2.rot2 = c2.rot2;
  cg2.srdH = mksrd(ws + WS_HF + (size_t)TT * 221184 + ks0 * 4096, nks * 4096);
  cg2.srdX = mksrd(ws + WS_X1, 2621440); cg2.srdB2 = mksrd(b2, D * 2); cg2.srdMD = srdMD;
  cg2.vx = tok * 1280 + (80 * cg + 4 * q) * 2; cg2.vb2 = (80 * cg + 4 * q) * 2; cg2.vg = (20 * cg + q) * 48 + 40;
  if (w == 0) poll_tags(ws + WS_FB + (TT >> 1) * 1024, 64, 16, tag, lane);
  __builtin_amdgcn_s_barrier();
  MARK(15);
  asm volatile("s_nop 4");
  g2_hload<0>(s2_, cg2); g2_hload<1>(s2_, cg2); g2_hload<2>(s2_, cg2);
  asm volatile("s_waitcnt vmcnt(12)" : "+v"(s2_.WX[0]), "+v"(s2_.WX[1]), "+v"(s2_.WX[2]), "+v"(s2_.WX[3]), "+v"(s2_.WX[4]), "+v"(s2_.WX[5]), "+v"(s2_.WX[6]), "+v"(s2_.WX[7]), "+v"(s2_.WX[8]), "+v"(s2_.WX[9]));
  g2_step<0>(s2_, W2, cg2);
  asm volatile("s_nop 7\n\ts_nop 1");
#pragma unroll
  for (int d = 0; d < 5; d++) asm volatile("" : "+v"(s2_.acc[d][0]), "+v"(s2_.acc[d][1]), "+v"(s2_.acc[d][2]), "+v"(s2_.acc[d][3]));
  MARK(16);
  f32x4* redq = (f32x4*)smem;
  f32x4 own[5];
#define G2_OWN(T) { _Pragma("unroll") for (int d = 0; d < 5; d++) { own[d] = s2_.acc[d][T]; \
    _Pragma("unroll") for (int t = 0; t < 4; t++) if (t != T) redq[((T * 4 + t) * 5 + d) * 64 + lane] = s2_.acc[d][t]; } }
  if (w == 0) G2_OWN(0) else if (w == 1) G2_OWN(1) else if (w == 2) G2_OWN(2) else G2_OWN(3)
#undef G2_OWN
  lds_barrier();
  asm volatile("s_waitcnt vmcnt(0)" : "+v"(s2_.xx[0]), "+v"(s2_.xx[1]), "+v"(s2_.xx[2]), "+v"(s2_.xx[3]), "+v"(s2_.xx[4]), "+v"(s2_.b2v[0]), "+v"(s2_.b2v[1]), "+v"(s2_.b2v[2]), "+v"(s2_.b2v[3]), "+v"(s2_.b2v[4]),
               "+v"(s2_.gg[0]), "+v"(s2_.gg[1]), "+v"(s2_.gg[2]), "+v"(s2_.gg[3]), "+v"(s2_.gg[4]));
  MARK(17);
  {
    const f32x4* r1 = redq + ((((w + 1) & 3) * 4 + w) * 5) * 64 + lane;
    const f32x4* r2 = redq + ((((w + 2) & 3) * 4 + w) * 5) * 64 + lane;
    const f32x4* r3 = redq + ((((w + 3) & 3) * 4 + w) * 5) * 64 + lane;
#pragma unroll
    for (int d = 0; d < 5; d++) {
      const f32x4 sacc = (own[d] + r1[d * 64]) + (r2[d * 64] + r3[d * 64]);
      u32x2 o;
#pragma unroll
      for (int h = 0; h < 2; h++) {
        o[h] = pk(up(s2_.xx[d][h]) + up(s2_.gg[d][h]) * ((f32x2){sacc[2 * h], sacc[2 * h + 1]} + up(s2_.b2v[d][h])));
      }
      *(u32x2*)(out + (size_t)tok * D + 80 * cg + 16 * d + 4 * q) = o;
    }
  }
  asm volatile(".Lkmain_end:");
  MARK(18);
  MARK_FLUSH(ws, 0, 24);
  STAMP_END(ws, 0);
}

extern "C" void kernel_launch(void* const* d_in, const int* in_sizes, int n_in, void* d_out, int out_size, void* d_ws, size_t ws_size,
                              hipStream_t stream) {
  const u16* q = (const u16*)d_in[0]; const u16* k = (const u16*)d_in[1]; const u16* v = (const u16*)d_in[2];
  const u16* shk = (const u16*)d_in[3]; const u16* ssk = (const u16*)d_in[4]; const u16* ssD = (const u16*)d_in[5];
  const u16* w1 = (const u16*)d_in[7]; const u16* b1 = (const u16*)d_in[8]; const u16* w2 = (const u16*)d_in[9];
  const u16* b2 = (const u16*)d_in[10]; const u16* c = (const u16*)d_in[11]; const u16* wmod = (const u16*)d_in[12];
  char* ws = (char*)d_ws;
  (void)hipFuncSetAttribute((const void*)k_main, hipFuncAttributeMaxDynamicSharedMemorySize, L_TOTAL);
  k_main<<<256, 256, L_TOTAL, stream>>>(q, k, v, shk, ssk, ssD, c, wmod, w1, b1, w2, b2, ws, (u16*)d_out);
}
```

```cpp
#include <hip/hip_runtime.h>
#include <stdint.h>

typedef unsigned short u16;
typedef unsigned int u32;
typedef short s16x4 __attribute__((ext_vector_type(4)));
typedef __bf16 bf16x2 __attribute__((ext_vector_type(2)));
typedef float f32x4 __attribute__((ext_vector_type(4)));
typedef float f32x2 __attribute__((ext_vector_type(2)));
typedef u32 u32x4 __attribute__((ext_vector_type(4)));
typedef u32 u32x2 __attribute__((ext_vector_type(2)));
#define LAS __attribute__((address_space(3)))
#define GAS __attribute__((address_space(1)))
#define DI __device__ __forceinline__

constexpr int S = 2048, D = 640, F = 1728;
constexpr size_t WS_MODP = 0;
constexpr size_t WS_EPOCH = 8192;
constexpr size_t WS_FU = 16384;
constexpr size_t WS_FA = WS_FU + 16384;
constexpr size_t WS_FB = WS_FA + 16384;
constexpr size_t WS_FG = WS_FB + 16384;
constexpr size_t WS_U    = 131072;
constexpr size_t WS_Y2B  = WS_U + 2621440;
constexpr size_t WS_X1   = WS_Y2B + 2621440;
constexpr size_t WS_Y3F  = WS_X1 + 2621440;
constexpr size_t WS_HF   = WS_Y3F + 2621440;
constexpr size_t WS_W2F  = WS_HF + 7077888;

DI float lo16(u32 x) { return __uint_as_float(x << 16); }
DI float hi16(u32 x) { return __uint_as_float(x & 0xffff0000u); }
DI u32 pk(f32x2 v) { return __builtin_bit_cast(u32, __builtin_convertvector(v, bf16x2)); }
DI u32 pk2(float a, float b) { return pk((f32x2){a, b}); }
DI f32x2 up(u32 x) { return (f32x2){lo16(x), hi16(x)}; }
DI f32x2 rb2(f32x2 v) { return up(pk(v)); }
DI float rbf(float a) { return lo16(pk2(a, a)); }
DI int rfl(int x) { return __builtin_amdgcn_readfirstlane(x); }
DI s16x4 trread(u32 ldsaddr) { return __builtin_amdgcn_ds_read_tr16_b64_v4i16((LAS s16x4*)(size_t)ldsaddr); }
DI u32 ldsaddr(const void* p) { return (u32)(size_t)(LAS const char*)p; }
DI __amdgpu_buffer_rsrc_t mksrd(const void* p, u32 bytes) { return __builtin_amdgcn_make_buffer_rsrc((void*)p, 0, bytes, 0x00020000); }

DI void ln_finish(float sum, float sq, float& mean, float& rinv) {
  const float m = sum * (1.f / D);
  const float var = fmaxf(sq * (1.f / D) - m * m, 0.f);
  mean = rbf(m);
  rinv = __builtin_amdgcn_rcpf(rbf(__builtin_amdgcn_sqrtf(rbf(rbf(var) + 1e-5f))));
}

template <int IMM> DI void bload_sc1(u32x4& dst, u32 voff, __amdgpu_buffer_rsrc_t srd, u32 soff) {
  asm volatile("buffer_load_dwordx4 %0, %1, %2, %3 offen offset:%c4 sc1" : "=v"(dst) : "v"(voff), "s"(srd), "s"(soff), "i"(IMM));
}
template <int IMM> DI void bload_a_sc1(u32x4& dst, u32 voff, __amdgpu_buffer_rsrc_t srd, u32 soff) {
  asm volatile("buffer_load_dwordx4 %0, %1, %2, %3 offen offset:%c4 sc1" : "=a"(dst) : "v"(voff), "s"(srd), "s"(soff), "i"(IMM));
}
template <int IMM> DI void bload2_sc1(u32x2& dst, u32 voff, __amdgpu_buffer_rsrc_t srd, u32 soff) {
  asm volatile("buffer_load_dwordx2 %0, %1, %2, %3 offen offset:%c4 sc1" : "=v"(dst) : "v"(voff), "s"(srd), "s"(soff), "i"(IMM));
}
template <int IMM> DI void bload2(u32x2& dst, u32 voff, __amdgpu_buffer_rsrc_t srd, u32 soff) {
  asm volatile("buffer_load_dwordx2 %0, %1, %2, %3 offen offset:%c4" : "=v"(dst) : "v"(voff), "s"(srd), "s"(soff), "i"(IMM));
}
DI void dma16(u32 m0v, u32 voff, __amdgpu_buffer_rsrc_t srd, u32 soff) {
  asm volatile("s_mov_b32 m0, %0\n\ts_nop 0\n\tbuffer_load_dwordx4 %1, %2, %3 offen lds" ::"s"(m0v), "v"(voff), "s"(srd), "s"(soff) : "m0");
}
DI void dma4(u32 m0v, u32 voff, __amdgpu_buffer_rsrc_t srd, u32 soff) {
  asm volatile("s_mov_b32 m0, %0\n\ts_nop 0\n\tbuffer_load_dword %1, %2, %3 offen lds" ::"s"(m0v), "v"(voff), "s"(srd), "s"(soff) : "m0");
}
typedef u32x4 frag;
DI void mfma_vv(f32x4& acc, const frag& a, const frag& b) { asm volatile("v_mfma_f32_16x16x32_bf16 %0, %1, %2, %0" : "+v"(acc) : "v"(a), "v"(b)); }
DI void mfma_vv0(f32x4& acc, const frag& a, const frag& b) { asm volatile("v_mfma_f32_16x16x32_bf16 %0, %1, %2, 0" : "=&v"(acc) : "v"(a), "v"(b)); }
DI void mfma_av(f32x4& acc, const frag& a, const frag& b) { asm volatile("v_mfma_f32_16x16x32_bf16 %0, %1, %2, %0" : "+v"(acc) : "a"(a), "v"(b)); }
DI void mfma_av0(f32x4& acc, const frag& a, const frag& b) { asm volatile("v_mfma_f32_16x16x32_bf16 %0, %1, %2, 0" : "=&v"(acc) : "a"(a), "v"(b)); }
template <int N> DI void waitv() { asm volatile("s_waitcnt vmcnt(%c0)" ::"i"(N) : "memory"); }
template <int N> DI void waitv2(u32x4& a, u32x4& b) { asm volatile("s_waitcnt vmcnt(%c2)" : "+v"(a), "+v"(b) : "i"(N)); }
template <int N> DI void waitv4(u32x4& a, u32x4& b, u32x4& c, u32x4& d) { asm volatile("s_waitcnt vmcnt(%c4)" : "+v"(a), "+v"(b), "+v"(c), "+v"(d) : "i"(N)); }
DI frag catf(s16x4 a, s16x4 b) { return __builtin_bit_cast(frag, __builtin_shufflevector(a, b, 0, 1, 2, 3, 4, 5, 6, 7)); }
#define SB0 __builtin_amdgcn_sched_barrier(0)

constexpr int CV_RS = 1184;
constexpr int CV_KB = 4128;
constexpr int CV_WB = CV_KB + 4 * CV_RS;
template <int OFF> DI frag cv_ldB(u32 vb) { return catf(*(LAS const s16x4*)(size_t)(vb + OFF), *(volatile LAS const s16x4*)(size_t)(vb + OFF + 8)); }
template <int G> constexpr int cv_off0() { return 2 * (G >> 4) * CV_RS + 64 * (G & 15); }
template <int G> constexpr int cv_off1() { return (2 * (G >> 4) + 1) * CV_RS + 64 * (15 - (G & 15)); }
template <int G, int I> DI void cv_one(f32x4 (&acc)[4][8], const frag (&A)[16], const frag& B0, const frag& B1) {
  constexpr int R = 2 * (G >> 4), N = G & 15;
  constexpr int c0 = N <= 1 ? 0 : N / 2;
  constexpr int n0 = 8 - c0;
  if constexpr (I < n0) {
    constexpr int cb = c0 + I, s = 1 - N + 2 * cb;
    if constexpr (N == 0) mfma_vv0(acc[R][cb], A[s], B0); else mfma_vv(acc[R][cb], A[s], B0);
  } else {
    constexpr int c1 = (15 - N) / 2, cb = c1 + (I - n0), s = N - 14 + 2 * cb;
    if constexpr (s == 0) mfma_vv0(acc[R + 1][cb], A[s], B1); else mfma_vv(acc[R + 1][cb], A[s], B1);
  }
}
struct W1C { u32 vo[7]; u32 m0b, sob; __amdgpu_buffer_rsrc_t srd; };
template <int N, int R> DI void w1_m0(const W1C& c) { asm volatile("s_add_u32 m0, %0, %1" ::"s"(c.m0b), "i"(N * 4 * 7168 + R * 1024) : "scc", "m0"); }
template <int N, int R> DI void w1_ld(const W1C& c) {
  asm volatile("buffer_load_dwordx4 %0, %1, %2 offen lds" ::"v"(c.vo[R]), "s"(c.srd), "s"(c.sob + N * 4 * (32 * F * 2)));
}
template <int N, int R> DI void w1_piece(const W1C& c) { w1_m0<N, R>(c); asm volatile("s_nop 0"); w1_ld<N, R>(c); }
template <int N> DI void w1_step(const W1C& c) {
  w1_piece<N, 0>(c); w1_piece<N, 1>(c); w1_piece<N, 2>(c); w1_piece<N, 3>(c); w1_piece<N, 4>(c); w1_piece<N, 5>(c); w1_piece<N, 6>(c);
}
template <int G> DI void cv_steps(f32x4 (&acc)[4][8], const frag (&A)[16], u32 vb, frag P0, frag P1, frag Q0, frag Q1, const W1C& wc) {
  frag N0, N1;
  cv_one<G, 0>(acc, A, P0, P1); SB0;
  if constexpr (G + 2 < 32) N0 = cv_ldB<cv_off0<G + 2>()>(vb);
  SB0; cv_one<G, 1>(acc, A, P0, P1); cv_one<G, 2>(acc, A, P0, P1); SB0;
  if constexpr (G + 2 < 32) N1 = cv_ldB<cv_off1<G + 2>()>(vb);
  SB0; cv_one<G, 3>(acc, A, P0, P1); cv_one<G, 4>(acc, A, P0, P1);
  if constexpr (G < 28) w1_m0<G / 7, G % 7>(wc);
  cv_one<G, 5>(acc, A, P0, P1);
  if constexpr (G < 28) w1_ld<G / 7, G % 7>(wc);
  cv_one<G, 6>(acc, A, P0, P1); cv_one<G, 7>(acc, A, P0, P1); cv_one<G, 8>(acc, A, P0, P1);
  SB0;
  if constexpr (G + 1 < 32) cv_steps<G + 1>(acc, A, vb, Q0, Q1, N0, N1, wc);
}
constexpr int L_W1 = 0;
constexpr int L_Y = 143360;
constexpr int L_MOD = L_Y + 10240;
constexpr int L_RED = L_MOD + 8192;
constexpr int L_TOTAL = 163840;
constexpr int L_CONV = L_TOTAL - 4 * CV_WB;

typedef unsigned long long u64;
DI u64 call_tag(u32 epoch) { const u32 t = epoch + 1u; return (u64)t | ((u64)(~t) << 32); }
DI void signal(char* ws, size_t off, u64 tag) { __hip_atomic_store((GAS u64*)(ws + off), tag, __ATOMIC_RELAXED, __HIP_MEMORY_SCOPE_AGENT); }
DI void poll_idx(const char* base, int idx, u64 tag) {
  const GAS u64* p = (const GAS u64*)(base + idx * 64);
#pragma unroll 1
  for (u32 spins = 0; spins < (1u << 18); spins++) {
    const u64 v = __hip_atomic_load(p, __ATOMIC_RELAXED, __HIP_MEMORY_SCOPE_AGENT);
    if (__all(v == tag)) break;
    __builtin_amdgcn_s_sleep(1);
  }
}
DI void poll_tags128(const char* base, u64 tag, int lane) {
  const __amdgpu_buffer_rsrc_t srd = mksrd(base, 1024);
  const u32 lo = (u32)tag, hi = (u32)(tag >> 32);
#pragma unroll 1
  for (u32 spins = 0; spins < (1u << 18); spins++) {
    asm volatile("" ::: "memory");
    const u32x4 a = __builtin_amdgcn_raw_buffer_load_b128(srd, lane * 16, 0, 16);
    if (__all(a[0] == lo && a[1] == hi && a[2] == lo && a[3] == hi)) break;
    __builtin_amdgcn_s_sleep(1);
  }
}
DI void poll_tags256(const char* base, u64 tag, int lane) {
  const __amdgpu_buffer_rsrc_t srd = mksrd(base, 2048);
  const u32 lo = (u32)tag, hi = (u32)(tag >> 32);
#pragma unroll 1
  for (u32 spins = 0; spins < (1u << 18); spins++) {
    asm volatile("" ::: "memory");
    const u32x4 a = __builtin_amdgcn_raw_buffer_load_b128(srd, lane * 32, 0, 16), b = __builtin_amdgcn_raw_buffer_load_b128(srd, lane * 32 + 16, 0, 16);
    const bool ok = a[0] == lo && a[1] == hi && a[2] == lo && a[3] == hi && b[0] == lo && b[1] == hi && b[2] == lo && b[3] == hi;
    if (__all(ok)) break;
    __builtin_amdgcn_s_sleep(1);
  }
}
DI void lds_barrier() { asm volatile("s_waitcnt lgkmcnt(0)" ::: "memory"); __builtin_amdgcn_s_barrier(); asm volatile("" ::: "memory"); }

DI float xsum8_16_32(float x) {
  x += __int_as_float(__builtin_amdgcn_update_dpp(0, __float_as_int(x), 0x128, 0xf, 0xf, false));
  { auto r = __builtin_amdgcn_permlane16_swap(__float_as_uint(x), __float_as_uint(x), false, false); x = __uint_as_float(r[0]) + __uint_as_float(r[1]); }
  { auto r = __builtin_amdgcn_permlane32_swap(__float_as_uint(x), __float_as_uint(x), false, false); x = __uint_as_float(r[0]) + __uint_as_float(r[1]); }
  return x;
}
DI void ln_stats(const f32x2 (&x)[10], f32x2* red, int w, int lane, float& mean, float& rinv) {
  f32x2 a = ((x[0] + x[1]) + (x[2] + x[3])) + ((x[4] + x[5]) + (x[6] + x[7])) + (x[8] + x[9]);
  f32x2 b0 = x[0] * x[0], b1 = x[1] * x[1];
#pragma unroll
  for (int h = 2; h < 10; h += 2) { b0 += x[h] * x[h]; b1 += x[h + 1] * x[h + 1]; }
  b0 += b1;
  const float s1 = xsum8_16_32(a[0] + a[1]), s2 = xsum8_16_32(b0[0] + b0[1]);
  if (lane < 8) red[w * 8 + lane] = (f32x2){s1, s2};
  lds_barrier();
  const int tk = lane & 7;
  const f32x2 t = (red[tk] + red[8 + tk]) + (red[16 + tk] + red[24 + tk]);
  ln_finish(t[0], t[1], mean, rinv);
}
DI void ln1(const u32x2 (&xr)[5], u32 (&park)[10], f32x2* red, int w, int lane) {
  f32x2 x[10];
#pragma unroll
  for (int i = 0; i < 5; i++) { x[2 * i] = up(xr[i][0]); x[2 * i + 1] = up(xr[i][1]); }
  float mean, rinv;
  ln_stats(x, red, w, lane, mean, rinv);
  const f32x2 mm = {mean, mean}, rr = {rinv, rinv};
#pragma unroll
  for (int h = 0; h < 10; h++) x[h] = rb2(x[h] - mm);
#pragma unroll
  for (int h = 0; h < 10; h++) { const u32 n = pk(x[h] * rr); asm volatile("v_accvgpr_write_b32 %0, %1" : "=a"(park[h]) : "v"(n)); }
}
template <int NT> DI void kv_item(const u32x2* kr, const u32x2* vr, const u32x4& s0, const u32x4& s1, char* dst, int stride) {
  const f32x2 sk[2][4] = {{{lo16(s0[0]), lo16(s0[2])}, {hi16(s0[0]), hi16(s0[2])}, {lo16(s0[1]), lo16(s0[3])}, {hi16(s0[1]), hi16(s0[3])}},
                          {{lo16(s1[0]), lo16(s1[2])}, {hi16(s1[0]), hi16(s1[2])}, {lo16(s1[1]), lo16(s1[3])}, {hi16(s1[1]), hi16(s1[3])}}};
  f32x2 kf[NT + 3][2], a[NT][2];
#pragma unroll
  for (int i = 0; i < NT + 3; i++) { kf[i][0] = up(kr[i][0]); kf[i][1] = up(kr[i][1]); }
#define KVS(EXPR) _Pragma("unroll") for (int r = 0; r < NT; r++) { _Pragma("unroll") for (int h = 0; h < 2; h++) { EXPR; } }
  KVS(a[r][h] = rb2(kf[r][h] * sk[h][3]))
  f32x2 pqt[NT][2];
  KVS(pqt[r][h] = rb2(kf[r + 1][h] * sk[h][2]))
  KVS(a[r][h] = rb2(a[r][h] + pqt[r][h]))
  KVS(pqt[r][h] = rb2(kf[r + 2][h] * sk[h][1]))
  KVS(a[r][h] = rb2(a[r][h] + pqt[r][h]))
  KVS(pqt[r][h] = rb2(kf[r + 3][h] * sk[h][0]))
  KVS(a[r][h] = rb2(a[r][h] + pqt[r][h]))
#undef KVS
#pragma unroll
  for (int r = 0; r < NT; r++) *(u32x2*)(dst + r * stride) = (u32x2){pk(a[r][0] * up(vr[r][0])), pk(a[r][1] * up(vr[r][1]))};
}
struct KVR { u32x2 kA[7], vA[4], kB[4], vB; u32x4 sA0, sA1, sB0, sB1; u32 dA, dB; };
constexpr int P_RED = 53248;
constexpr int P_US = 12288, P_K = 24576, P_V = P_K + 12288, P_S = P_V + 10240;
DI void prep_request(const u16* __restrict__ k, const u16* __restrict__ v, const u16* __restrict__ shk, u32 lds0, int t16, int hf, int w, int lane) {
  const __amdgpu_buffer_rsrc_t sK = mksrd(k, S * D * 2), sV = mksrd(v, S * D * 2), sS = mksrd(shk + 320 * hf * 4, 2560);
  const u32 base = 16 * t16 * (D * 2) + 640 * hf;
#pragma unroll
  for (int n = 0; n < 3; n++) {
    const u32 c = 64 * (w + 4 * n) + lane, row = (c * 1639u) >> 16, o = row * 640 + c * 16 + base;
    dma16(lds0 + P_K + (w + 4 * n) * 1024, c < 760 ? o - 3 * (D * 2) : 0x80000000u, sK, 0);
    if (n < 2 || w < 2) dma16(lds0 + P_V + (w + 4 * n) * 1024, o, sV, 0);
  }
  if (w < 3) dma16(lds0 + P_S + w * 1024, w * 1024 + lane * 16, sS, 0);
  waitv<0>();
}
DI void prep_read(KVR& r, const char* smem, int tid) {
  const u32 mgA = ((u32)tid * 820u) >> 16, cqA = tid - 80 * mgA;
  const u32 cqB = 16 + (tid >> 2), tB = 12 + (tid & 3);
  const char* pA = smem + 4 * mgA * 640 + 8 * cqA;
  const char* pB = smem + tB * 640 + 8 * cqB;
#pragma unroll
  for (int i = 0; i < 7; i++) r.kA[i] = *(const u32x2*)(pA + P_K + i * 640);
#pragma unroll
  for (int i = 0; i < 4; i++) r.vA[i] = *(const u32x2*)(pA + P_V + i * 640);
#pragma unroll
  for (int i = 0; i < 4; i++) r.kB[i] = *(const u32x2*)(pB + P_K + i * 640);
  r.vB = *(const u32x2*)(pB + P_V);
  r.sA0 = *(const u32x4*)(smem + P_S + 32 * cqA); r.sA1 = *(const u32x4*)(smem + P_S + 32 * cqA + 16);
  r.sB0 = *(const u32x4*)(smem + P_S + 32 * cqB); r.sB1 = *(const u32x4*)(smem + P_S + 32 * cqB + 16);
  r.dA = 4 * mgA * 656 + 8 * cqA; r.dB = tB * 656 + 8 * cqB;
}
DI void prep_finish(u32x2 (&xr)[5], char* __restrict__ ws, char* smem, int t16, int hf, int tid, int lane, int w, u64 tag) {
  KVR r;
  prep_read(r, smem, tid);
  kv_item<4>(r.kA, r.vA, r.sA0, r.sA1, smem + r.dA, 656);
  kv_item<1>(r.kB, &r.vB, r.sB0, r.sB1, smem + r.dB, 656);
  lds_barrier();
  char* us = smem + P_US;
  {
    const int rho = lane >> 4, i16 = lane & 15, jj = i16 >> 2, p = i16 & 3;
    const u32 src = ldsaddr(smem) + (4 * jj + rho) * 656 + 8 * p + 32 * w;
    char* dst = us + (4 * w + (i16 >> 2)) * 144 + (i16 & 3) * 32 + rho * 8;
    s16x4 v[5];
#pragma unroll
    for (int n = 0; n < 5; n++) v[n] = trread(src + 128 * n);
#pragma unroll
    for (int n = 0; n < 5; n++) *(s16x4*)(dst + n * 16 * 144) = v[n];
  }
  lds_barrier();
  {
    const __amdgpu_buffer_rsrc_t sU = mksrd(ws + WS_U, 2621440);
    const u32 uo = (t16 * 160 + 80 * hf + (lane >> 3)) * 128 + (lane & 7) * 16;
#pragma unroll
    for (int n = 0; n < 3; n++) {
      const int c8 = w + 4 * n;
      if (n < 2 || w < 2) __builtin_amdgcn_raw_buffer_store_b128(*(const u32x4*)(us + (8 * c8 + (lane >> 3)) * 144 + (lane & 7) * 16), sU, uo + c8 * 1024, 0, 16);
    }
  }
  asm volatile("s_waitcnt vmcnt(0)" : "+v"(xr[0]), "+v"(xr[1]), "+v"(xr[2]), "+v"(xr[3]), "+v"(xr[4]) :: "memory");
  __builtin_amdgcn_s_barrier();
  if (tid < 4) signal(ws, WS_FU + (hf * 4 + tid) * 1024 + t16 * 8, tag);
}
DI void q_request(u32x2 (&xr)[5], const u16* __restrict__ qin, int t0, int w, int lane) {
  const __amdgpu_buffer_rsrc_t srdQ = mksrd(qin + (size_t)t0 * D, 10240);
  const u32 vq = (lane & 7) * 1280 + (40 * w + (lane >> 3)) * 8;
  asm volatile("s_nop 4");
  bload2<0>(xr[0], vq, srdQ, 0); bload2<64>(xr[1], vq, srdQ, 0); bload2<128>(xr[2], vq, srdQ, 0); bload2<192>(xr[3], vq, srdQ, 0); bload2<256>(xr[4], vq, srdQ, 0);
}

DI void phase_conv(const u32x4 (&rk)[4], u32 Draw, const u32x2 (&xr)[5], u32 (&park)[10], char* __restrict__ ws, char* smem, int xcd, int j, int w, int lane, u64 tag, const W1C& wc) {
  const int ch = 80 * xcd + 4 * j + w;
  char* kim = smem + L_CONV + w * CV_WB;
  char* uim = kim + CV_KB;
  if (lane < 32) *(u32x4*)(uim + (lane >> 3) * CV_RS + 16 * (lane & 7)) = (u32x4){0u, 0u, 0u, 0u};
  if (lane < 2) *(u32x4*)(kim + 16 * lane) = (u32x4){0u, 0u, 0u, 0u};
#pragma unroll
  for (int i = 0; i < 4; i++) *(u32x4*)(kim + 32 + 16 * (64 * i + lane)) = rk[i];
  frag A[16];
  {
    const int q = lane >> 4, cc = lane & 15;
    const u32 va = ldsaddr(kim) + 224 - 64 * q - 8 * (cc >> 2) + 8 * (cc & 3);
#pragma unroll
    for (int s = 0; s < 16; s++) A[s] = catf(trread(va + 256 * s + 32), trread(va + 256 * s));
  }
  if (w == 0) poll_tags128(ws + WS_FU + ((xcd >> 2) * 4 + (xcd & 3)) * 1024, tag, lane);
  __builtin_amdgcn_s_barrier();
  {
    const __amdgpu_buffer_rsrc_t sU = mksrd(ws + WS_U, 2621440);
    const u32 uo = ((32 * w + (lane >> 3)) * 160 + 20 * xcd + j) * 128 + (lane & 7) * 16;
    u32x4 ru[4];
#pragma unroll
    for (int i = 0; i < 4; i++) ru[i] = __builtin_amdgcn_raw_buffer_load_b128(sU, uo + i * (8 * 160 * 128), 0, 16);
    SB0;
    ln1(xr, park, (f32x2*)(smem + P_RED), w, lane);
    SB0;
    char* ud = smem + L_CONV + ((lane & 7) >> 1) * CV_WB + CV_KB + 2 * (lane & 1) * CV_RS + 128 + 8 * (32 * w + (lane >> 3));
#pragma unroll
    for (int i = 0; i < 4; i++) { *(u32x2*)(ud + 64 * i) = (u32x2){ru[i][0], ru[i][1]}; *(u32x2*)(ud + 64 * i + CV_RS) = (u32x2){ru[i][2], ru[i][3]}; }
  }
  lds_barrier();
  const int q = lane >> 4, cc = lane & 15;
  const u32 vb = ldsaddr(uim) + 8 + 8 * cc + 16 * q;
  f32x4 acc[4][8];
  frag P0, P1, Q0, Q1;
  P0 = cv_ldB<cv_off0<0>()>(vb); P1 = cv_ldB<cv_off1<0>()>(vb); Q0 = cv_ldB<cv_off0<1>()>(vb); Q1 = cv_ldB<cv_off1<1>()>(vb);
  cv_steps<0>(acc, A, vb, P0, P1, Q0, Q1, wc);
  asm volatile("s_nop 7\n\ts_nop 1");
#pragma unroll
  for (int r = 0; r < 4; r++)
#pragma unroll
    for (int cb = 0; cb < 8; cb += 4) asm volatile("" : "+v"(acc[r][cb]), "+v"(acc[r][cb + 1]), "+v"(acc[r][cb + 2]), "+v"(acc[r][cb + 3]));
  const int idx = 4 * (lane == 0 ? 63 : (lane >= 16 ? lane - 16 : lane + 47));
  const u32 ub = ldsaddr(uim) + 2 * (64 + 4 * cc + q);
  char* yl = kim + 32 * cc + 8 * q;
  const float Dch = (w & 1) ? hi16(Draw) : lo16(Draw);
  const f32x2 DD = {Dch, Dch};
  float o[8][4], cy[8][3];
  u32 u01[8], u23[8];
#pragma unroll
  for (int cb = 0; cb < 8; cb++) {
    const LAS u16* up_ = (const LAS u16*)(size_t)(ub + 128 * cb);
    u01[cb] = (u32)up_[0] | ((u32)up_[CV_RS / 2] << 16);
    u23[cb] = (u32)up_[CV_RS] | ((u32)up_[3 * CV_RS / 2] << 16);
  }
#pragma unroll
  for (int cb = 0; cb < 8; cb++) {
    const f32x4 z0 = acc[0][cb], z1 = acc[1][cb], z2 = acc[2][cb], z3 = acc[3][cb];
    o[cb][0] = z0[0]; o[cb][1] = z0[1] + z1[0]; o[cb][2] = z0[2] + z1[1] + z2[0]; o[cb][3] = (z0[3] + z1[2]) + (z2[1] + z3[0]);
    cy[cb][0] = z1[3] + z2[2] + z3[1]; cy[cb][1] = z2[3] + z3[2]; cy[cb][2] = z3[3];
  }
#pragma unroll
  for (int cb = 0; cb < 8; cb++)
#pragma unroll
    for (int e = 0; e < 3; e++) {
      const float t = lane == 63 ? (cb ? cy[cb - 1][e] : 0.f) : cy[cb][e];
      o[cb][e] += __int_as_float(__builtin_amdgcn_ds_bpermute(idx, __float_as_int(t)));
    }
#pragma unroll
  for (int cb = 0; cb < 8; cb++) {
    const f32x2 y01 = rb2((f32x2){o[cb][0], o[cb][1]}) + rb2(up(u01[cb]) * DD), y23 = rb2((f32x2){o[cb][2], o[cb][3]}) + rb2(up(u23[cb]) * DD);
    *(u32x2*)(yl + 512 * cb) = (u32x2){pk(y01), pk(y23)};
  }
  lds_barrier();
  {
    const __amdgpu_buffer_rsrc_t sY = mksrd(ws + WS_Y2B, 2621440);
    const int c4 = (lane >> 1) & 3, tgl = 32 * w + (lane >> 3), hf = lane & 1;
    const char* src = smem + L_CONV + c4 * CV_WB + tgl * 32 + 16 * hf;
    const u32 yo = (tgl * D + 80 * xcd + 4 * j + c4) * 32 + 16 * hf;
    u32x4 v[4];
#pragma unroll
    for (int i = 0; i < 4; i++) v[i] = *(const u32x4*)(src + 256 * i);
#pragma unroll
    for (int i = 0; i < 4; i++) __builtin_amdgcn_raw_buffer_store_b128(v[i], sY, yo + i * (8 * D * 32), 0, 16);
  }
}

DI void gemv_request(u32x4 (&r)[20], const u16* __restrict__ wmod, int r0, int tid) {
  const __amdgpu_buffer_rsrc_t sW = mksrd(wmod, D * 6 * D * 2);
  const u32 o = ((tid >> 3) * (6 * D) + 64 * r0 + 8 * (tid & 7)) * 2;
#pragma unroll
  for (int i = 0; i < 20; i++) r[i] = __builtin_amdgcn_raw_buffer_load_b128(sW, o, i * (32 * 6 * D * 2), 0);
}
DI void gemv_finish(const u32x4 (&r)[20], const u16* __restrict__ c, char* __restrict__ ws, char* smem, int r0, int w, int tid, int lane) {
    const __amdgpu_buffer_rsrc_t sWS = mksrd(ws, 65536);
    float* sc = (float*)(smem + L_Y);
    float* red = sc + 640;
    const int col0 = 64 * r0, rg = tid >> 3;
    for (int i = tid; i < D; i += 256) { float x = lo16(c[i]); sc[i] = rbf(x / (1.f + __expf(-x))); }
    __syncthreads();
    f32x2 a[4] = {{0.f, 0.f}, {0.f, 0.f}, {0.f, 0.f}, {0.f, 0.f}};
#pragma unroll
    for (int i = 0; i < 20; i++) {
      const float s = sc[rg + 32 * i];
#pragma unroll
      for (int e = 0; e < 4; e++) a[e] += up(r[i][e]) * (f32x2){s, s};
    }
    float af[8] = {a[0][0], a[0][1], a[1][0], a[1][1], a[2][0], a[2][1], a[3][0], a[3][1]};
#pragma unroll
    for (int e = 0; e < 8; e++) af[e] += __int_as_float(__builtin_amdgcn_update_dpp(0, __float_as_int(af[e]), 0x128, 0xf, 0xf, false));
#pragma unroll
    for (int e = 0; e < 8; e++) { auto rr = __builtin_amdgcn_permlane16_swap(__float_as_uint(af[e]), __float_as_uint(af[e]), false, false); af[e] = __uint_as_float(rr[0]) + __uint_as_float(rr[1]); }
#pragma unroll
    for (int e = 0; e < 8; e++) { auto rr = __builtin_amdgcn_permlane32_swap(__float_as_uint(af[e]), __float_as_uint(af[e]), false, false); af[e] = __uint_as_float(rr[0]) + __uint_as_float(rr[1]); }
    if (lane < 8) { *(f32x4*)(red + w * 64 + 8 * lane) = (f32x4){af[0], af[1], af[2], af[3]}; *(f32x4*)(red + w * 64 + 8 * lane + 4) = (f32x4){af[4], af[5], af[6], af[7]}; }
    __syncthreads();
    if (tid < 32) {
      const int c0 = 2 * tid;
      float s0 = rbf((red[c0] + red[64 + c0]) + (red[128 + c0] + red[192 + c0]));
      float s1 = rbf((red[c0 + 1] + red[65 + c0]) + (red[129 + c0] + red[193 + c0]));
      const int jc = col0 + c0, vv = jc / D, ch = jc - vv * D;
      if (vv == 1 || vv == 4) { s0 += 1.f; s1 += 1.f; }
      const int slot = vv == 0 ? 1 : vv == 1 ? 0 : vv == 2 ? 2 : vv == 3 ? 4 : vv == 4 ? 3 : 5;
      __builtin_amdgcn_raw_buffer_store_b32(pk2(s0, s1), sWS, WS_MODP + ((ch >> 2) * 24 + slot * 4 + (ch & 3)) * 2, 0, 16);
    }
}
DI void retile_request(u32x4 (&r)[4][4], const u16* __restrict__ w2, int r0, int w, int lane) {
  const __amdgpu_buffer_rsrc_t sW = mksrd(w2, F * D * 2);
  const int wv = (r0 - 60) * 4 + w;
#pragma unroll
  for (int n = 0; n < 4; n++) {
    const int u = wv + 144 * n, uc = u < 540 ? u : 539, ks = uc / 10, cc = uc - 10 * ks;
#pragma unroll
    for (int i = 0; i < 4; i++) r[n][i] = __builtin_amdgcn_raw_buffer_load_b128(sW, ((8 * i + (lane >> 3)) * D + 8 * (lane & 7)) * 2, (32 * ks * D + 64 * cc) * 2, 0);
  }
}
DI void retile_finish(const u32x4 (&r)[4][4], char* __restrict__ ws, char* smem, int r0, int w, int lane) {
    const int wv = (r0 - 60) * 4 + w;
    char* mb = smem + L_Y + w * (32 * 144);
    const int q = lane >> 4, i16 = lane & 15, jj = i16 >> 2, p = i16 & 3;
    const __amdgpu_buffer_rsrc_t sO = mksrd(ws + WS_W2F, 2211840);
    const u32 a0 = ldsaddr(mb) + (4 * q + jj) * 144 + 8 * p;
#pragma unroll
    for (int n = 0; n < 4; n++) {
      const int u = wv + 144 * n, uc = u < 540 ? u : 539, ks = uc / 10, cc = uc - 10 * ks;
#pragma unroll
      for (int i = 0; i < 4; i++) *(u32x4*)(mb + (8 * i + (lane >> 3)) * 144 + 16 * (lane & 7)) = r[n][i];
      frag v[4];
#pragma unroll
      for (int f = 0; f < 4; f++) v[f] = catf(trread(a0 + 32 * f), trread(a0 + 32 * f + 16 * 144));
#pragma unroll
      for (int f = 0; f < 4; f++) {
        const int ft = 4 * cc + f;
        if (u < 540) __builtin_amdgcn_raw_buffer_store_b128(v[f], sO, (((ft / 5) * 54 + ks) * 5 + (ft % 5)) * 1024 + lane * 16, 0, 16);
      }
    }
}

DI f32x2 gelu2(f32x2 x) {
  const float c0 = -2.f * 0.7978845608028654f * 1.4426950408889634f, c1 = c0 * 0.044715f;
  const f32x2 z = x * (x * x * (f32x2){c1, c1} + (f32x2){c0, c0});
  const f32x2 d = (f32x2){__builtin_amdgcn_exp2f(z[0]), __builtin_amdgcn_exp2f(z[1])} + (f32x2){1.f, 1.f};
  return x * (f32x2){__builtin_amdgcn_rcpf(d[0]), __builtin_amdgcn_rcpf(d[1])};
}
struct W2C { u32 l16; __amdgpu_buffer_rsrc_t srd; };
template <int I> DI void w2_load1(frag (&W2)[60], const W2C& c) {
  const u32 vo = c.l16 + (I / 5) * 5120;
  if constexpr (I % 5 < 4) bload_a_sc1<(I % 5) * 1024>(W2[I], vo, c.srd, 0); else bload_a_sc1<0>(W2[I], vo + 4096, c.srd, 0);
}
template <int I, int N> DI void w2_loads(frag (&W2)[60], const W2C& c) {
  w2_load1<I>(W2, c);
  if constexpr (N > 1) w2_loads<I + 1, N - 1>(W2, c);
}
constexpr int W2_M0 = 8, W2_M1 = 16, W2_G1 = 20, W2_GE = 6;
template <int LO, int N, int LIM> DI void w2_part(frag (&W2)[60], const W2C& c) {
  if constexpr (LO < LIM) w2_loads<LO, (LO + N <= LIM ? N : LIM - LO)>(W2, c);
}

constexpr int G1R = 6;
struct AF { u32x2 lo, hi; };
struct G1S {
  frag Y[G1R][2];
  AF A0[7], A1[7];
  f32x4 acc[7][2];
};
template <int FI> DI void g1_lda(AF& a, u32 ab) {
  asm volatile("ds_read_b64_tr_b16 %0, %2 offset:%c3\n\tds_read_b64_tr_b16 %1, %2 offset:%c4" : "=&v"(a.lo), "=&v"(a.hi) : "v"(ab), "i"(32 * FI), "i"(32 * FI + 1792));
}
DI frag af(const AF& a) { return (frag){a.lo[0], a.lo[1], a.hi[0], a.hi[1]}; }
template <int N> DI void g1_waitA4(AF& a, AF& b, AF& c, AF& d) {
  asm volatile("s_waitcnt lgkmcnt(%c8)" : "+v"(a.lo), "+v"(a.hi), "+v"(b.lo), "+v"(b.hi), "+v"(c.lo), "+v"(c.hi), "+v"(d.lo), "+v"(d.hi) : "i"(N));
}
template <int N> DI void g1_waitA3(AF& a, AF& b, AF& c) {
  asm volatile("s_waitcnt lgkmcnt(%c6)" : "+v"(a.lo), "+v"(a.hi), "+v"(b.lo), "+v"(b.hi), "+v"(c.lo), "+v"(c.hi) : "i"(N));
}
struct G1C { u32 b0, l16, ybase; __amdgpu_buffer_rsrc_t srdY; };
template <int KS, int FI>
DI void g1_f(G1S& s, AF (&Ac)[7], AF (&An)[7], frag (&W2)[60], const G1C& c, const W2C& c2, u32 ab, u32 yo) {
  if constexpr (FI == 0) g1_waitA4<6>(Ac[0], Ac[1], Ac[2], Ac[3]);
  if constexpr (FI == 4) g1_waitA3<(KS < 19) ? 8 : 0>(Ac[4], Ac[5], Ac[6]);
  const frag a = af(Ac[FI]);
  mfma_vv(s.acc[FI][0], a, s.Y[KS % G1R][0]);
  if constexpr (KS < 19) g1_lda<FI>(An[FI], ab);
  mfma_vv(s.acc[FI][1], a, s.Y[KS % G1R][1]);
  if constexpr (KS >= 1 && KS - 1 + G1R < 20 && (FI == 1 || FI == 4)) bload_sc1<(FI == 4) * 1024>(s.Y[(KS - 1) % G1R][FI == 4], c.l16, c.srdY, yo);
  if constexpr (KS < W2_G1 && FI == 6) w2_load1<W2_M0 + W2_M1 + KS>(W2, c2);
  if constexpr (FI < 6) g1_f<KS, FI + 1>(s, Ac, An, W2, c, c2, ab, yo);
}
template <int KS> constexpr int g1_younger() {
  int cnt = 0; bool seen = false;
  for (int k = 0; k < G1R; k++) { if (seen) cnt += 2; if (k == KS) seen = true; }
  for (int n = 0; n < KS; n++) {
    if (n >= 1 && n - 1 + G1R < 20) { if (seen) cnt += 2; if (n - 1 + G1R == KS) seen = true; }
    if (n < W2_G1 && seen) cnt += 1;
  }
  return cnt;
}
template <int KS>
DI void g1_step(G1S& s, frag (&W2)[60], const G1C& c, const W2C& c2) {
  waitv2<g1_younger<KS>()>(s.Y[KS % G1R][0], s.Y[KS % G1R][1]);
  const u32 ab = c.b0 + (KS + 1) * 7168;
  const u32 yo = c.ybase + (KS - 1 + G1R) * 2048;
  if constexpr (KS & 1) g1_f<KS, 0>(s, s.A1, s.A0, W2, c, c2, ab, yo);
  else                  g1_f<KS, 0>(s, s.A0, s.A1, W2, c, c2, ab, yo);
  if constexpr (KS < 19) g1_step<KS + 1>(s, W2, c, c2);
}
struct G2S {
  frag H[4][4];
  frag WX[10];
  f32x4 acc[5][4];
  u32x2 xx[5], gg[5], b2v[5];
};
struct G2C { u32 l16, vx, vb2, vg; __amdgpu_buffer_rsrc_t srdH, srdX, srdB2, srdMD; };
template <int N, int T> DI void g2_hload1(G2S& s, const G2C& c, u32 vo) { bload_sc1<T * 1024>(s.H[N & 3][T], vo, c.srdH, 0); }
template <int N> DI void g2_hload(G2S& s, const G2C& c) {
  const u32 vo = c.l16 + N * 4096;
  g2_hload1<N, 0>(s, c, vo); g2_hload1<N, 1>(s, c, vo); g2_hload1<N, 2>(s, c, vo); g2_hload1<N, 3>(s, c, vo);
}
template <int I> DI void g2_eload(G2S& s, const G2C& c) {
  if constexpr (I < 5) bload2_sc1<32 * I>(s.xx[I], c.vx, c.srdX, 0);
  else if constexpr (I < 10) bload2<32 * (I - 5)>(s.b2v[I - 5], c.vb2, c.srdB2, 0);
  else bload2_sc1<192 * (I - 10)>(s.gg[I - 10], c.vg, c.srdMD, 0);
}
template <int N, int M>
DI void g2_m(G2S& s, frag (&W2)[60], const G2C& c, u32 vo) {
  constexpr int d = M >> 2, t = M & 3;
  if constexpr (N == 0) mfma_av0(s.acc[d][t], W2[d], s.H[0][t]);
  else if constexpr (N < 12) mfma_av(s.acc[d][t], W2[N * 5 + d], s.H[N & 3][t]);
  else mfma_vv(s.acc[d][t], s.WX[(N - 12) * 5 + d], s.H[N & 3][t]);
  if constexpr (N + 3 < 14 && (M == 1 || M == 6 || M == 11 || M == 16)) g2_hload1<N + 3, (M - 1) / 5>(s, c, vo);
  if constexpr (N >= 11 && (M == 1 || M == 5 || M == 9 || M == 13 || M == 17)) g2_eload<5 * (N - 11) + (M - 1) / 4>(s, c);
  if constexpr (M < 19) g2_m<N, M + 1>(s, W2, c, vo);
}
template <int N> constexpr int g2_younger() {
  int cnt = 0; bool seen = false;
  for (int h = 0; h < 3; h++) { if (seen) cnt += 4; if (h == N) seen = true; }
  for (int n = 0; n < N; n++) {
    if (n + 3 < 14) { if (seen) cnt += 4; if (n + 3 == N) seen = true; }
    if (n >= 11 && seen) cnt += 5;
  }
  return cnt;
}
template <int N>
DI void g2_step(G2S& s, frag (&W2)[60], const G2C& c) {
  waitv4<g2_younger<N>()>(s.H[N & 3][0], s.H[N & 3][1], s.H[N & 3][2], s.H[N & 3][3]);
  const u32 vo = c.l16 + (N + 3) * 4096;
  g2_m<N, 0>(s, W2, c, vo);
  if constexpr (N < 13) g2_step<N + 1>(s, W2, c);
}

__global__ __launch_bounds__(256, 1) void k_main(const u16* __restrict__ qin, const u16* __restrict__ kin, const u16* __restrict__ vin,
                                                 const u16* __restrict__ shk, const u16* __restrict__ ssk, const u16* __restrict__ ssD,
                                                 const u16* __restrict__ cin, const u16* __restrict__ wmod, const u16* __restrict__ w1,
                                                 const u16* __restrict__ b1, const u16* __restrict__ w2, const u16* __restrict__ b2,
                                                 char* __restrict__ ws, u16* __restrict__ out) {
  extern __shared__ __attribute__((aligned(16))) char smem[];
  const int tid = threadIdx.x, lane = tid & 63, w = rfl(tid >> 6);
  const int b = blockIdx.x, xcd = b & 7, j = b >> 3;
  const int tile = 4 * (xcd >> 1) + (j >> 3), g = 8 * (xcd & 1) + (j & 7);
  const int TT = 4 * xcd + (j >> 3), cg = j & 7;
  const u32 l16 = lane * 16, lds0 = ldsaddr(smem);
  W1C wc;
#pragma unroll
  for (int r = 0; r < 7; r++) { const u32 idx = 64 * r + lane, row = (idx * 4682u) >> 16; wc.vo[r] = row * (F * 2 - 224) + idx * 16; }
  wc.m0b = lds0 + L_W1 + w * 7168; wc.sob = w * (32 * F * 2) + 224 * g; wc.srd = mksrd(w1, D * F * 2);
  const int tg = 16 * tile + g, t0 = 8 * tg;
  const int tk = lane & 7, slot = lane >> 3, q = lane >> 4;
  const u64 tag = call_tag(__hip_atomic_load((const GAS u32*)(ws + WS_EPOCH), __ATOMIC_RELAXED, __HIP_MEMORY_SCOPE_AGENT));
  if (w == 3) {
    unsigned long long pc; u32 len;
    asm volatile("s_getpc_b64 %0\n.Lkmain_here:\n\ts_mov_b32 %1, .Lkmain_end-.Lkmain_here" : "=s"(pc), "=s"(len));
    asm volatile("s_nop 4");
    dma4(lds0 + L_RED + 768, (j + 32 * lane) * 128, mksrd((const void*)pc, len), 0);
  }
  asm volatile("s_nop 4");
  prep_request(kin, vin, shk, lds0, b >> 1, b & 1, w, lane);
  __builtin_amdgcn_s_barrier();
  u32x2 xr[5];
  u32 park[10];
  const int r0 = (j - 20) * 8 + xcd;
  const int ch = 80 * xcd + 4 * j + w;
  u32x4 rk[4];
  u32 Draw;
  {
    const __amdgpu_buffer_rsrc_t sF = mksrd(ssk + (size_t)(j < 20 ? ch : 0) * S, j < 20 ? S * 2 : 0);
#pragma unroll
    for (int i = 0; i < 4; i++) rk[i] = __builtin_amdgcn_raw_buffer_load_b128(sF, l16 + 1024 * i, 0, 0);
  }
  Draw = __builtin_amdgcn_raw_buffer_load_b32(mksrd(ssD, D * 2), (ch >> 1) * 4, 0, 0);
  q_request(xr, qin, t0, w, lane);
  prep_finish(xr, ws, smem, b >> 1, b & 1, tid, lane, w, tag);
  asm volatile("" : "+v"(rk[0]), "+v"(rk[1]), "+v"(rk[2]), "+v"(rk[3]), "+v"(Draw));
  if (j < 20) {
    phase_conv(rk, Draw, xr, park, ws, smem, xcd, j, w, lane, tag, wc);
  } else {
    {
      const __amdgpu_buffer_rsrc_t sW = mksrd(w1, D * F * 2);
#pragma unroll
      for (int n = 0; n < 3; n++) {
        const u32 idx = (((j - 20) * 4 + w) * 3 + n) * 64 + lane, row = (idx * 74899u) >> 20;
        dma4(lds0 + L_RED + 768, idx < 8960 ? row * (F * 2 - 1792) + idx * 128 + 1792 * (xcd & 1) : 0x80000000u, sW, 0);
      }
    }
    if (r0 < 60) {
      u32x4 r[20];
      gemv_request(r, wmod, r0, tid);
      SB0;
      gemv_finish(r, cin, ws, smem, r0, w, tid, lane);
    } else {
      u32x4 r[4][4];
      retile_request(r, w2, r0, w, lane);
      SB0;
      retile_finish(r, ws, smem, r0, w, lane);
    }
    ln1(xr, park, (f32x2*)(smem + P_RED), w, lane);
  }
  waitv<0>();
  __builtin_amdgcn_s_barrier();
  if (tid < 8) signal(ws, WS_FG + tid * 2048 + b * 8, tag);
  const int ks0 = w < 2 ? 14 * w : 28 + 13 * (w - 2), nks = w < 2 ? 14 : 13;
  W2C c2;
  c2.l16 = l16; c2.srd = mksrd(ws + WS_W2F + (size_t)cg * 276480 + ks0 * 5120, nks * 5120);
  frag W2[60];
  f32x2 x[10], t[10];
  f32x2* red = (f32x2*)(smem + L_RED);
  if (j >= 20) { w1_step<0>(wc); w1_step<1>(wc); w1_step<2>(wc); w1_step<3>(wc); }
  if (w == 0) poll_tags256(ws + WS_FG + xcd * 2048, tag, lane);
  __builtin_amdgcn_s_barrier();
  if (b == 0 && tid == 0) __hip_atomic_store((GAS u32*)(ws + WS_EPOCH), (u32)tag, __ATOMIC_RELAXED, __HIP_MEMORY_SCOPE_AGENT);
  const __amdgpu_buffer_rsrc_t srdMD = mksrd(ws + WS_MODP, 7680);
  u32x2 bb[7];
  {
    const __amdgpu_buffer_rsrc_t srdYB = mksrd(ws + WS_Y2B + (size_t)(tg >> 1) * 20480, 20480);
    const __amdgpu_buffer_rsrc_t srdB1 = mksrd(b1, F * 2);
    u32x4 yr[3], mr[2];
    const u32 v16 = tid * 16, vb = (16 * 7 * g + 4 * q) * 2;
    asm volatile("s_nop 4");
    const u32 vy = tid * 32 + 16 * (tg & 1);
    bload_sc1<0>(yr[0], vy, srdYB, 0); bload_sc1<0>(yr[1], vy + 8192, srdYB, 0); bload_sc1<0>(yr[2], vy + 16384, srdYB, 0);
    bload_sc1<0>(mr[0], v16, srdMD, 0); bload_sc1<0>(mr[1], v16 + 4096, srdMD, 0);
    w1_step<4>(wc);
    SB0;
#pragma unroll
      for (int i = 0; i < 5; i++) { x[2 * i] = up(xr[i][0]); x[2 * i + 1] = up(xr[i][1]); }
#pragma unroll
      for (int h = 0; h < 10; h++) { u32 n; asm volatile("v_accvgpr_read_b32 %0, %1" : "=v"(n) : "a"(park[h])); t[h] = up(n); asm volatile("" : "+v"(x[h]), "+v"(t[h])); }
    SB0;
    asm volatile("s_waitcnt vmcnt(7)" : "+v"(yr[0]), "+v"(yr[1]), "+v"(yr[2]), "+v"(mr[0]), "+v"(mr[1]));
    *(u32x4*)(smem + L_Y + v16) = yr[0]; *(u32x4*)(smem + L_Y + 4096 + v16) = yr[1];
    if (tid < 128) *(u32x4*)(smem + L_Y + 8192 + v16) = yr[2];
    *(u32x4*)(smem + L_MOD + v16) = mr[0]; *(u32x4*)(smem + L_MOD + 4096 + v16) = mr[1];
    lds_barrier();
    const int i16 = lane & 15, jj = i16 >> 2, p = i16 & 3;
    const u32 ya = lds0 + L_Y + (4 * (40 * w + 2 * q + (p >> 1)) + jj) * 16 + 8 * (p & 1);
    const char* mdp = smem + L_MOD + (40 * w + slot) * 48;
    u32 yy[10], w1v[10], sav[10], gav[10];
#pragma unroll
    for (int i = 0; i < 5; i++) {
      const u32x2 yu = __builtin_bit_cast(u32x2, trread(ya + i * 8 * 64));
      const u32x4 m0 = *(const u32x4*)(mdp + i * 384);
      const u32x2 m1 = *(const u32x2*)(mdp + i * 384 + 16);
      yy[2 * i] = yu[0]; yy[2 * i + 1] = yu[1]; w1v[2 * i] = m0[0]; w1v[2 * i + 1] = m0[1]; sav[2 * i] = m0[2]; sav[2 * i + 1] = m0[3]; gav[2 * i] = m1[0]; gav[2 * i + 1] = m1[1];
    }
    u32 o[10];
#define STAGE(EXPR) _Pragma("unroll") for (int h = 0; h < 10; h++) { EXPR; }
    STAGE(t[h] = rb2(t[h] * up(w1v[h])))
    SB0; w2_part<0, 2, W2_M0>(W2, c2); SB0;
    STAGE(t[h] = rb2(t[h] + up(sav[h])))
    SB0; w2_part<2, 2, W2_M0>(W2, c2); SB0;
    STAGE(t[h] = rb2(t[h] * up(yy[h])))
    SB0; w2_part<4, 2, W2_M0>(W2, c2); SB0;
    STAGE(t[h] = rb2(up(gav[h]) * t[h]))
    SB0; w2_part<6, 2, W2_M0>(W2, c2); SB0;
    STAGE(o[h] = pk(x[h] + t[h]))
    const __amdgpu_buffer_rsrc_t srdX1 = mksrd(ws + WS_X1, 2621440);
    const u32 xo = (t0 + tk) * 1280 + (40 * w + slot) * 8;
#pragma unroll
    for (int i = 0; i < 5; i++) __builtin_amdgcn_raw_buffer_store_b64((u32x2){o[2 * i], o[2 * i + 1]}, srdX1, xo + i * 64, 0, 16);
    STAGE(x[h] = up(o[h]))
    float mean, rinv;
    ln_stats(x, red, w, lane, mean, rinv);
    {
      const f32x2 m2 = {mean, mean}, r2 = {rinv, rinv};
#pragma unroll
      for (int i = 0; i < 5; i++) {
        const u32x4 m0 = *(const u32x4*)(mdp + i * 384 + 16);
        const u32x2 m1 = *(const u32x2*)(mdp + i * 384 + 32);
        x[2 * i] = (x[2 * i] - m2) * r2 * up(m0[2]) + up(m1[0]);
        x[2 * i + 1] = (x[2 * i + 1] - m2) * r2 * up(m0[3]) + up(m1[1]);
      }
    }
    SB0;
    {
      bload2<0>(bb[0], vb, srdB1, 0); bload2<32>(bb[1], vb, srdB1, 0); bload2<64>(bb[2], vb, srdB1, 0); bload2<96>(bb[3], vb, srdB1, 0);
      bload2<128>(bb[4], vb, srdB1, 0); bload2<160>(bb[5], vb, srdB1, 0); bload2<192>(bb[6], vb, srdB1, 0);
    }
    SB0;
    ln_stats(x, red + 32, w, lane, mean, rinv);
    {
      const f32x2 m3 = {mean, mean}, r3 = {rinv, rinv};
      STAGE(o[h] = pk((x[h] - m3) * r3))
      const int rt = tg >> 1, r = 8 * (tg & 1) + tk;
      const __amdgpu_buffer_rsrc_t srdY3 = mksrd(ws + WS_Y3F, 2621440);
      const u32 yo = (rt >> 1) * 40960 + (rt & 1) * 1024 + ((2 * (slot >> 2) + (slot & 1)) * 16 + r) * 16 + 8 * ((slot >> 1) & 1) + 5 * w * 2048;
#pragma unroll
      for (int i = 0; i < 5; i++) __builtin_amdgcn_raw_buffer_store_b64((u32x2){o[2 * i], o[2 * i + 1]}, srdY3, yo + i * 2048, 0, 16);
    }
#undef STAGE
  }
  asm volatile("s_waitcnt vmcnt(0)" : "+v"(bb[0]), "+v"(bb[1]), "+v"(bb[2]), "+v"(bb[3]), "+v"(bb[4]), "+v"(bb[5]), "+v"(bb[6]) :: "memory");
  __builtin_amdgcn_s_barrier();
  if (tid == 0) signal(ws, WS_FA + (tile * 16 + g) * 64, tag);
  w2_loads<W2_M0, W2_M1>(W2, c2);
  poll_idx(ws + WS_FA + tile * 1024, 4 * w + (lane & 3), tag);
  f32x4 hacc[7][2];
  {
    G1C c1;
    const int i16 = lane & 15, jj = i16 >> 2, p = i16 & 3;
    c1.srdY = mksrd(ws + WS_Y3F, 2621440); c1.l16 = l16;
    c1.ybase = (tile * 4 + w) * 40960;
    c1.b0 = lds0 + L_W1 + (16 * (q >> 1) + 4 * (q & 1) + jj) * 224 + 8 * p;
    G1S s;
    asm volatile("s_nop 4");
#pragma unroll
    for (int k = 0; k < G1R; k++) { const u32 yo = c1.ybase + k * 2048; bload_sc1<0>(s.Y[k][0], l16, c1.srdY, yo); bload_sc1<1024>(s.Y[k][1], l16, c1.srdY, yo); }
#pragma unroll
    for (int f = 0; f < 7; f++) {
      const f32x4 bv = {lo16(bb[f][0]), hi16(bb[f][0]), lo16(bb[f][1]), hi16(bb[f][1])};
      s.acc[f][0] = bv; s.acc[f][1] = bv;
      asm volatile("" : "+v"(s.acc[f][0]), "+v"(s.acc[f][1]));
    }
    const u32 ab0 = c1.b0;
    asm volatile("s_waitcnt lgkmcnt(0)" ::: "memory");
    g1_lda<0>(s.A0[0], ab0); g1_lda<1>(s.A0[1], ab0); g1_lda<2>(s.A0[2], ab0); g1_lda<3>(s.A0[3], ab0);
    g1_lda<4>(s.A0[4], ab0); g1_lda<5>(s.A0[5], ab0); g1_lda<6>(s.A0[6], ab0);
    g1_step<0>(s, W2, c1, c2);
    asm volatile("s_nop 7\n\ts_nop 1");
#pragma unroll
    for (int f = 0; f < 7; f++) { asm volatile("" : "+v"(s.acc[f][0]), "+v"(s.acc[f][1])); hacc[f][0] = s.acc[f][0]; hacc[f][1] = s.acc[f][1]; }
  }
  G2S s2_;
  {
    const __amdgpu_buffer_rsrc_t srdH = mksrd(ws + WS_HF, 7077888);
    const int rt0 = 8 * tile + 2 * w;
    const u32 ho = ((rt0 >> 2) * 54 * 4 + (rt0 & 3)) * 1024 + l16 + ((7 * g) >> 1) * 4096;
    auto gel = [&](int f, int tt) -> u32x2 {
      const f32x4 a = hacc[f][tt];
      return (u32x2){pk(gelu2((f32x2){a[0], a[1]})), pk(gelu2((f32x2){a[2], a[3]}))};
    };
    SB0; w2_part<W2_M0 + W2_M1 + W2_G1, W2_GE, 60>(W2, c2); SB0;
    if (g & 1) {
      { const u32x2 a = gel(0, 0), c = gel(0, 1);
        __builtin_amdgcn_raw_buffer_store_b64(a, srdH, ho + 8, 0, 16); __builtin_amdgcn_raw_buffer_store_b64(c, srdH, ho + 1024 + 8, 0, 16); }
#pragma unroll
      for (int i = 0; i < 3; i++) {
        const u32x2 a0 = gel(2 * i + 1, 0), a1 = gel(2 * i + 2, 0), c0 = gel(2 * i + 1, 1), c1 = gel(2 * i + 2, 1);
        if (7 * g + 2 * i + 2 < 108) {
          __builtin_amdgcn_raw_buffer_store_b128((u32x4){a0[0], a0[1], a1[0], a1[1]}, srdH, ho + (1 + i) * 4096, 0, 16);
          __builtin_amdgcn_raw_buffer_store_b128((u32x4){c0[0], c0[1], c1[0], c1[1]}, srdH, ho + (1 + i) * 4096 + 1024, 0, 16);
        }
      }
    } else {
#pragma unroll
      for (int i = 0; i < 3; i++) {
        const u32x2 a0 = gel(2 * i, 0), a1 = gel(2 * i + 1, 0), c0 = gel(2 * i, 1), c1 = gel(2 * i + 1, 1);
        __builtin_amdgcn_raw_buffer_store_b128((u32x4){a0[0], a0[1], a1[0], a1[1]}, srdH, ho + i * 4096, 0, 16);
        __builtin_amdgcn_raw_buffer_store_b128((u32x4){c0[0], c0[1], c1[0], c1[1]}, srdH, ho + i * 4096 + 1024, 0, 16);
      }
      { const u32x2 a = gel(6, 0), c = gel(6, 1);
        __builtin_amdgcn_raw_buffer_store_b64(a, srdH, ho + 3 * 4096, 0, 16); __builtin_amdgcn_raw_buffer_store_b64(c, srdH, ho + 3 * 4096 + 1024, 0, 16); }
    }
  }
  waitv<0>();
  __builtin_amdgcn_s_barrier();
  if (tid == 0) signal(ws, WS_FB + (tile * 16 + g) * 64, tag);
  {
    const u32 vo = l16 + 12 * 5120, v1 = l16 + 13 * 5120;
    w2_part<W2_M0 + W2_M1 + W2_G1 + W2_GE, 60, 60>(W2, c2);
    bload_sc1<0>(s2_.WX[0], vo, c2.srd, 0); bload_sc1<1024>(s2_.WX[1], vo, c2.srd, 0); bload_sc1<2048>(s2_.WX[2], vo, c2.srd, 0); bload_sc1<3072>(s2_.WX[3], vo, c2.srd, 0);
    bload_sc1<0>(s2_.WX[4], vo + 4096, c2.srd, 0);
    bload_sc1<0>(s2_.WX[5], v1, c2.srd, 0); bload_sc1<1024>(s2_.WX[6], v1, c2.srd, 0); bload_sc1<2048>(s2_.WX[7], v1, c2.srd, 0); bload_sc1<3072>(s2_.WX[8], v1, c2.srd, 0);
    bload_sc1<0>(s2_.WX[9], v1 + 4096, c2.srd, 0);
  }
  const int cc = lane & 15, tok = 64 * TT + 16 * w + cc;
  G2C cg2;
  cg2.l16 = l16;
  cg2.srdH = mksrd(ws + WS_HF + (size_t)TT * 221184 + ks0 * 4096, nks * 4096);
  cg2.srdX = mksrd(ws + WS_X1, 2621440); cg2.srdB2 = mksrd(b2, D * 2); cg2.srdMD = srdMD;
  cg2.vx = tok * 1280 + (80 * cg + 4 * q) * 2; cg2.vb2 = (80 * cg + 4 * q) * 2; cg2.vg = (20 * cg + q) * 48 + 40;
  {
    const int gl = (2 * ks0) / 7, gh = (2 * (ks0 + nks) - 1) / 7, l7 = lane & 7;
    poll_idx(ws + WS_FB + (TT >> 1) * 1024, l7 < 6 ? (gl + l7 < gh ? gl + l7 : gh) : 8 * (TT & 1) + 2 * w + (l7 - 6), tag);
  }
  asm volatile("s_nop 4");
  g2_hload<0>(s2_, cg2); g2_hload<1>(s2_, cg2); g2_hload<2>(s2_, cg2);
  asm volatile("s_waitcnt vmcnt(12)" : "+v"(s2_.WX[0]), "+v"(s2_.WX[1]), "+v"(s2_.WX[2]), "+v"(s2_.WX[3]), "+v"(s2_.WX[4]), "+v"(s2_.WX[5]), "+v"(s2_.WX[6]), "+v"(s2_.WX[7]), "+v"(s2_.WX[8]), "+v"(s2_.WX[9]));
  g2_step<0>(s2_, W2, cg2);
  asm volatile("s_nop 7\n\ts_nop 1");
#pragma unroll
  for (int d = 0; d < 5; d++) asm volatile("" : "+v"(s2_.acc[d][0]), "+v"(s2_.acc[d][1]), "+v"(s2_.acc[d][2]), "+v"(s2_.acc[d][3]));
  f32x4* redq = (f32x4*)smem;
  f32x4 own[5];
#define G2_OWN(T) { _Pragma("unroll") for (int d = 0; d < 5; d++) { own[d] = s2_.acc[d][T]; \
    _Pragma("unroll") for (int t = 0; t < 4; t++) if (t != T) redq[((T * 4 + t) * 5 + d) * 64 + lane] = s2_.acc[d][t]; } }
  if (w == 0) G2_OWN(0) else if (w == 1) G2_OWN(1) else if (w == 2) G2_OWN(2) else G2_OWN(3)
#undef G2_OWN
  lds_barrier();
  asm volatile("s_waitcnt vmcnt(0)" : "+v"(s2_.xx[0]), "+v"(s2_.xx[1]), "+v"(s2_.xx[2]), "+v"(s2_.xx[3]), "+v"(s2_.xx[4]), "+v"(s2_.b2v[0]), "+v"(s2_.b2v[1]), "+v"(s2_.b2v[2]), "+v"(s2_.b2v[3]), "+v"(s2_.b2v[4]),
               "+v"(s2_.gg[0]), "+v"(s2_.gg[1]), "+v"(s2_.gg[2]), "+v"(s2_.gg[3]), "+v"(s2_.gg[4]));
  {
    const f32x4* r1 = redq + ((((w + 1) & 3) * 4 + w) * 5) * 64 + lane;
    const f32x4* r2 = redq + ((((w + 2) & 3) * 4 + w) * 5) * 64 + lane;
    const f32x4* r3 = redq + ((((w + 3) & 3) * 4 + w) * 5) * 64 + lane;
#pragma unroll
    for (int d = 0; d < 5; d++) {
      const f32x4 sacc = (own[d] + r1[d * 64]) + (r2[d * 64] + r3[d * 64]);
      u32x2 o;
#pragma unroll
      for (int h = 0; h < 2; h++) {
        o[h] = pk(up(s2_.xx[d][h]) + up(s2_.gg[d][h]) * ((f32x2){sacc[2 * h], sacc[2 * h + 1]} + up(s2_.b2v[d][h])));
      }
      *(u32x2*)(out + (size_t)tok * D + 80 * cg + 16 * d + 4 * q) = o;
    }
  }
  asm volatile(".Lkmain_end:");
}

extern "C" void kernel_launch(void* const* d_in, const int* in_sizes, int n_in, void* d_out, int out_size, void* d_ws, size_t ws_size,
                              hipStream_t stream) {
  const u16* q = (const u16*)d_in[0]; const u16* k = (const u16*)d_in[1]; const u16* v = (const u16*)d_in[2];
  const u16* shk = (const u16*)d_in[3]; const u16* ssk = (const u16*)d_in[4]; const u16* ssD = (const u16*)d_in[5];
  const u16* w1 = (const u16*)d_in[7]; const u16* b1 = (const u16*)d_in[8]; const u16* w2 = (const u16*)d_in[9];
  const u16* b2 = (const u16*)d_in[10]; const u16* c = (const u16*)d_in[11]; const u16* wmod = (const u16*)d_in[12];
  char* ws = (char*)d_ws;
  (void)hipFuncSetAttribute((const void*)k_main, hipFuncAttributeMaxDynamicSharedMemorySize, L_TOTAL);
  k_main<<<256, 256, L_TOTAL, stream>>>(q, k, v, shk, ssk, ssD, c, wmod, w1, b1, w2, b2, ws, (u16*)d_out);
}
```
